```python
import torch

def _softmax(x, axis=-1):
    return torch.softmax(x, dim=axis)
def _gelu(x):
    return torch.nn.functional.gelu(x, approximate="tanh")
def _silu(x):
    return torch.nn.functional.silu(x)
def _sigmoid(x):
    return torch.sigmoid(x)
def _layernorm(x, axis=-1, eps=1e-5):
    m = x.mean(dim=axis, keepdim=True)
    v = x.var(dim=axis, keepdim=True, unbiased=False)
    return (x - m) / torch.sqrt(v + eps)
def _scatter_add(tgt, idx, src, axis=0):
    return tgt.clone().index_add_(axis, idx, src)
def _segment_sum(src, seg, n):
    out = torch.zeros((n,) + src.shape[1:], dtype=src.dtype)
    return out.index_add_(0, seg, src)
def _pad(x, pw):
    flat = tuple(v for pair in reversed(pw) for v in pair)
    return torch.nn.functional.pad(x, flat)

kernel_name = 'gated_mix__spectral_fnet__moe_arch_mod__prompt'

def setup_inputs():

    _w = (lambda a: (lambda t: t.to(torch.bfloat16) if t.is_floating_point() else t)(torch.as_tensor(a)))
    def _A_setup():
        B, S, D = 1, 2048, 512
        ns = torch.arange(S)[:, None] * torch.arange(S)[None, :]
        nd = torch.arange(D)[:, None] * torch.arange(D)[None, :]
        Ws_re = torch.cos(-2.0 * torch.pi * ns / S).to(float)
        Ws_im = torch.sin(-2.0 * torch.pi * ns / S).to(float)
        Wd_re = torch.cos(-2.0 * torch.pi * nd / D).to(float)
        Wd_im = torch.sin(-2.0 * torch.pi * nd / D).to(float)
        return {
            "x": _w((lambda *s: torch.randn(*s, dtype=torch.bfloat16) * 0.05)(B, S, D)),
            "dft_seq_re": _w(Ws_re),
            "dft_seq_im": _w(Ws_im),
            "dft_hid_re": _w(Wd_re),
            "dft_hid_im": _w(Wd_im),
        }
    _A_in = _A_setup()

    def _B_setup():
        B, S, D, F, C = 1, 2048, 512, 1792, 1
        return {
            "x": _w((lambda *s: torch.randn(*s, dtype=torch.bfloat16) * 0.05)(B, S, D)),
            "w_r": _w((lambda *s: torch.randn(*s, dtype=torch.bfloat16) * 1.6)(D)),
            "w_gate": _w((lambda *s: torch.randn(*s, dtype=torch.bfloat16) * 1.6)(D, F)),
            "w_up": _w((lambda *s: torch.randn(*s, dtype=torch.bfloat16) * 1.6)(D, F)),
            "w_down": _w((lambda *s: torch.randn(*s, dtype=torch.bfloat16) * 1.6)(F, D)),
        }
    _B_in = _B_setup()

    return {
        "A_x": _A_in["x"],
        "A_dft_seq_re": _A_in["dft_seq_re"],
        "A_dft_seq_im": _A_in["dft_seq_im"],
        "A_dft_hid_re": _A_in["dft_hid_re"],
        "A_dft_hid_im": _A_in["dft_hid_im"],
        "B_x": _B_in["x"],
        "B_w_r": _B_in["w_r"],
        "B_w_gate": _B_in["w_gate"],
        "B_w_up": _B_in["w_up"],
        "B_w_down": _B_in["w_down"],
        "gate_w": _w((lambda *s: torch.randn(*s, dtype=torch.bfloat16) * 0.05)(512) * 16.0),
    }

def reference(A_x, A_dft_seq_re, A_dft_seq_im, A_dft_hid_re, A_dft_hid_im, B_x, B_w_r, B_w_gate, B_w_up, B_w_down, gate_w):
    def _A_ref(x, dft_seq_re, dft_seq_im, dft_hid_re, dft_hid_im):
        a = torch.einsum("bsd,ed->bse", x, dft_hid_re)
        b = torch.einsum("bsd,ed->bse", x, dft_hid_im)
        y = torch.einsum("ts,bse->bte", dft_seq_re, a) - torch.einsum("ts,bse->bte", dft_seq_im, b)
        return y

    def _B_ref(x, w_r, w_gate, w_up, w_down):
        B, S, D = x.shape
        C = 1
        r = x @ w_r
        idx = torch.argsort(-r, axis=-1)[:, :C]
        idx_e = (lambda x, n, axis: torch.repeat_interleave(x, n, dim=axis))(idx.reshape(B, C, 1), D, axis=2)
        x_sel = (lambda x, i, axis: torch.take_along_dim(x, i, dim=axis))(x, idx_e, axis=1)
        g = _silu(x_sel @ w_gate)
        u = x_sel @ w_up
        y = (g * u) @ w_down
        r_sel = _sigmoid((lambda x, i, axis: torch.take_along_dim(x, i, dim=axis))(r, idx, axis=1))
        y = y * r_sel.reshape(B, C, 1)
        b_off = (torch.arange(B) * S).reshape(B, 1)
        flat_idx = (idx + b_off).reshape(B * C)
        zero = (x * 0.0).reshape(B * S, D)
        upd = _scatter_add(zero, flat_idx, y.reshape(B * C, D), axis=0)
        return x + upd.reshape(B, S, D)

    x = A_x
    g = _sigmoid((x * gate_w).sum(-1, keepdims=True))
    a = _A_ref(x, A_dft_seq_re, A_dft_seq_im, A_dft_hid_re, A_dft_hid_im)
    b = _B_ref(x, B_w_r, B_w_gate, B_w_up, B_w_down)
    return g * a + (1 - g) * b
```

```cpp
#include <hip/hip_runtime.h>
#include <hip/hip_bf16.h>
#include <stdint.h>
#include <type_traits>

using bf16 = __hip_bfloat16;
using bf16x8 = __attribute__((ext_vector_type(8))) short;
using f32x4 = __attribute__((ext_vector_type(4))) float;
using f32x2 = __attribute__((ext_vector_type(2))) float;
using u32x4 = __attribute__((ext_vector_type(4))) unsigned;
using u32x2 = __attribute__((ext_vector_type(2))) unsigned;
typedef __attribute__((ext_vector_type(2))) __bf16 bf16x2;
typedef unsigned short u16;
typedef unsigned long long u64;
typedef __attribute__((address_space(3))) unsigned lds_u32;

constexpr int S_ = 2048, D_ = 512, F_ = 1792;

__device__ __forceinline__ float bf2f(u16 v) { return __uint_as_float(((unsigned)v) << 16); }
__device__ __forceinline__ u16 f2bf(float f) {
  bf16 h = __float2bfloat16(f);
  return *reinterpret_cast<u16*>(&h);
}
__device__ __forceinline__ float rbf(float f) { return bf2f(f2bf(f)); }
__device__ __forceinline__ unsigned pk2(float a, float b) {
  return __builtin_bit_cast(unsigned, __builtin_convertvector(f32x2{a, b}, bf16x2));
}
__device__ __forceinline__ float lo2f(unsigned v) { return __uint_as_float(v << 16); }
__device__ __forceinline__ float hi2f(unsigned v) { return __uint_as_float(v & 0xffff0000u); }
__device__ __forceinline__ float sigmoidf_(float v) { return 1.0f / (1.0f + __expf(-v)); }

template <int CTRL>
__device__ __forceinline__ unsigned dpp(unsigned v) { return __builtin_amdgcn_update_dpp(0, v, CTRL, 0xf, 0xf, false); }
template <int CTRL>
__device__ __forceinline__ float dpp_add(float v) { return v + __uint_as_float(dpp<CTRL>(__float_as_uint(v))); }
constexpr int DPP_X1 = 0xb1, DPP_X2 = 0x4e, DPP_HM = 0x141, DPP_M = 0x140;
__device__ __forceinline__ unsigned row_max(unsigned v) {
  v = max(v, dpp<DPP_X1>(v));
  v = max(v, dpp<DPP_X2>(v));
  v = max(v, dpp<DPP_HM>(v));
  return max(v, dpp<DPP_M>(v));
}
__device__ __forceinline__ float sw32(float a, float b) {
  const auto r = __builtin_amdgcn_permlane32_swap(__float_as_uint(a), __float_as_uint(b), false, false);
  return __uint_as_float(r[0]) + __uint_as_float(r[1]);
}
__device__ __forceinline__ float sw16(float a, float b) {
  const auto r = __builtin_amdgcn_permlane16_swap(__float_as_uint(a), __float_as_uint(b), false, false);
  return __uint_as_float(r[0]) + __uint_as_float(r[1]);
}
__device__ __forceinline__ unsigned wave_max(unsigned v) {
  v = row_max(v);
  const auto r = __builtin_amdgcn_permlane16_swap(v, v, false, false);
  v = max(r[0], r[1]);
  const auto q = __builtin_amdgcn_permlane32_swap(v, v, false, false);
  return max(q[0], q[1]);
}

#define WAIT_V(n) asm volatile("s_waitcnt vmcnt(%0)" ::"i"(n) : "memory")
#define WAIT_L(n) asm volatile("s_waitcnt lgkmcnt(%0)" ::"i"(n) : "memory")
#define SB() __builtin_amdgcn_sched_barrier(0)

constexpr int A_B = 128 * 64 * 2;
constexpr int RING = 155648;
constexpr int SCR = 8192;
constexpr int SPIN_MAX = 1 << 16;
constexpr int L_LF = 0;
constexpr int L_CP = 1024;
constexpr int L_RG = 2048;
constexpr int L_G = 4096;
constexpr int L_FLAG = 256;
constexpr int L_H = 4608;
constexpr int L_UPD = 4736;
constexpr int L_RF = 4992;
constexpr int L_X = 7168;

constexpr size_t W_AB = 0;
constexpr size_t W_XB = (size_t)4 << 20;
constexpr size_t W_E = (size_t)16 << 20;
constexpr size_t W_BEST = W_E + 4096;
constexpr size_t W_SEEN = W_E + 8192;
constexpr size_t W_KEY = W_E + 16384;
constexpr size_t W_PA = W_E + 65536;
constexpr size_t W_PB = W_E + 524288;
constexpr unsigned C_SEEN = W_SEEN - W_E, C_BEST = W_BEST - W_E, C_KEY = W_KEY - W_E, C_PA = W_PA - W_E, C_PB = W_PB - W_E;
constexpr int PSLEEP = 4, LSLEEP = 1, GSLEEP = 1, FSTR = 64;
constexpr unsigned C_FL2 = 1u << 20;

template <int OFF>
__device__ __forceinline__ void lds_rd128(bf16x8& d, unsigned a) {
  asm volatile("ds_read_b128 %0, %1 offset:%2" : "=v"(d) : "v"(a), "i"(OFF) : "memory");
}
__device__ __forceinline__ void glds(unsigned lds, unsigned voff, const void* sbase) {
  asm volatile("s_mov_b32 m0, %0\n\ts_nop 0\n\tglobal_load_lds_dwordx4 %1, %2" ::"s"(lds), "v"(voff), "s"(sbase) : "memory");
}
#define GLDS4(POL) asm volatile("s_add_u32 m0, %0, %6\n\ts_nop 0\n\tglobal_load_lds_dwordx4 %1, %5" POL "\n\t"                        \
                                "global_load_lds_dwordx4 %2, %5 offset:1024" POL "\n\tglobal_load_lds_dwordx4 %3, %5 offset:2048" POL "\n\t" \
                                "global_load_lds_dwordx4 %4, %5 offset:3072" POL                                                       \
                                ::"s"(lds), "v"(v0), "v"(v1), "v"(v2), "v"(v3), "s"(sbase), "i"(OFF) : "scc", "memory")
template <int OFF>
__device__ __forceinline__ void glds4(unsigned lds, unsigned v0, unsigned v1, unsigned v2, unsigned v3, const void* sbase) { GLDS4(""); }
template <int OFF>
__device__ __forceinline__ void glds4_sc1(unsigned lds, unsigned v0, unsigned v1, unsigned v2, unsigned v3, const void* sbase) { GLDS4(" sc1"); }
template <int ADD, int MASK, int SH>
__device__ __forceinline__ const char* padd(const char* base, unsigned o) {
  const u64 bb = (u64)base;
  unsigned lo, hi;
  asm volatile("s_add_u32 %0, %4, %5\n\ts_and_b32 %0, %0, %6\n\ts_lshl_b32 %0, %0, %7\n\ts_add_u32 %0, %2, %0\n\ts_addc_u32 %1, %3, 0"
               : "=&s"(lo), "=s"(hi)
               : "s"((unsigned)bb), "s"((unsigned)(bb >> 32)), "s"(o), "i"(ADD), "i"(MASK), "i"(SH)
               : "scc");
  return (const char*)(((u64)hi << 32) | lo);
}
__device__ __forceinline__ void gate(u64& seen, u64 want, unsigned sh, unsigned fo, unsigned tag, const void* wc, unsigned& bud) {
  u64 t, w;
  unsigned v;
  asm volatile("s_lshl_b64 %[w], %[want], %[sh]\n\ts_andn2_b64 %[t], %[w], %[seen]\n\ts_cbranch_scc0 .Lsb%=\n"
               ".Lsa%=:\n\tglobal_load_dword %[v], %[fo], %[wc] sc1\n\ts_waitcnt vmcnt(0)\n\tv_cmp_eq_u32 %[t], %[tag], %[v]\n\t"
               "s_or_b64 %[seen], %[seen], %[t]\n\ts_andn2_b64 %[t], %[w], %[seen]\n\ts_cbranch_scc0 .Lsb%=\n\ts_sleep %[sl]\n\t"
               "s_sub_u32 %[bud], %[bud], 16\n\ts_cbranch_scc0 .Lsa%=\n\ts_mov_b32 %[bud], 0\n.Lsb%=:"
               : [seen] "+s"(seen), [t] "=&s"(t), [w] "=&s"(w), [v] "=&v"(v), [bud] "+s"(bud)
               : [want] "s"(want), [sh] "s"(sh), [fo] "v"(fo), [tag] "s"(tag), [wc] "s"(wc), [sl] "i"(GSLEEP)
               : "scc", "memory");
}
template <int S>
__device__ __forceinline__ const char* k1tile(const char* base, unsigned rot) { return padd<S * 128, 0x380, 0>(base, rot << 7); }
__device__ __forceinline__ void gst128_sc1(unsigned voff, u32x4 v, const void* sbase) {
  asm volatile("global_store_dwordx4 %0, %1, %2 sc1\n\ts_nop 1" ::"v"(voff), "v"(v), "s"(sbase) : "memory");
}

__device__ __forceinline__ void lds_rd32(unsigned& d, unsigned a) { asm volatile("ds_read_b32 %0, %1" : "=v"(d) : "v"(a) : "memory"); }
__device__ __forceinline__ void sigv(unsigned row, const void* tab, unsigned n4) {
  asm volatile("s_mov_b32 m0, %0\n\ts_nop 0\n\tglobal_load_lds_dword %2, %1" ::"s"(row), "s"(tab), "v"(n4) : "memory");
}
template <int N>
__device__ __forceinline__ void sig(unsigned row, const void* tab) {
  unsigned t;
  asm volatile("s_mov_b32 m0, %1\n\tv_mov_b32 %0, %3\n\tglobal_load_lds_dword %0, %2" : "=&v"(t) : "s"(row), "s"(tab), "i"(N * 4) : "memory");
}
template <int N>
__device__ __forceinline__ void spin(unsigned a, unsigned& bud) {
  unsigned t;
  asm volatile(".Lsa%=:\n\tds_read_b32 %0, %2\n\ts_waitcnt lgkmcnt(0)\n\tv_cmp_gt_u32 vcc, %3, %0\n\ts_cbranch_vccz .Lsb%=\n\t"
               "s_sub_u32 %1, %1, 1\n\ts_cbranch_scc0 .Lsa%=\n\ts_mov_b32 %1, 0\n.Lsb%=:"
               : "=&v"(t), "+s"(bud) : "v"(a), "i"(N) : "vcc", "scc", "memory");
}
#define FT0 ""
#define FT1 "\n"
#define FLAG_ASM "s_getreg_b32 %[ib], hwreg(HW_REG_IB_STS)\n\ts_and_b32 %[ib], %[ib], 0xc0000f\n\ts_or_b32 %[ib], %[ib], %[done]\n\t"   \
                 "s_cmp_gt_u32 %[ib], %[y]\n\ts_cbranch_scc1 .Lsk%=\n\ts_waitcnt vmcnt(%[y])\n\ts_mov_b64 exec, 0xffff\n\t"                     \
                 FT0 "global_store_dword %[fo], %[tag], %[wc] sc1\n\ts_mov_b64 exec, -1\n\ts_mov_b32 %[done], 64\n\t" FT1 ".Lsk%=:\n\t"
template <int Y>
__device__ __forceinline__ void tryflag(unsigned& done, unsigned fo, unsigned tag, const void* wc) {
  static_assert(Y < 16, "");
  unsigned ib;
  asm volatile(FLAG_ASM : [ib] "=&s"(ib), [done] "+s"(done) : [fo] "v"(fo), [tag] "v"(tag), [wc] "s"(wc), [y] "i"(Y) : "scc", "memory");
}
template <int N, int Y>
__device__ __forceinline__ void spinf(unsigned a, unsigned& bud, unsigned& done, unsigned fo, unsigned tag, const void* wc) {
  static_assert(Y < 16, "");
  unsigned t, ib;
  asm volatile(".Lsa%=:\n\tds_read_b32 %[t], %[a]\n\t" FLAG_ASM "s_waitcnt lgkmcnt(0)\n\tv_cmp_gt_u32 vcc, %[n], %[t]\n\ts_cbranch_vccz .Lsb%=\n\t"
               "s_sub_u32 %[bud], %[bud], 1\n\ts_cbranch_scc0 .Lsa%=\n\ts_mov_b32 %[bud], 0\n.Lsb%=:"
               : [t] "=&v"(t), [bud] "+s"(bud), [ib] "=&s"(ib), [done] "+s"(done)
               : [a] "v"(a), [n] "i"(N), [fo] "v"(fo), [tag] "v"(tag), [wc] "s"(wc), [y] "i"(Y)
               : "vcc", "scc", "memory");
}
template <int N, int SL>
__device__ __forceinline__ void serve(unsigned a, unsigned& bud) {
  unsigned t;
  asm volatile(".Lsa%=:\n\tds_read_b32 %0, %2\n\ts_waitcnt lgkmcnt(0)\n\tv_cmp_gt_u32 vcc, %3, %0\n\ts_cbranch_vccz .Lsb%=\n\ts_sleep %4\n\t"
               "s_sub_u32 %1, %1, 1\n\ts_cbranch_scc0 .Lsa%=\n\ts_mov_b32 %1, 0\n.Lsb%=:"
               : "=&v"(t), "+s"(bud) : "v"(a), "i"(N), "i"(SL) : "vcc", "scc", "memory");
}
__device__ __forceinline__ void mm(f32x4& d, const bf16x8& a, const bf16x8& b) {
  asm volatile("v_mfma_f32_16x16x32_bf16 %0, %1, %2, %0" : "+v"(d) : "v"(a), "v"(b));
}
template <int N>
__device__ __forceinline__ void mmw(f32x4& d, const bf16x8& a, const bf16x8& b) {
  __builtin_amdgcn_s_waitcnt(0xc07f | (N << 8));
  SB();
  asm volatile("v_mfma_f32_16x16x32_bf16 %0, %1, %2, %0" : "+v"(d) : "v"(a), "v"(b) : "memory");
}
__device__ __forceinline__ void mm0(f32x4& d, const bf16x8& a, const bf16x8& b) {
  asm volatile("v_mfma_f32_16x16x32_bf16 %0, %1, %2, 0" : "=&v"(d) : "v"(a), "v"(b));
}
#define CHK_TAIL "s_cbranch_vccnz .Lsa%=\n.Lsb%=:\n\t.subsection 1\n.Lsa%=:\n\tds_read_b32 %[fl], %[a]\n\ts_waitcnt lgkmcnt(0)\n\t"        \
                 "v_cmp_gt_u32 vcc, %[n], %[fl]\n\ts_cbranch_vccz .Lsb%=\n\ts_sub_u32 %[bud], %[bud], 1\n\ts_cbranch_scc0 .Lsa%=\n\t"       \
                 "s_mov_b32 %[bud], 0\n\ts_branch .Lsb%=\n\t.subsection 0"
template <int N>
__device__ __forceinline__ void mmchk(f32x4& d, const bf16x8& x, const bf16x8& y, unsigned& fl, unsigned a, unsigned& bud) {
  asm volatile("v_cmp_gt_u32 vcc, %[n], %[fl]\n\tv_mfma_f32_16x16x32_bf16 %[d], %[x], %[y], %[d]\n\t" CHK_TAIL
               : [d] "+v"(d), [fl] "+v"(fl), [bud] "+s"(bud) : [x] "v"(x), [y] "v"(y), [a] "v"(a), [n] "i"(N) : "vcc", "scc", "memory");
}
template <int N, int W>
__device__ __forceinline__ void mmchkw(f32x4& d, const bf16x8& x, const bf16x8& y, unsigned& fl, unsigned a, unsigned& bud) {
  __builtin_amdgcn_s_waitcnt(0xc07f | (W << 8));
  SB();
  asm volatile("v_cmp_gt_u32 vcc, %[n], %[fl]\n\tv_mfma_f32_16x16x32_bf16 %[d], %[x], %[y], %[d]\n\t" CHK_TAIL
               : [d] "+v"(d), [fl] "+v"(fl), [bud] "+s"(bud) : [x] "v"(x), [y] "v"(y), [a] "v"(a), [n] "i"(N) : "vcc", "scc", "memory");
}
template <int N>
__device__ __forceinline__ void mm0w(f32x4& d, const bf16x8& a, const bf16x8& b) {
  asm volatile("s_waitcnt lgkmcnt(%3)\n\tv_mfma_f32_16x16x32_bf16 %0, %1, %2, 0" : "=&v"(d) : "v"(a), "v"(b), "i"(N) : "memory");
}
template <int N>
__device__ __forceinline__ void chk(unsigned& fl, unsigned a, unsigned& bud) {
  asm volatile("v_cmp_gt_u32 vcc, %[n], %[fl]\n\t" CHK_TAIL : [fl] "+v"(fl), [bud] "+s"(bud) : [a] "v"(a), [n] "i"(N) : "vcc", "scc", "memory");
}

__global__ __launch_bounds__(512, 2) void fused_k(const u16* __restrict__ wsre, const u16* __restrict__ wsim,
                                               const u16* __restrict__ x, const u16* __restrict__ w_r,
                                               const u16* __restrict__ gate_w, const u16* __restrict__ wdre,
                                               const u16* __restrict__ wdim, const u16* __restrict__ w_gate,
                                               const u16* __restrict__ w_up, const u16* __restrict__ w_down,
                                               char* ws, u16* __restrict__ out) {
  __shared__ __attribute__((aligned(16))) char smem[RING + SCR];
  static_assert(RING + SCR == 163840, "the whole LDS: one block per CU, the array starts at LDS address 0");
  asm volatile("ds_write2st64_b32 %0, %1, %1 offset1:8\n\ts_waitcnt lgkmcnt(0)\n\ts_barrier" ::"v"((unsigned)(size_t)smem + RING + ((threadIdx.x ^ 256u) << 2)), "v"(0u) : "memory");
  asm volatile("" ::"s"(wsre), "s"(wsim), "s"(x), "s"(w_r), "s"(gate_w), "s"(wdre), "s"(wdim), "s"(w_gate), "s"(w_up), "s"(w_down), "s"(ws));
  const char* wc = ws + W_E;
  const unsigned c_in = (unsigned)__builtin_amdgcn_s_memtime(), r_in = (unsigned)__builtin_amdgcn_s_memrealtime();
#define UNTIL_(now, t0, lim) do { _Pragma("clang loop unroll(disable)") for (int i_ = 0; i_ < 4096; ++i_) { if ((unsigned)(now) - (t0) >= (lim)) break; __builtin_amdgcn_s_sleep(1); } } while (0)
#define UNTIL(ticks, cyc) do { UNTIL_(__builtin_amdgcn_s_memrealtime(), r_in, ticks); UNTIL_(__builtin_amdgcn_s_memtime(), c_in, cyc); } while (0)
  const int tid = threadIdx.x, lane = tid & 63;
  const int wid = __builtin_amdgcn_readfirstlane(tid >> 6);
  const int b = blockIdx.x;
  const int xcd = b & 7, off = b >> 3;
  const int q = xcd >> 1;
  const int h = q >> 1, kh = q & 1;
  const int ej = off & 3, tl = off >> 2;
  const int e0 = (ej >> 1) * 256 + q * 64 + (ej & 1) * 32;
  const bool isq = ej & 1;
  unsigned isp = (unsigned)(~ej & 1);
  asm volatile("" : "+s"(isp));
  const int ti = (xcd & 1) * 8 + tl;
  const int si = (xcd & 3) * 4 + (tl & 3), cp1 = (tl >> 2) * 2 + (ej >> 1);
  const int fr = lane & 15, fq = lane >> 4;
  const int r8 = lane >> 3, c8 = lane & 7;
  const unsigned sb = (unsigned)(size_t)smem;
  volatile lds_u32* lflag = (volatile lds_u32*)(smem + RING + L_FLAG);
  const char* ab = ws + W_AB;
  const unsigned rot = (unsigned)(tl), flip = (unsigned)((ej & 2) * 4);
#define KADDR(P, SH, base) padd<(P) & 7, 7, SH>((base) + ((((P) & 8) ^ flip) << (SH)), rot)
  const unsigned rot1 = (unsigned)((2 * (cp1 + tl) + (xcd & 1)) & 7);
  const unsigned row_s = __builtin_amdgcn_readfirstlane(sb + RING + ((wid ^ 4) << 8));
  u64 pc;
  unsigned tab_o;
  asm volatile("s_getpc_b64 %0\n.Lfk_here:\n\ts_mov_b32 %1, .Lfk_tab-.Lfk_here\n\t.subsection 2\n\t.p2align 8\n.Lfk_tab:\n\t"
               ".set fk_i, 0\n\t.rept 128\n\t.long fk_i\n\t.set fk_i, fk_i+1\n\t.endr\n\t.subsection 0"
               : "=s"(pc), "=s"(tab_o));
  const char* tab = reinterpret_cast<const char*>(pc + tab_o);
#define SIG(n) sig<n>(row_s, tab)
  const unsigned lf_rd = sb + RING + L_LF + (lane & 1) * 516, cp_rd = sb + RING + ((lane & 7) == 4 ? L_LF + 784 : L_CP + (lane & 3) * 260);
  const unsigned la_rd = sb + RING + L_LF, lb_rd = sb + RING + L_LF + 512;
  const unsigned own_rd = sb + RING + L_FLAG + 32 + (lane & 3) * 4;
#define IC(k) std::integral_constant<int, k>{}

  unsigned bud = 1u << 20;
#define SERVE(ADDR, N) serve<N, LSLEEP>(ADDR, bud)
  const unsigned best_rd = sb + RING + L_FLAG + 12, d_rd = sb + RING + L_FLAG + 48;
  const unsigned rf_rd = sb + RING + L_RG + 1024;
  const unsigned tg_rd = sb + RING + L_FLAG + 56, gt_rd = sb + RING + L_FLAG + 4;
#define TAG_WAIT() do { SERVE(tg_rd, 1); tag = __builtin_amdgcn_readfirstlane(lflag[15]); } while (0)
  const unsigned lbase = __builtin_amdgcn_readfirstlane(sb);
  auto row_addr = [&](unsigned (&va)[8]) {
    unsigned l = (unsigned)lane;
    asm volatile("" : "+v"(l));
#pragma unroll
    for (int i = 0; i < 8; ++i) va[i] = (i * 8 + (l >> 3)) * (D_ * 2) + (((l & 7) ^ (l >> 3)) << 4) - (i & 3) * 1024;
  };
  const char* xs = (const char*)x + (size_t)si * 128 * D_ * 2;
  const char* wds = (const char*)(h ? wdim : wdre) + (size_t)cp1 * 128 * D_ * 2;
  if ((wid | 2) == 6) {
    unsigned va[8];
    row_addr(va);
    const bool a = wid == 4;
    const char* base = a ? xs : wds;
    const unsigned n = isq ? (a ? 1 * 2u : 1 * 2u) : 2u;
    const unsigned r0 = rot1 + (isq ? 1u : 0u);
    unsigned l0 = lbase + (a ? 0 : isq ? 131072 : 81920);
    if (!a && !isq) {
      glds(lbase + RING + L_RF, (unsigned)(lane * 16), w_r);
      glds(lbase + RING + L_RF + 1152, (unsigned)(lane * 16), gate_w);
      sig<1>(lbase + RING + L_RG + 1024, tab);
    }
    if (isq) __builtin_amdgcn_s_sleep(20);
#pragma clang loop unroll(disable)
    for (unsigned u = 0; u < n; ++u) {
      const char* p = base + (((((u >> 1) + r0) & 7u) << 7) + ((u & 1u) << 16));
      glds4<0>(l0, va[0], va[1], va[2], va[3], p);
      glds4<4096>(l0, va[4], va[5], va[6], va[7], p);
      sigv(row_s, tab, ((u + 1u) >> 1) << 2);
      l0 += 8192u;
    }
  } else {
    const unsigned len = tab_o + 512u;
    const unsigned po = (unsigned)((wid * 32 + off) * 1024 + lane * 16);
    if (po + 16u <= len) glds(lbase + RING + L_X, po, reinterpret_cast<const char*>(pc & ~63ull));
  }
  unsigned E;
  asm volatile("s_cmp_lg_u32 %3, 7\n\ts_cbranch_scc1 .Le%=\n\tglobal_load_dword %0, %1, %2 sc1\n.Le%=:" : "=&v"(E) : "v"(0u), "s"(wc), "s"(wid) : "scc", "memory");
  if (wid >= 4) {
  unsigned vw[16], vx[16];
  auto X_ADDR = [&]() {
    unsigned va[8];
    row_addr(va);
#pragma unroll
    for (int i = 0; i < 16; ++i) vx[i] = va[i & 7] + (unsigned)((i >> 3) * 64 * D_ * 2);
  };
  const char* Ws = (const char*)(h ? wsim : wsre) + (size_t)ti * 128 * S_ * 2 + kh * S_;
  auto tile16 = [&](auto O, const char* p, const unsigned (&v)[16]) {
    glds4<decltype(O)::value>(lbase, v[0], v[1], v[2], v[3], p);
    glds4<decltype(O)::value + 4096>(lbase, v[4], v[5], v[6], v[7], p);
    glds4<decltype(O)::value + 8192>(lbase, v[8], v[9], v[10], v[11], p);
    glds4<decltype(O)::value + 12288>(lbase, v[12], v[13], v[14], v[15], p);
  };
  auto W_ADDR = [&]() {
    unsigned r8_ = (unsigned)r8;
    asm volatile("" : "+v"(r8_));
#pragma unroll
    for (int i = 0; i < 16; ++i) vw[i] = (i * 8 + r8_) * (S_ * 2) + (((unsigned)c8 ^ r8_) << 4) - (i & 3) * 1024;
  };
  auto stageW = [&](auto P, auto O) { tile16(O, KADDR(decltype(P)::value, 7, Ws), vw); };
  const int qi = (off >> 1) * 8 + xcd;
  const unsigned g_o = (unsigned)(C_KEY + ti * 1024 + (lane >> 2 & 7) * 128 + (lane & 3) * 16);
#define G_PUT(gv, best) asm volatile("ds_write_b128 %0, %1\n\tds_write_b32 %2, %3" ::"v"(sb + RING + L_G + (lane & 31) * 16),                                    \
                                     "v"(u32x4{gv.x << 16, gv.x & 0xffff0000u, gv.y << 16, gv.y & 0xffff0000u}), "v"(best_rd), "v"(best) : "memory")
  if (wid == 4) {
    X_ADDR();
    auto stageX = [&](auto K, auto O) { tile16(O, k1tile<decltype(K)::value>(xs, rot1), vx); };
    W_ADDR();
    if (isp) {
    stageX(IC(1), IC(16384));
    SIG(2);
    stageX(IC(2), IC(32768));
    SIG(3);
    stageX(IC(3), IC(49152));
    SIG(4);
    stageX(IC(4), IC(65536));
    SIG(5);
    SERVE(cp_rd, 1);
    stageX(IC(5), IC(0));
    SIG(6);
    SERVE(cp_rd, 2);
    stageX(IC(6), IC(16384));
    SIG(7);
    SERVE(cp_rd, 3);
    stageX(IC(7), IC(32768));
    SIG(8);
    }
    if (isq) {
    }
    if (isp) SERVE(cp_rd, 4);
    stageW(IC(0), IC(49152));
    if (isp) SERVE(cp_rd, 5);
    stageW(IC(1), IC(65536));
    SIG(9);
    stageW(IC(2), IC(81920));
    if (isp) SERVE(cp_rd, 6);
    stageW(IC(3), IC(0));
    SIG(10);
    SERVE(own_rd, 1);
    stageW(IC(4), IC(98304));
    if (isp) SERVE(cp_rd, 7);
    stageW(IC(5), IC(16384));
    SIG(11);
    stageW(IC(6), IC(114688));
    if (isp) SERVE(cp_rd, 8);
    stageW(IC(7), IC(32768));
    SIG(15);
    if (isq) { SERVE(lb_rd, 12); }
    stageW(IC(8), IC(131072));
    SIG(16);
    SERVE(own_rd, 2);
    stageW(IC(9), IC(49152));
    SIG(17);
    stageW(IC(10), IC(65536));
    SIG(18);
    SERVE(own_rd, 3);
    stageW(IC(11), IC(81920));
    SIG(64);
    bud = 1u << 18;
    __builtin_amdgcn_s_setprio(3);
    unsigned tag;
    TAG_WAIT();
    if (isp) {
      UNTIL(855u, 17991u);
      u32x2 kb;
      u32x4 gv;
#pragma clang loop unroll(disable)
      for (int it = 0; it < SPIN_MAX; ++it) {
        asm volatile("global_load_dwordx2 %0, %2, %4 sc1\n\tglobal_load_dwordx4 %1, %3, %4 sc1\n\ts_waitcnt vmcnt(0)" : "=&v"(kb), "=&v"(gv) : "v"((unsigned)C_BEST), "v"(g_o), "s"(wc) : "memory");
        if (__all(kb.y == tag && gv.w == tag)) break;
        __builtin_amdgcn_s_sleep(PSLEEP);
      }
      G_PUT(gv, kb.x);
    }
    SERVE(best_rd, 1);
    const unsigned best = __builtin_amdgcn_readfirstlane(lflag[3]);
    const int idx = (4095 - (int)(best & 0xfffu)) & (S_ - 1);
    if ((idx >> 7) == ti) {
      const unsigned uo = best & 0xffff0000u;
      const float rsel = rbf(sigmoidf_(__uint_as_float((uo & 0x80000000u) ? (uo & 0x7fffffffu) : (~uo & 0xffff0000u))));
      const int col = (lane & 7) * 2 + (lane >> 5);
      unsigned xv[2];
      asm volatile("global_load_ushort %0, %2, %3\n\tglobal_load_ushort %1, %2, %3 offset:32\n\ts_waitcnt vmcnt(0)"
                   : "=&v"(xv[0]), "=&v"(xv[1]) : "v"((unsigned)((idx * D_ + e0 + col) * 2)), "s"(x) : "memory");
      float rs_ = rsel;
      unsigned pa = sb + RING + L_UPD + col * 2;
      u32x4 ov[8];
      unsigned go = (unsigned)(C_PB + r8 * 4096 + e0 * 8 + c8 * 16);
      unsigned g3 = lane < 32 ? go + 98304u : (unsigned)(C_SEEN + (lane & 7) * 256);
      asm volatile("" : "+v"(rs_), "+v"(pa), "+v"(go), "+v"(g3));
      SERVE(cp_rd, 18); UNTIL(1045u, 21373u);
#pragma clang loop unroll(disable)
      for (int it = 0; it < SPIN_MAX; ++it) {
        asm volatile("global_load_dwordx4 %0, %8, %12 sc1\n\tglobal_load_dwordx4 %1, %9, %12 sc1\n\t"
                     "global_load_dwordx4 %2, %10, %12 sc1\n\tglobal_load_dwordx4 %3, %11, %12 sc1\n\t"
                     "global_load_dwordx4 %4, %8, %12 offset:128 sc1\n\tglobal_load_dwordx4 %5, %9, %12 offset:128 sc1\n\t"
                     "global_load_dwordx4 %6, %10, %12 offset:128 sc1\n\tglobal_load_dwordx4 %7, %11, %12 offset:128 sc1\n\ts_waitcnt vmcnt(0)"
                     : "=&v"(ov[0]), "=&v"(ov[1]), "=&v"(ov[2]), "=&v"(ov[3]), "=&v"(ov[4]), "=&v"(ov[5]), "=&v"(ov[6]), "=&v"(ov[7])
                     : "v"(go), "v"(go + 32768u), "v"(go + 65536u), "v"(g3), "s"(wc)
                     : "memory");
        bool ok = true;
#pragma unroll
        for (int k = 0; k < 8; ++k) ok = ok && ov[k].y == tag && ov[k].w == tag;
        if (__all(ok)) break;
        __builtin_amdgcn_s_sleep(PSLEEP);
      }
      if (ej == 0 && q == 0 && lane == 0)
        asm volatile("global_store_dword %0, %1, %2 sc1\n\ts_nop 1" ::"v"(0u), "v"(tag), "s"(wc) : "memory");
      float u[2];
#pragma unroll
      for (int c = 0; c < 2; ++c) {
        if (lane >= 32) ov[c * 4 + 3].x = ov[c * 4 + 3].z = 0u;
        float s0 = (__uint_as_float(ov[c * 4].x) + __uint_as_float(ov[c * 4 + 1].x)) + (__uint_as_float(ov[c * 4 + 2].x) + __uint_as_float(ov[c * 4 + 3].x));
        float s1 = (__uint_as_float(ov[c * 4].z) + __uint_as_float(ov[c * 4 + 1].z)) + (__uint_as_float(ov[c * 4 + 2].z) + __uint_as_float(ov[c * 4 + 3].z));
        s0 = dpp_add<0x128>(s0); s1 = dpp_add<0x128>(s1);
        const float t2 = sw16(sw32(s0, s1), sw32(s0, s1));
        u[c] = rbf(rbf(t2) * rs_);
      }
      const unsigned p0 = f2bf(lo2f(xv[0]) + u[0]), p1 = f2bf(lo2f(xv[1]) + u[1]);
      if ((lane & 31) < 8)
        asm volatile("ds_write_b16 %0, %1\n\tds_write_b16 %0, %2 offset:32"
                     ::"v"(pa), "v"(p0), "v"(p1) : "memory");
    }
    asm volatile("ds_write_b32 %0, %1\n\ts_waitcnt vmcnt(0) lgkmcnt(0)" ::"v"(d_rd), "v"(1u) : "memory");
  } else if (wid == 6) {
    X_ADDR();
    const unsigned vb0 = (unsigned)(lane * 16), vb2 = vb0 + 2048u;
    const char* abg = ab + (size_t)((h * 4 + ej) * 32 + kh * 8) * A_B;
    const char* abA = abg + (h ^ 1) * 16 * A_B;
    const char* abH = abg + h * 16 * A_B;
    auto stageD = [&](auto K, auto O) { tile16(O, k1tile<decltype(K)::value>(wds, rot1), vx); };
    auto stageQ = [&](auto O) {
      const char* p = (const char*)x + (size_t)ti * 128 * D_ * 2 + e0 * 2;
      const unsigned v = (unsigned)((lane >> 2) * (D_ * 2) + ((lane & 3) ^ (lane >> 4)) * 16);
      glds4<decltype(O)::value>(lbase, v, v + 15360u, v + 30720u, v + 46080u, p);
      glds4<decltype(O)::value + 4096>(lbase, v, v + 15360u, v + 30720u, v + 46080u, p + 64 * (D_ * 2));
    };
    unsigned tag;
    u64 seen = 0ull;
    const unsigned flag_o = (unsigned)(C_FL2 + (((h * 4 + ej) * 2 + kh) * 32 + (lane & 31)) * FSTR);
    auto GATE_ALL = [&]() {
      gate(seen, ~0ull, 0u, flag_o, tag, wc, bud);
      if (isq) asm volatile("ds_write_b32 %0, %1" ::"v"(gt_rd), "v"(1u) : "memory");
    };
    const unsigned dl = (unsigned)(kh ^ 1) * 2048u;
    const unsigned vc0 = vb2 + dl, vc1 = vb2 - dl, vc2 = vc0 + 4096u, vc3 = vc1 + 4096u;
    auto stageA = [&](auto P, auto H, auto O, auto G) {
      constexpr int hf = decltype(H)::value;
      const char* p = KADDR(decltype(P)::value, 13, (hf ? abH : abA) - 2048);
      glds4_sc1<decltype(O)::value>(lbase, vc0, vc0, vc1, vc1, p);
      glds4_sc1<decltype(O)::value + 4096>(lbase, vc2, vc2, vc3, vc3, p);
    };
    auto stageN = [&](auto P, auto O, const char* base) {
      glds4_sc1<decltype(O)::value>(lbase, vb0, vb0, vb2, vb2, KADDR(decltype(P)::value, 13, base));
      glds4_sc1<decltype(O)::value + 4096>(lbase, vb0, vb0, vb2, vb2, KADDR(decltype(P)::value + 1, 13, base));
    };
    if (isp) {
    stageD(IC(1), IC(98304));
    SIG(2);
    WAIT_V(18);
    stageD(IC(2), IC(114688));
    SIG(3);
    WAIT_V(18);
    stageD(IC(3), IC(131072));
    SIG(4);
    SERVE(cp_rd, 1);
    WAIT_V(18);
    stageD(IC(4), IC(81920));
    SIG(5);
    SERVE(cp_rd, 2);
    WAIT_V(18);
    stageD(IC(5), IC(98304));
    SIG(6);
    SERVE(cp_rd, 3);
    WAIT_V(18);
    stageD(IC(6), IC(114688));
    SIG(7);
    SERVE(cp_rd, 4);
    WAIT_V(18);
    stageD(IC(7), IC(131072));
    SIG(8);
    }
    if (isq) UNTIL(144u, 3075u);
    stageQ(IC(147456));
    sig<12>(row_s, tab);
    W_ADDR();
    TAG_WAIT();
    SERVE(own_rd, 1);
    SERVE(cp_rd, 8);
    SERVE(la_rd, 9);
    SERVE(own_rd, 4); if (isq) SERVE(own_rd, 5);
    UNTIL(569u, 11787u);
    GATE_ALL();
    SERVE(own_rd, 3);
    stageA(IC(0), IC(0), IC(0), IC(0));
    SIG(13);
    stageA(IC(0), IC(1), IC(8192), IC(0));
    SIG(14);
    SERVE(own_rd, 4);
    stageA(IC(1), IC(0), IC(98304), IC(0));
    SIG(15);
    stageA(IC(1), IC(1), IC(106496), IC(0));
    SIG(16);
    stageA(IC(2), IC(0), IC(16384), IC(0));
    stageA(IC(2), IC(1), IC(24576), IC(0));
    SIG(17);
    SERVE(own_rd, 5);
    stageA(IC(3), IC(0), IC(114688), IC(0));
    stageA(IC(3), IC(1), IC(122880), IC(0));
    SIG(18);
    stageA(IC(4), IC(0), IC(32768), IC(0));
    stageA(IC(4), IC(1), IC(40960), IC(0));
    SIG(19);
    SERVE(cp_rd, 9);
    stageA(IC(5), IC(0), IC(0), IC(0));
    SERVE(cp_rd, 10);
    stageA(IC(5), IC(1), IC(8192), IC(0));
    SIG(20);
    SERVE(cp_rd, 11);
    stageW(IC(12), IC(98304));
    stageA(IC(6), IC(0), IC(131072), IC(0));
    stageA(IC(7), IC(0), IC(139264), IC(0));
    SIG(21);
    SERVE(cp_rd, 12);
    stageW(IC(13), IC(16384));
    stageN(IC(6), IC(49152), abH + dl);
    stageN(IC(8), IC(57344), abA + dl);
    SIG(22);
    SERVE(cp_rd, 13);
    stageW(IC(14), IC(65536));
    stageN(IC(10), IC(114688), abA + dl);
    SERVE(cp_rd, 14);
    stageW(IC(15), IC(32768));
    stageN(IC(12), IC(122880), abA + dl);
    SIG(23);
    stageN(IC(14), IC(81920), abA + dl);
    stageN(IC(8), IC(90112), abA + (dl ^ 2048u));
    SIG(24);
    SERVE(cp_rd, 15);
    stageN(IC(10), IC(0), abA + (dl ^ 2048u));
    stageN(IC(12), IC(8192), abA + (dl ^ 2048u));
    SIG(25);
    SERVE(cp_rd, 16);
    stageN(IC(14), IC(131072), abA + (dl ^ 2048u));
    stageN(IC(8), IC(139264), abH + dl);
    SIG(26);
    stageN(IC(10), IC(98304), abH + dl);
    stageN(IC(12), IC(106496), abH + dl);
    SIG(27);
    SERVE(cp_rd, 17);
    stageN(IC(14), IC(49152), abH + dl);
    stageN(IC(6), IC(57344), abH + (dl ^ 2048u));
    SIG(28);
    stageN(IC(8), IC(16384), abH + (dl ^ 2048u));
    stageN(IC(10), IC(24576), abH + (dl ^ 2048u));
    SIG(29);
    SERVE(cp_rd, 18);
    stageN(IC(12), IC(114688), abH + (dl ^ 2048u));
    stageN(IC(14), IC(122880), abH + (dl ^ 2048u));
    SIG(64);
    WAIT_V(0);
  } else {
    const int sw = wid >> 1 & 1;
    __builtin_amdgcn_s_setprio(2);
    unsigned tag;
    int idx = 0;
    auto TAG_PUT = [&]() {
      unsigned es;
      asm volatile("s_waitcnt vmcnt(0)\n\tv_readfirstlane_b32 %0, %1" : "=s"(es) : "v"(E) : "memory");
      asm volatile("ds_write2_b32 %0, %1, %2 offset1:1" ::"v"(tg_rd), "v"(1u), "v"(es * 0x9E3779B1u + 0x7F4A7C15u) : "memory");
    };
    auto ROUTER = [&]() {
      const unsigned ra = sb + ((h * 4 + cp1) * 16 + fr) * 128 + ((fq ^ (fr & 7)) << 4), rb = sb + RING + L_RF + (fr & 1) * 1152 + fq * 16;
      f32x4 d;
      SERVE(rf_rd, 1);
#define RSTEP(s, o) do { bf16x8 a0, a1, b0, b1; SERVE(la_rd, (s) + 1);                                                                       \
      asm volatile("ds_read_b128 %0, %4\n\tds_read_b128 %1, %5\n\tds_read_b128 %2, %6\n\tds_read_b128 %3, %6 offset:64\n\ts_waitcnt lgkmcnt(0)"  \
                   : "=&v"(a0), "=&v"(a1), "=&v"(b0), "=&v"(b1) : "v"(ra + (o)), "v"((ra ^ 64u) + (o)), "v"(rb + (((s) + rot1) & 7u) * 128u) : "memory"); \
      sig<(s) == 7 ? 64 : (s) + 1>(lbase + RING + L_LF + 768, tab); if ((s) == 0) mm0(d, a0, b0); else mm(d, a0, b0); mm(d, a1, b1); } while (0)
      RSTEP(0, 0u);
      RSTEP(1, 16384u);
      RSTEP(2, 32768u);
      RSTEP(3, 49152u);
      RSTEP(4, 65536u);
      RSTEP(5, 0u);
      RSTEP(6, 16384u);
      RSTEP(7, 32768u);
      asm volatile("s_nop 7" : "+v"(d));
      const int rid = si * 8 + h * 4 + cp1;
      unsigned key = 0u;
      float g[4];
#pragma unroll
      for (int i = 0; i < 4; ++i) {
        const float r = rbf(d[i]);
        g[i] = sigmoidf_(r);
        const unsigned u = __float_as_uint(r);
        const unsigned uo = (u & 0x80000000u) ? ~u : (u | 0x80000000u);
        key = max(key, (uo & 0xffff0000u) | (unsigned)(4095 - (rid * 16 + fq * 4 + i)));
      }
      key = dpp<0xa0>(key);
      const auto k1 = __builtin_amdgcn_permlane16_swap(key, key, false, false);
      key = max(k1[0], k1[1]);
      const auto k2 = __builtin_amdgcn_permlane32_swap(key, key, false, false);
      key = max(k2[0], k2[1]);
      if ((fr | 2) == 3) gst128_sc1((unsigned)(C_KEY + rid * 128 + (fr >> 1) * 64 + fq * 16), u32x4{pk2(g[0], g[1]), pk2(g[2], g[3]), key, tag}, wc);
    };
    auto POLL_KEYS = [&]() {
      u32x2 k0, k1;
      u32x4 gv;
#pragma clang loop unroll(disable)
      for (int it = 0; it < SPIN_MAX; ++it) {
        asm volatile("global_load_dwordx2 %0, %3, %6 sc1\n\tglobal_load_dwordx2 %1, %4, %6 sc1\n\tglobal_load_dwordx4 %2, %5, %6 sc1\n\ts_waitcnt vmcnt(0)"
                     : "=&v"(k0), "=&v"(k1), "=&v"(gv) : "v"((unsigned)(C_KEY + lane * 128 + 8)), "v"((unsigned)(C_KEY + lane * 128 + 8200)), "v"(g_o), "s"(wc) : "memory");
        if (__all(k0.y == tag && k1.y == tag && gv.w == tag)) break;
        __builtin_amdgcn_s_sleep(PSLEEP);
      }
      const unsigned best = wave_max(max(k0.x, k1.x));
      G_PUT(gv, best);
      if (b == 8 && lane < 8) gst128_sc1((unsigned)(C_BEST + lane * 16), u32x4{best, tag, best, tag}, wc);
    };
    auto IDX_READ = [&]() { idx = (4095 - (int)(__builtin_amdgcn_readfirstlane(lflag[3]) & 0xfffu)) & (S_ - 1); };
    const int on = 111 - qi;
    const bool ffn = on >= 0;
    const int fo = ffn ? qi >> 2 : 0, fp = qi & 3;
    u32x4 fg[16];
    const u16* wgu = sw ? w_up : w_gate;
    const unsigned vg = (unsigned)((fp * 128 + r8 * 8) * (F_ * 2) + fo * 128 + c8 * 16);
#define LOAD4(d, v, step, base, on)                                                                                        \
  asm volatile("s_cmp_lt_i32 %9, 0\n\ts_cbranch_scc1 .Lq%=\n\tglobal_load_dwordx4 %0, %4, %8\n\tglobal_load_dwordx4 %1, %5, %8\n\t" \
               "global_load_dwordx4 %2, %6, %8\n\tglobal_load_dwordx4 %3, %7, %8\n.Lq%=:"                                   \
               : "+v"((d)[0]), "+v"((d)[1]), "+v"((d)[2]), "+v"((d)[3])                                                    \
               : "v"(v), "v"((v) + (step)), "v"((v) + 2u * (step)), "v"((v) + 3u * (step)), "s"(base), "s"(on) : "scc", "memory")
#define ANY8(w) asm volatile("" : "=v"((w)[0]), "=v"((w)[1]), "=v"((w)[2]), "=v"((w)[3]), "=v"((w)[4]), "=v"((w)[5]), "=v"((w)[6]), "=v"((w)[7]))
    ANY8(fg);
    ANY8(fg + 8);
#define FFN_LOAD(p) LOAD4(fg + (p) * 4, vg + (((p) & 1) * 4u + ((p) >> 1) * 64u) * (F_ * 2), (unsigned)(F_ * 2), wgu, on)
    auto turn = [&](u32x4* w) {
      u32x4 t[8];
#pragma unroll
      for (int d = 0; d < 4; ++d)
#pragma unroll
        for (int j = 0; j < 4; ++j) {
          t[2 * d][j] = __builtin_amdgcn_perm(w[2 * j + 1][d], w[2 * j][d], 0x05040100u);
          t[2 * d + 1][j] = __builtin_amdgcn_perm(w[2 * j + 1][d], w[2 * j][d], 0x07060302u);
        }
#pragma unroll
      for (int i = 0; i < 8; ++i) w[i] = t[i];
      asm volatile("" : "+v"(w[0]), "+v"(w[1]), "+v"(w[2]), "+v"(w[3]), "+v"(w[4]), "+v"(w[5]), "+v"(w[6]), "+v"(w[7]));
    };
#define PIN8(w) asm volatile("s_waitcnt vmcnt(0)" : "+v"((w)[0]), "+v"((w)[1]), "+v"((w)[2]), "+v"((w)[3]), "+v"((w)[4]), "+v"((w)[5]), "+v"((w)[6]), "+v"((w)[7])::"memory")
    unsigned pa_o = (unsigned)(C_PA + qi * 1024 + (sw * 64 + c8 * 8 + fq * 2) * 8);
    auto FFN_RUN = [&]() {
      bf16x8 xa_[2];
      asm volatile("global_load_dwordx4 %0, %2, %3\n\tglobal_load_dwordx4 %1, %2, %3 offset:128\n\ts_waitcnt vmcnt(0)"
                   : "=&v"(xa_[0]), "=&v"(xa_[1]) : "v"((unsigned)(idx * (D_ * 2) + (fp * 128 + (fq * 2 + (fr & 1)) * 8) * 2)), "s"(x) : "memory");
      f32x4 d[8];
#pragma unroll
      for (int j = 0; j < 8; ++j) mm0(d[j], xa_[0], __builtin_bit_cast(bf16x8, fg[j]));
#pragma unroll
      for (int j = 0; j < 8; ++j) mm(d[j], xa_[1], __builtin_bit_cast(bf16x8, fg[8 + j]));
      asm volatile("s_nop 7" : "+v"(d[0]), "+v"(d[1]), "+v"(d[2]), "+v"(d[3]), "+v"(d[4]), "+v"(d[5]), "+v"(d[6]), "+v"(d[7]));
      float dp[8];
#pragma unroll
      for (int j = 0; j < 8; ++j) dp[j] = fr < 8 ? d[j][0] : d[j][1];
      const float s0 = dpp_add<0x128>(lane < 32 ? (fq & 1 ? dp[2] : dp[0]) : (fq & 1 ? dp[6] : dp[4]));
      const float s1 = dpp_add<0x128>(lane < 32 ? (fq & 1 ? dp[3] : dp[1]) : (fq & 1 ? dp[7] : dp[5]));
      const u32x4 o = {__float_as_uint(s0), tag, __float_as_uint(s1), tag};
      SERVE(gt_rd, 1);
      if ((lane & 8) == 0)
        gst128_sc1(pa_o, o, wc);
    };
    u32x4 fd[16];
    const unsigned vd = (unsigned)((fo * 64 + fq * 16) * (D_ * 2) + fp * 256 + fr * 16);
    ANY8(fd);
    ANY8(fd + 8);
#define DOWN_LOAD(p) LOAD4(fd + (p) * 4, vd + (p) * 4096u, 1024u, w_down, on | (sw - 1))
    unsigned pb_o = (unsigned)(C_PB + fo * 4096 + (fp * 128 + fr * 8 + fq * 2) * 8), pvo = (unsigned)(C_PA + fo * 4096 + lane * 16);
    auto PREP_F = [&]() { PIN8(fg); PIN8(fg + 8); turn(fg); turn(fg + 8); asm volatile("" : "+v"(pa_o)); };
    auto PREP_D = [&]() { PIN8(fd); PIN8(fd + 8); turn(fd); turn(fd + 8); asm volatile("" : "+v"(pb_o), "+v"(pvo)); };
    auto SEEN = [&]() { if (lane < 8) gst128_sc1((unsigned)(C_SEEN + (qi - 112) * 128 + lane * 16), u32x4{tag, tag, tag, tag}, wc); };
    auto RED_AB = [&]() {
      u32x4 pv[4];
#pragma clang loop unroll(disable)
      for (int it = 0; it < SPIN_MAX; ++it) {
        asm volatile("global_load_dwordx4 %0, %4, %5 sc1\n\tglobal_load_dwordx4 %1, %4, %5 offset:1024 sc1\n\t"
                     "global_load_dwordx4 %2, %4, %5 offset:2048 sc1\n\tglobal_load_dwordx4 %3, %4, %5 offset:3072 sc1\n\ts_waitcnt vmcnt(0)"
                     : "=&v"(pv[0]), "=&v"(pv[1]), "=&v"(pv[2]), "=&v"(pv[3]) : "v"(pvo), "s"(wc) : "memory");
        bool ok = true;
#pragma unroll
        for (int k = 0; k < 4; ++k) ok = ok && pv[k].y == tag && pv[k].w == tag;
        if (__all(ok)) break;
        __builtin_amdgcn_s_sleep(PSLEEP);
      }
      float s0 = 0.f, s1 = 0.f;
#pragma unroll
      for (int k = 0; k < 4; ++k) { s0 += __uint_as_float(pv[k].x); s1 += __uint_as_float(pv[k].z); }
      const auto r = __builtin_amdgcn_permlane32_swap(__float_as_uint(s0), __float_as_uint(s1), false, false);
      const float gt = rbf(__uint_as_float(r[0]));
      const unsigned hb = f2bf(rbf(gt * sigmoidf_(gt)) * rbf(__uint_as_float(r[1])));
      asm volatile("ds_write_b16 %0, %1" ::"v"(sb + RING + L_H + (lane & 31) * 4 + (lane >> 5) * 2), "v"(hb) : "memory");
    };
    auto RED_C = [&]() {
      bf16x8 hp[2];
      asm volatile("ds_read_b128 %0, %2\n\tds_read_b128 %1, %2 offset:16\n\ts_waitcnt lgkmcnt(0)" : "=&v"(hp[0]), "=&v"(hp[1]) : "v"(sb + RING + L_H + fq * 32) : "memory");
      f32x4 d[8];
#pragma unroll
      for (int j = 0; j < 8; ++j) mm0(d[j], hp[0], __builtin_bit_cast(bf16x8, fd[j]));
#pragma unroll
      for (int j = 0; j < 8; ++j) mm(d[j], hp[1], __builtin_bit_cast(bf16x8, fd[8 + j]));
      asm volatile("s_nop 7" : "+v"(d[0]), "+v"(d[1]), "+v"(d[2]), "+v"(d[3]), "+v"(d[4]), "+v"(d[5]), "+v"(d[6]), "+v"(d[7]));
      const float dp[8] = {d[0][0], d[1][0], d[2][0], d[3][0], d[4][0], d[5][0], d[6][0], d[7][0]};
      const float v0 = lane < 32 ? (fq & 1 ? dp[2] : dp[0]) : (fq & 1 ? dp[6] : dp[4]);
      const float v1 = lane < 32 ? (fq & 1 ? dp[3] : dp[1]) : (fq & 1 ? dp[7] : dp[5]);
      gst128_sc1(pb_o, u32x4{__float_as_uint(v0), tag, __float_as_uint(v1), tag}, wc);
    };
    if (sw) TAG_PUT();
    if (isp) {
      TAG_WAIT();
      if (sw == 0) ROUTER();
    SERVE(lb_rd, 16);
      DOWN_LOAD(0);
      DOWN_LOAD(1);
      DOWN_LOAD(2);
      DOWN_LOAD(3);
      if (sw) PREP_D();
    SERVE(cp_rd, 14);
    UNTIL(855u, 17991u);
      if (ffn && sw) RED_AB();
      if (ffn && sw) RED_C();
    }
    if (isq) {
      TAG_WAIT();
      if (!ffn && sw) SEEN();
      if (sw == 0) sig<64>(lbase + RING + L_LF + 768, tab);
    SERVE(la_rd, 11);
      FFN_LOAD(0);
      FFN_LOAD(1);
      FFN_LOAD(2);
      FFN_LOAD(3);
    SERVE(own_rd, 5);
    UNTIL(574u, 11890u);
      if (sw == 0) POLL_KEYS();
      PREP_F();
      SERVE(best_rd, 1);
      IDX_READ();
      if (ffn) FFN_RUN();
    }
  }
  } else {
    const int wm = wid >> 1, wn = wid & 1;
    unsigned fl;
    unsigned flagged = (unsigned)(ej & 1) * 64u;
    const unsigned flag_o = (unsigned)(C_FL2 + (((h * 4 + (cp1 & 2) + wn) * 2 + kh) * 32 + ((si & 7) * 2 + (cp1 & 1)) * 2 + wm) * FSTR + lane * 4);
    unsigned tagv;
#define TRYFLAG(Y) tryflag<Y>(flagged, flag_o, tagv, wc)
#define FLAG(Y) do { WAIT_V(Y); TRYFLAG(Y); } while (0)
    const unsigned own_wr = sb + RING + L_FLAG + 32 + wid * 4;
#define OWN_SIG(n) do { unsigned t_; asm volatile("v_mov_b32 %0, %2\n\tds_write_b32 %1, %0" : "=&v"(t_) : "v"(own_wr), "i"(n) : "memory"); } while (0)
#define SPIN(ADDR, N) spin<N>(ADDR, bud)
#define SPINF(ADDR, N, Y) spinf<N, Y>(ADDR, bud, flagged, flag_o, tagv, wc)
    const unsigned wa0 = sb + (wid * 32 + fr) * 128 + ((fq ^ (fr & 7)) << 4), wa1 = wa0 ^ 64u;
    const unsigned xa = sb + lane * 16;
    const unsigned wa0_0 = wa0 + 0u;
    const unsigned wa0_1 = wa0 + 65536u;
    const unsigned wa0_2 = wa0 + 131072u;
    const unsigned wa1_0 = wa1 + 0u;
    const unsigned wa1_1 = wa1 + 65536u;
    const unsigned wa1_2 = wa1 + 131072u;
    const unsigned xa_0 = xa + 0u;
    const unsigned xa_1 = xa + 65536u;
    const unsigned xa_2 = xa + 131072u;
    bf16x8 wr[16][4], fb[9];
    if (isq) {
      SIG(8);
      OWN_SIG(1);
    }
    if (isp) {
      const int row = wm * 64 + fr, xr = wn * 32 + fr;
      const unsigned pa0 = sb + row * 128 + ((fq ^ (row & 7)) << 4) + wn * 4096, pa1 = pa0 ^ 64u;
      const unsigned pb0 = sb + row * 128 + ((fq ^ (row & 7)) << 4) + (1 - wn) * 4096, pb1 = pb0 ^ 64u;
      const unsigned px0 = sb + xr * 128 + ((fq ^ (xr & 7)) << 4), px1 = px0 ^ 64u;
      f32x4 ac1[4][4];
      bf16x8 hw[2][4], hx[2][4];
      unsigned go[2];
      go[0] = (unsigned)((((h * 4 + (cp1 & 2) + wn) * 2 + (cp1 & 1)) * 16 + si) * A_B + wm * 8192 + wn * 4096 + (fq & 1) * 1024 + (fq >> 1) * 256 + fr * 16);
      go[1] = go[0] ^ 4096u;
#define P1_SETUP() asm volatile("" : "+v"(go[0]), "+v"(go[1]))
      unsigned pv[4];
#define PUB0(n, mi) asm volatile("v_cvt_pk_bf16_f32 %0, %2, %3\n\tv_cvt_pk_bf16_f32 %1, %4, %5" : "=&v"(pv[0]), "=&v"(pv[2])             \
                                 : "v"(ac1[n][mi][0]), "v"(ac1[n][mi][1]), "v"(ac1[(n) + 1][mi][0]), "v"(ac1[(n) + 1][mi][1]))
#define PUB1(n, mi) asm volatile("v_cvt_pk_bf16_f32 %0, %2, %3\n\tv_cvt_pk_bf16_f32 %1, %4, %5" : "=&v"(pv[1]), "=&v"(pv[3])             \
                                 : "v"(ac1[n][mi][2]), "v"(ac1[n][mi][3]), "v"(ac1[(n) + 1][mi][2]), "v"(ac1[(n) + 1][mi][3]))
#define PUB2() asm volatile("s_nop 1\n\tv_permlane16_swap_b32 %0, %2\n\tv_permlane16_swap_b32 %1, %3" : "+v"(pv[0]), "+v"(pv[1]), "+v"(pv[2]), "+v"(pv[3]))
#define PUB3(n, mi) asm volatile("global_store_dwordx4 %0, %1, %2 offset:%3 sc1\n\ts_nop 1" ::"v"(go[(mi) >> 1]), "v"(u32x4{pv[0], pv[1], pv[2], pv[3]}), "s"(ab), "i"(((mi) & 1) * 512 + (n) * 1024) : "memory")
      const unsigned pa0_0 = pa0 + 0u;
      const unsigned pa0_1 = pa0 + 65536u;
      const unsigned pa1_0 = pa1 + 0u;
      const unsigned pa1_1 = pa1 + 65536u;
      const unsigned pb0_0 = pb0 + 0u;
      const unsigned pb0_1 = pb0 + 65536u;
      const unsigned pb1_0 = pb1 + 0u;
      const unsigned pb1_1 = pb1 + 65536u;
      const unsigned px0_1 = px0 + 65536u;
      const unsigned px0_2 = px0 + 131072u;
      const unsigned px1_1 = px1 + 65536u;
      const unsigned px1_2 = px1 + 131072u;
      SPIN(lf_rd, 1); SB();
      lds_rd128<16384>(hx[0][0], px0_1); SB();
      lds_rd128<0>(hw[0][0], pa0_0); SB();
      lds_rd128<2048>(hw[0][1], pa0_0); SB();
      lds_rd128<0>(hw[0][2], pb0_0); SB();
      lds_rd128<2048>(hw[0][3], pb0_0); SB();
      lds_rd128<18432>(hx[0][1], px0_1); SB();
      lds_rd128<24576>(hx[0][2], px0_1); SB();
      lds_rd128<26624>(hx[0][3], px0_1); SB();
      mm0w<6>(ac1[0][0], hw[0][0], hx[0][0]); SB();
      lds_rd128<16384>(hx[1][0], px1_1); SB();
      mm0w<6>(ac1[0][1], hw[0][1], hx[0][0]); SB();
      lds_rd128<0>(hw[1][0], pa1_0); SB();
      mm0w<6>(ac1[0][2], hw[0][2], hx[0][0]); SB();
      lds_rd128<2048>(hw[1][1], pa1_0); SB();
      mm0w<6>(ac1[0][3], hw[0][3], hx[0][0]); SB();
      lds_rd128<0>(hw[1][2], pb1_0); SB();
      mm0w<6>(ac1[1][3], hw[0][3], hx[0][1]); SB();
      lds_rd128<2048>(hw[1][3], pb1_0); SB();
      mm0(ac1[1][2], hw[0][2], hx[0][1]); SB();
      lds_rd128<18432>(hx[1][1], px1_1); SB();
      mm0(ac1[1][1], hw[0][1], hx[0][1]); SB();
      lds_rd128<24576>(hx[1][2], px1_1); SB();
      mm0(ac1[1][0], hw[0][0], hx[0][1]); SB();
      lds_rd128<26624>(hx[1][3], px1_1); SB();
      mm0w<8>(ac1[2][0], hw[0][0], hx[0][2]); SB();
      mm0(ac1[2][1], hw[0][1], hx[0][2]); SB();
      mm0(ac1[2][2], hw[0][2], hx[0][2]); SB();
      mm0(ac1[2][3], hw[0][3], hx[0][2]); SB();
      mm0(ac1[3][3], hw[0][3], hx[0][3]); SB();
      mm0(ac1[3][2], hw[0][2], hx[0][3]); SB();
      asm volatile("s_waitcnt lgkmcnt(0)" : "+v"(hx[1][0]), "+v"(hw[1][0]), "+v"(hw[1][1]), "+v"(hw[1][2]), "+v"(hw[1][3]), "+v"(hx[1][1]), "+v"(hx[1][2]), "+v"(hx[1][3])::"memory"); SB();
      SIG(1); SB();
      mm0(ac1[3][1], hw[0][1], hx[0][3]); SB();
      mm0(ac1[3][0], hw[0][0], hx[0][3]); SB();
      mm(ac1[0][0], hw[1][0], hx[1][0]); SB();
      lds_rd32(fl, lf_rd); SB();
      mm(ac1[0][1], hw[1][1], hx[1][0]); SB();
      mm(ac1[0][2], hw[1][2], hx[1][0]); SB();
      mm(ac1[0][3], hw[1][3], hx[1][0]); SB();
      mm(ac1[1][3], hw[1][3], hx[1][1]); SB();
      mm(ac1[1][2], hw[1][2], hx[1][1]); SB();
      mmchkw<2, 0>(ac1[1][1], hw[1][1], hx[1][1], fl, lf_rd, bud); SB();
      lds_rd128<32768>(hx[0][0], px0_1); SB();
      mm(ac1[1][0], hw[1][0], hx[1][1]); SB();
      lds_rd128<16384>(hw[0][0], pa0_0); SB();
      mm(ac1[2][0], hw[1][0], hx[1][2]); SB();
      lds_rd128<18432>(hw[0][1], pa0_0); SB();
      mm(ac1[2][1], hw[1][1], hx[1][2]); SB();
      lds_rd128<16384>(hw[0][2], pb0_0); SB();
      mm(ac1[2][2], hw[1][2], hx[1][2]); SB();
      lds_rd128<18432>(hw[0][3], pb0_0); SB();
      mm(ac1[2][3], hw[1][3], hx[1][2]); SB();
      lds_rd128<34816>(hx[0][1], px0_1); SB();
      mm(ac1[3][3], hw[1][3], hx[1][3]); SB();
      lds_rd128<40960>(hx[0][2], px0_1); SB();
      mm(ac1[3][2], hw[1][2], hx[1][3]); SB();
      lds_rd128<43008>(hx[0][3], px0_1); SB();
      mm(ac1[3][1], hw[1][1], hx[1][3]); SB();
      mm(ac1[3][0], hw[1][0], hx[1][3]); SB();
      mmw<5>(ac1[0][0], hw[0][0], hx[0][0]); SB();
      lds_rd128<32768>(hx[1][0], px1_1); SB();
      mm(ac1[0][1], hw[0][1], hx[0][0]); SB();
      lds_rd128<16384>(hw[1][0], pa1_0); SB();
      mmw<5>(ac1[0][2], hw[0][2], hx[0][0]); SB();
      lds_rd128<18432>(hw[1][1], pa1_0); SB();
      mm(ac1[0][3], hw[0][3], hx[0][0]); SB();
      lds_rd128<16384>(hw[1][2], pb1_0); SB();
      mmw<5>(ac1[1][3], hw[0][3], hx[0][1]); SB();
      lds_rd128<18432>(hw[1][3], pb1_0); SB();
      mm(ac1[1][2], hw[0][2], hx[0][1]); SB();
      lds_rd128<34816>(hx[1][1], px1_1); SB();
      mm(ac1[1][1], hw[0][1], hx[0][1]); SB();
      lds_rd128<40960>(hx[1][2], px1_1); SB();
      mm(ac1[1][0], hw[0][0], hx[0][1]); SB();
      lds_rd128<43008>(hx[1][3], px1_1); SB();
      mm(ac1[2][0], hw[0][0], hx[0][2]); SB();
      mm(ac1[2][1], hw[0][1], hx[0][2]); SB();
      mm(ac1[2][2], hw[0][2], hx[0][2]); SB();
      mm(ac1[2][3], hw[0][3], hx[0][2]); SB();
      mmw<7>(ac1[3][3], hw[0][3], hx[0][3]); SB();
      mm(ac1[3][2], hw[0][2], hx[0][3]); SB();
      asm volatile("s_waitcnt lgkmcnt(0)" : "+v"(hw[1][0]), "+v"(hw[1][1]), "+v"(hw[1][2]), "+v"(hw[1][3]), "+v"(hx[1][1]), "+v"(hx[1][2]), "+v"(hx[1][3])::"memory"); SB();
      SIG(2); SB();
      mm(ac1[3][1], hw[0][1], hx[0][3]); SB();
      mm(ac1[3][0], hw[0][0], hx[0][3]); SB();
      mm(ac1[0][0], hw[1][0], hx[1][0]); SB();
      lds_rd32(fl, lf_rd); SB();
      mm(ac1[0][1], hw[1][1], hx[1][0]); SB();
      mm(ac1[0][2], hw[1][2], hx[1][0]); SB();
      mm(ac1[0][3], hw[1][3], hx[1][0]); SB();
      mm(ac1[1][3], hw[1][3], hx[1][1]); SB();
      mm(ac1[1][2], hw[1][2], hx[1][1]); SB();
      mmchkw<3, 0>(ac1[1][1], hw[1][1], hx[1][1], fl, lf_rd, bud); SB();
      lds_rd128<49152>(hx[0][0], px0_1); SB();
      mm(ac1[1][0], hw[1][0], hx[1][1]); SB();
      lds_rd128<32768>(hw[0][0], pa0_0); SB();
      mm(ac1[2][0], hw[1][0], hx[1][2]); SB();
      lds_rd128<34816>(hw[0][1], pa0_0); SB();
      mm(ac1[2][1], hw[1][1], hx[1][2]); SB();
      lds_rd128<32768>(hw[0][2], pb0_0); SB();
      mm(ac1[2][2], hw[1][2], hx[1][2]); SB();
      lds_rd128<34816>(hw[0][3], pb0_0); SB();
      mm(ac1[2][3], hw[1][3], hx[1][2]); SB();
      lds_rd128<51200>(hx[0][1], px0_1); SB();
      mm(ac1[3][3], hw[1][3], hx[1][3]); SB();
      lds_rd128<57344>(hx[0][2], px0_1); SB();
      mm(ac1[3][2], hw[1][2], hx[1][3]); SB();
      lds_rd128<59392>(hx[0][3], px0_1); SB();
      mm(ac1[3][1], hw[1][1], hx[1][3]); SB();
      mm(ac1[3][0], hw[1][0], hx[1][3]); SB();
      mmw<5>(ac1[0][0], hw[0][0], hx[0][0]); SB();
      lds_rd128<49152>(hx[1][0], px1_1); SB();
      mm(ac1[0][1], hw[0][1], hx[0][0]); SB();
      lds_rd128<32768>(hw[1][0], pa1_0); SB();
      mmw<5>(ac1[0][2], hw[0][2], hx[0][0]); SB();
      lds_rd128<34816>(hw[1][1], pa1_0); SB();
      mm(ac1[0][3], hw[0][3], hx[0][0]); SB();
      lds_rd128<32768>(hw[1][2], pb1_0); SB();
      mmw<5>(ac1[1][3], hw[0][3], hx[0][1]); SB();
      lds_rd128<34816>(hw[1][3], pb1_0); SB();
      mm(ac1[1][2], hw[0][2], hx[0][1]); SB();
      lds_rd128<51200>(hx[1][1], px1_1); SB();
      mm(ac1[1][1], hw[0][1], hx[0][1]); SB();
      lds_rd128<57344>(hx[1][2], px1_1); SB();
      mm(ac1[1][0], hw[0][0], hx[0][1]); SB();
      lds_rd128<59392>(hx[1][3], px1_1); SB();
      mm(ac1[2][0], hw[0][0], hx[0][2]); SB();
      mm(ac1[2][1], hw[0][1], hx[0][2]); SB();
      mm(ac1[2][2], hw[0][2], hx[0][2]); SB();
      mm(ac1[2][3], hw[0][3], hx[0][2]); SB();
      mmw<7>(ac1[3][3], hw[0][3], hx[0][3]); SB();
      mm(ac1[3][2], hw[0][2], hx[0][3]); SB();
      asm volatile("s_waitcnt lgkmcnt(0)" : "+v"(hw[1][0]), "+v"(hw[1][1]), "+v"(hw[1][2]), "+v"(hw[1][3]), "+v"(hx[1][1]), "+v"(hx[1][2]), "+v"(hx[1][3])::"memory"); SB();
      SIG(3); SB();
      mm(ac1[3][1], hw[0][1], hx[0][3]); SB();
      mm(ac1[3][0], hw[0][0], hx[0][3]); SB();
      mm(ac1[0][0], hw[1][0], hx[1][0]); SB();
      lds_rd32(fl, lf_rd); SB();
      mm(ac1[0][1], hw[1][1], hx[1][0]); SB();
      mm(ac1[0][2], hw[1][2], hx[1][0]); SB();
      mm(ac1[0][3], hw[1][3], hx[1][0]); SB();
      mm(ac1[1][3], hw[1][3], hx[1][1]); SB();
      mm(ac1[1][2], hw[1][2], hx[1][1]); SB();
      mmchkw<4, 0>(ac1[1][1], hw[1][1], hx[1][1], fl, lf_rd, bud); SB();
      lds_rd128<0>(hx[0][0], px0_2); SB();
      mm(ac1[1][0], hw[1][0], hx[1][1]); SB();
      lds_rd128<49152>(hw[0][0], pa0_0); SB();
      mm(ac1[2][0], hw[1][0], hx[1][2]); SB();
      lds_rd128<51200>(hw[0][1], pa0_0); SB();
      mm(ac1[2][1], hw[1][1], hx[1][2]); SB();
      lds_rd128<49152>(hw[0][2], pb0_0); SB();
      mm(ac1[2][2], hw[1][2], hx[1][2]); SB();
      lds_rd128<51200>(hw[0][3], pb0_0); SB();
      mm(ac1[2][3], hw[1][3], hx[1][2]); SB();
      lds_rd128<2048>(hx[0][1], px0_2); SB();
      mm(ac1[3][3], hw[1][3], hx[1][3]); SB();
      lds_rd128<8192>(hx[0][2], px0_2); SB();
      mm(ac1[3][2], hw[1][2], hx[1][3]); SB();
      lds_rd128<10240>(hx[0][3], px0_2); SB();
      mm(ac1[3][1], hw[1][1], hx[1][3]); SB();
      mm(ac1[3][0], hw[1][0], hx[1][3]); SB();
      mmw<5>(ac1[0][0], hw[0][0], hx[0][0]); SB();
      lds_rd128<0>(hx[1][0], px1_2); SB();
      mm(ac1[0][1], hw[0][1], hx[0][0]); SB();
      lds_rd128<49152>(hw[1][0], pa1_0); SB();
      mmw<5>(ac1[0][2], hw[0][2], hx[0][0]); SB();
      lds_rd128<51200>(hw[1][1], pa1_0); SB();
      mm(ac1[0][3], hw[0][3], hx[0][0]); SB();
      lds_rd128<49152>(hw[1][2], pb1_0); SB();
      mmw<5>(ac1[1][3], hw[0][3], hx[0][1]); SB();
      lds_rd128<51200>(hw[1][3], pb1_0); SB();
      mm(ac1[1][2], hw[0][2], hx[0][1]); SB();
      lds_rd128<2048>(hx[1][1], px1_2); SB();
      mm(ac1[1][1], hw[0][1], hx[0][1]); SB();
      lds_rd128<8192>(hx[1][2], px1_2); SB();
      mm(ac1[1][0], hw[0][0], hx[0][1]); SB();
      lds_rd128<10240>(hx[1][3], px1_2); SB();
      mm(ac1[2][0], hw[0][0], hx[0][2]); SB();
      mm(ac1[2][1], hw[0][1], hx[0][2]); SB();
      mm(ac1[2][2], hw[0][2], hx[0][2]); SB();
      mm(ac1[2][3], hw[0][3], hx[0][2]); SB();
      mmw<7>(ac1[3][3], hw[0][3], hx[0][3]); SB();
      mm(ac1[3][2], hw[0][2], hx[0][3]); SB();
      asm volatile("s_waitcnt lgkmcnt(0)" : "+v"(hw[1][0]), "+v"(hw[1][1]), "+v"(hw[1][2]), "+v"(hw[1][3]), "+v"(hx[1][1]), "+v"(hx[1][2]), "+v"(hx[1][3])::"memory"); SB();
      SIG(4); SB();
      mm(ac1[3][1], hw[0][1], hx[0][3]); SB();
      mm(ac1[3][0], hw[0][0], hx[0][3]); SB();
      mm(ac1[0][0], hw[1][0], hx[1][0]); SB();
      lds_rd32(fl, lf_rd); SB();
      mm(ac1[0][1], hw[1][1], hx[1][0]); SB();
      mm(ac1[0][2], hw[1][2], hx[1][0]); SB();
      mm(ac1[0][3], hw[1][3], hx[1][0]); SB();
      mm(ac1[1][3], hw[1][3], hx[1][1]); SB();
      mm(ac1[1][2], hw[1][2], hx[1][1]); SB();
      mmchkw<5, 0>(ac1[1][1], hw[1][1], hx[1][1], fl, lf_rd, bud); SB();
      lds_rd128<16384>(hx[0][0], px0_1); SB();
      mm(ac1[1][0], hw[1][0], hx[1][1]); SB();
      lds_rd128<0>(hw[0][0], pa0_1); SB();
      mm(ac1[2][0], hw[1][0], hx[1][2]); SB();
      lds_rd128<2048>(hw[0][1], pa0_1); SB();
      mm(ac1[2][1], hw[1][1], hx[1][2]); SB();
      lds_rd128<0>(hw[0][2], pb0_1); SB();
      mm(ac1[2][2], hw[1][2], hx[1][2]); SB();
      lds_rd128<2048>(hw[0][3], pb0_1); SB();
      mm(ac1[2][3], hw[1][3], hx[1][2]); SB();
      lds_rd128<18432>(hx[0][1], px0_1); SB();
      mm(ac1[3][3], hw[1][3], hx[1][3]); SB();
      lds_rd128<24576>(hx[0][2], px0_1); SB();
      mm(ac1[3][2], hw[1][2], hx[1][3]); SB();
      lds_rd128<26624>(hx[0][3], px0_1); SB();
      mm(ac1[3][1], hw[1][1], hx[1][3]); SB();
      mm(ac1[3][0], hw[1][0], hx[1][3]); SB();
      mmw<5>(ac1[0][0], hw[0][0], hx[0][0]); SB();
      lds_rd128<16384>(hx[1][0], px1_1); SB();
      mm(ac1[0][1], hw[0][1], hx[0][0]); SB();
      lds_rd128<0>(hw[1][0], pa1_1); SB();
      mmw<5>(ac1[0][2], hw[0][2], hx[0][0]); SB();
      lds_rd128<2048>(hw[1][1], pa1_1); SB();
      mm(ac1[0][3], hw[0][3], hx[0][0]); SB();
      lds_rd128<0>(hw[1][2], pb1_1); SB();
      mmw<5>(ac1[1][3], hw[0][3], hx[0][1]); SB();
      lds_rd128<2048>(hw[1][3], pb1_1); SB();
      mm(ac1[1][2], hw[0][2], hx[0][1]); SB();
      lds_rd128<18432>(hx[1][1], px1_1); SB();
      mm(ac1[1][1], hw[0][1], hx[0][1]); SB();
      lds_rd128<24576>(hx[1][2], px1_1); SB();
      mm(ac1[1][0], hw[0][0], hx[0][1]); SB();
      lds_rd128<26624>(hx[1][3], px1_1); SB();
      mm(ac1[2][0], hw[0][0], hx[0][2]); SB();
      mm(ac1[2][1], hw[0][1], hx[0][2]); SB();
      mm(ac1[2][2], hw[0][2], hx[0][2]); SB();
      mm(ac1[2][3], hw[0][3], hx[0][2]); SB();
      mmw<7>(ac1[3][3], hw[0][3], hx[0][3]); SB();
      mm(ac1[3][2], hw[0][2], hx[0][3]); SB();
      asm volatile("s_waitcnt lgkmcnt(0)" : "+v"(hw[1][0]), "+v"(hw[1][1]), "+v"(hw[1][2]), "+v"(hw[1][3]), "+v"(hx[1][1]), "+v"(hx[1][2]), "+v"(hx[1][3])::"memory"); SB();
      SIG(5); SB();
      mm(ac1[3][1], hw[0][1], hx[0][3]); SB();
      mm(ac1[3][0], hw[0][0], hx[0][3]); SB();
      P1_SETUP(); SB();
      mm(ac1[0][0], hw[1][0], hx[1][0]); SB();
      lds_rd32(fl, lf_rd); SB();
      mm(ac1[0][1], hw[1][1], hx[1][0]); SB();
      mm(ac1[0][2], hw[1][2], hx[1][0]); SB();
      mm(ac1[0][3], hw[1][3], hx[1][0]); SB();
      mm(ac1[1][3], hw[1][3], hx[1][1]); SB();
      mm(ac1[1][2], hw[1][2], hx[1][1]); SB();
      mmchkw<6, 0>(ac1[1][1], hw[1][1], hx[1][1], fl, lf_rd, bud); SB();
      lds_rd128<32768>(hx[0][0], px0_1); SB();
      mm(ac1[1][0], hw[1][0], hx[1][1]); SB();
      lds_rd128<0>(hw[0][0], pa0_0); SB();
      mm(ac1[2][0], hw[1][0], hx[1][2]); SB();
      lds_rd128<2048>(hw[0][1], pa0_0); SB();
      mm(ac1[2][1], hw[1][1], hx[1][2]); SB();
      lds_rd128<0>(hw[0][2], pb0_0); SB();
      mm(ac1[2][2], hw[1][2], hx[1][2]); SB();
      lds_rd128<2048>(hw[0][3], pb0_0); SB();
      mm(ac1[2][3], hw[1][3], hx[1][2]); SB();
      lds_rd128<34816>(hx[0][1], px0_1); SB();
      mm(ac1[3][3], hw[1][3], hx[1][3]); SB();
      lds_rd128<40960>(hx[0][2], px0_1); SB();
      mm(ac1[3][2], hw[1][2], hx[1][3]); SB();
      lds_rd128<43008>(hx[0][3], px0_1); SB();
      mm(ac1[3][1], hw[1][1], hx[1][3]); SB();
      mm(ac1[3][0], hw[1][0], hx[1][3]); SB();
      mmw<5>(ac1[0][0], hw[0][0], hx[0][0]); SB();
      lds_rd128<32768>(hx[1][0], px1_1); SB();
      mm(ac1[0][1], hw[0][1], hx[0][0]); SB();
      lds_rd128<0>(hw[1][0], pa1_0); SB();
      mmw<5>(ac1[0][2], hw[0][2], hx[0][0]); SB();
      lds_rd128<2048>(hw[1][1], pa1_0); SB();
      mm(ac1[0][3], hw[0][3], hx[0][0]); SB();
      lds_rd128<0>(hw[1][2], pb1_0); SB();
      mmw<5>(ac1[1][3], hw[0][3], hx[0][1]); SB();
      lds_rd128<2048>(hw[1][3], pb1_0); SB();
      mm(ac1[1][2], hw[0][2], hx[0][1]); SB();
      lds_rd128<34816>(hx[1][1], px1_1); SB();
      mm(ac1[1][1], hw[0][1], hx[0][1]); SB();
      lds_rd128<40960>(hx[1][2], px1_1); SB();
      mm(ac1[1][0], hw[0][0], hx[0][1]); SB();
      lds_rd128<43008>(hx[1][3], px1_1); SB();
      mm(ac1[2][0], hw[0][0], hx[0][2]); SB();
      mm(ac1[2][1], hw[0][1], hx[0][2]); SB();
      mm(ac1[2][2], hw[0][2], hx[0][2]); SB();
      mm(ac1[2][3], hw[0][3], hx[0][2]); SB();
      mmw<7>(ac1[3][3], hw[0][3], hx[0][3]); SB();
      mm(ac1[3][2], hw[0][2], hx[0][3]); SB();
      asm volatile("s_waitcnt lgkmcnt(0)" : "+v"(hw[1][0]), "+v"(hw[1][1]), "+v"(hw[1][2]), "+v"(hw[1][3]), "+v"(hx[1][1]), "+v"(hx[1][2]), "+v"(hx[1][3])::"memory"); SB();
      SIG(6); SB();
      mm(ac1[3][1], hw[0][1], hx[0][3]); SB();
      mm(ac1[3][0], hw[0][0], hx[0][3]); SB();
      mm(ac1[0][0], hw[1][0], hx[1][0]); SB();
      lds_rd32(fl, lf_rd); SB();
      mm(ac1[0][1], hw[1][1], hx[1][0]); SB();
      mm(ac1[0][2], hw[1][2], hx[1][0]); SB();
      mm(ac1[0][3], hw[1][3], hx[1][0]); SB();
      mm(ac1[1][3], hw[1][3], hx[1][1]); SB();
      mm(ac1[1][2], hw[1][2], hx[1][1]); SB();
      mmchkw<7, 0>(ac1[1][1], hw[1][1], hx[1][1], fl, lf_rd, bud); SB();
      lds_rd128<49152>(hx[0][0], px0_1); SB();
      mm(ac1[1][0], hw[1][0], hx[1][1]); SB();
      lds_rd128<16384>(hw[0][0], pa0_0); SB();
      mm(ac1[2][0], hw[1][0], hx[1][2]); SB();
      lds_rd128<18432>(hw[0][1], pa0_0); SB();
      mm(ac1[2][1], hw[1][1], hx[1][2]); SB();
      lds_rd128<16384>(hw[0][2], pb0_0); SB();
      mm(ac1[2][2], hw[1][2], hx[1][2]); SB();
      lds_rd128<18432>(hw[0][3], pb0_0); SB();
      mm(ac1[2][3], hw[1][3], hx[1][2]); SB();
      lds_rd128<51200>(hx[0][1], px0_1); SB();
      mm(ac1[3][3], hw[1][3], hx[1][3]); SB();
      lds_rd128<57344>(hx[0][2], px0_1); SB();
      mm(ac1[3][2], hw[1][2], hx[1][3]); SB();
      lds_rd128<59392>(hx[0][3], px0_1); SB();
      mm(ac1[3][1], hw[1][1], hx[1][3]); SB();
      mm(ac1[3][0], hw[1][0], hx[1][3]); SB();
      mmw<5>(ac1[0][0], hw[0][0], hx[0][0]); SB();
      lds_rd128<49152>(hx[1][0], px1_1); SB();
      mm(ac1[0][1], hw[0][1], hx[0][0]); SB();
      lds_rd128<16384>(hw[1][0], pa1_0); SB();
      mmw<5>(ac1[0][2], hw[0][2], hx[0][0]); SB();
      lds_rd128<18432>(hw[1][1], pa1_0); SB();
      mm(ac1[0][3], hw[0][3], hx[0][0]); SB();
      lds_rd128<16384>(hw[1][2], pb1_0); SB();
      mmw<5>(ac1[1][3], hw[0][3], hx[0][1]); SB();
      lds_rd128<18432>(hw[1][3], pb1_0); SB();
      mm(ac1[1][2], hw[0][2], hx[0][1]); SB();
      lds_rd128<51200>(hx[1][1], px1_1); SB();
      mm(ac1[1][1], hw[0][1], hx[0][1]); SB();
      lds_rd128<57344>(hx[1][2], px1_1); SB();
      mm(ac1[1][0], hw[0][0], hx[0][1]); SB();
      lds_rd128<59392>(hx[1][3], px1_1); SB();
      mm(ac1[2][0], hw[0][0], hx[0][2]); SB();
      mm(ac1[2][1], hw[0][1], hx[0][2]); SB();
      mm(ac1[2][2], hw[0][2], hx[0][2]); SB();
      mm(ac1[2][3], hw[0][3], hx[0][2]); SB();
      mmw<7>(ac1[3][3], hw[0][3], hx[0][3]); SB();
      mm(ac1[3][2], hw[0][2], hx[0][3]); SB();
      asm volatile("s_waitcnt lgkmcnt(0)" : "+v"(hw[1][0]), "+v"(hw[1][1]), "+v"(hw[1][2]), "+v"(hw[1][3]), "+v"(hx[1][1]), "+v"(hx[1][2]), "+v"(hx[1][3])::"memory"); SB();
      SIG(7); SB();
      mm(ac1[3][1], hw[0][1], hx[0][3]); SB();
      mm(ac1[3][0], hw[0][0], hx[0][3]); SB();
      mm(ac1[0][0], hw[1][0], hx[1][0]); SB();
      lds_rd32(fl, lf_rd); SB();
      mm(ac1[0][1], hw[1][1], hx[1][0]); SB();
      mm(ac1[0][2], hw[1][2], hx[1][0]); SB();
      mm(ac1[0][3], hw[1][3], hx[1][0]); SB();
      mm(ac1[1][3], hw[1][3], hx[1][1]); SB();
      mm(ac1[1][2], hw[1][2], hx[1][1]); SB();
      mmchkw<8, 0>(ac1[1][1], hw[1][1], hx[1][1], fl, lf_rd, bud); SB();
      lds_rd128<0>(hx[0][0], px0_2); SB();
      mm(ac1[1][0], hw[1][0], hx[1][1]); SB();
      lds_rd128<32768>(hw[0][0], pa0_0); SB();
      mm(ac1[2][0], hw[1][0], hx[1][2]); SB();
      lds_rd128<34816>(hw[0][1], pa0_0); SB();
      mm(ac1[2][1], hw[1][1], hx[1][2]); SB();
      lds_rd128<32768>(hw[0][2], pb0_0); SB();
      mm(ac1[2][2], hw[1][2], hx[1][2]); SB();
      lds_rd128<34816>(hw[0][3], pb0_0); SB();
      mm(ac1[2][3], hw[1][3], hx[1][2]); SB();
      lds_rd128<2048>(hx[0][1], px0_2); SB();
      mm(ac1[3][3], hw[1][3], hx[1][3]); SB();
      lds_rd128<8192>(hx[0][2], px0_2); SB();
      mm(ac1[3][2], hw[1][2], hx[1][3]); SB();
      lds_rd128<10240>(hx[0][3], px0_2); SB();
      mm(ac1[3][1], hw[1][1], hx[1][3]); SB();
      mm(ac1[3][0], hw[1][0], hx[1][3]); SB();
      mmw<5>(ac1[0][0], hw[0][0], hx[0][0]); SB();
      lds_rd128<0>(hx[1][0], px1_2); SB();
      mmw<3>(ac1[1][0], hw[0][0], hx[0][1]); SB();
      lds_rd128<32768>(hw[1][0], pa1_0); SB();
      mm(ac1[0][1], hw[0][1], hx[0][0]); SB();
      lds_rd128<34816>(hw[1][1], pa1_0); SB();
      mm(ac1[1][1], hw[0][1], hx[0][1]); SB();
      lds_rd128<32768>(hw[1][2], pb1_0); SB();
      mm(ac1[0][2], hw[0][2], hx[0][0]); SB();
      lds_rd128<34816>(hw[1][3], pb1_0); SB();
      mm(ac1[1][2], hw[0][2], hx[0][1]); SB();
      lds_rd128<2048>(hx[1][1], px1_2); SB();
      mm(ac1[0][3], hw[0][3], hx[0][0]); SB();
      lds_rd128<8192>(hx[1][2], px1_2); SB();
      mm(ac1[1][3], hw[0][3], hx[0][1]); SB();
      lds_rd128<10240>(hx[1][3], px1_2); SB();
      mmw<6>(ac1[0][0], hw[1][0], hx[1][0]); SB();
      mmw<2>(ac1[1][0], hw[1][0], hx[1][1]); SB();
      mm(ac1[0][1], hw[1][1], hx[1][0]); SB();
      mm(ac1[1][1], hw[1][1], hx[1][1]); SB();
      mm(ac1[0][2], hw[1][2], hx[1][0]); SB();
      asm volatile("s_waitcnt lgkmcnt(0)" : "+v"(hx[1][2]), "+v"(hx[1][3])::"memory"); SB();
      SIG(8); SB();
      mm(ac1[1][2], hw[1][2], hx[1][1]); SB();
      PUB0(0, 0); SB();
      mm(ac1[0][3], hw[1][3], hx[1][0]); SB();
      PUB1(0, 0); SB();
      mm(ac1[1][3], hw[1][3], hx[1][1]); SB();
      PUB2(); SB();
      mm(ac1[2][0], hw[0][0], hx[0][2]); SB();
      PUB3(0, 0); SB();
      mm(ac1[3][0], hw[0][0], hx[0][3]); SB();
      PUB0(0, 1); SB();
      mm(ac1[2][0], hw[1][0], hx[1][2]); SB();
      PUB1(0, 1); SB();
      mm(ac1[3][0], hw[1][0], hx[1][3]); SB();
      PUB2(); SB();
      mm(ac1[2][1], hw[0][1], hx[0][2]); SB();
      PUB3(0, 1); SB();
      mm(ac1[3][1], hw[0][1], hx[0][3]); SB();
      PUB0(0, 2); SB();
      mm(ac1[2][1], hw[1][1], hx[1][2]); SB();
      PUB1(0, 2); SB();
      mm(ac1[3][1], hw[1][1], hx[1][3]); SB();
      PUB2(); SB();
      mm(ac1[2][2], hw[0][2], hx[0][2]); SB();
      PUB3(0, 2); SB();
      mm(ac1[3][2], hw[0][2], hx[0][3]); SB();
      PUB0(0, 3); SB();
      mm(ac1[2][2], hw[1][2], hx[1][2]); SB();
      PUB1(0, 3); SB();
      mm(ac1[3][2], hw[1][2], hx[1][3]); SB();
      PUB2(); SB();
      mm(ac1[2][3], hw[0][3], hx[0][2]); SB();
      PUB3(0, 3); SB();
      mm(ac1[3][3], hw[0][3], hx[0][3]); SB();
      PUB0(2, 0); SB();
      mm(ac1[2][3], hw[1][3], hx[1][2]); SB();
      PUB1(2, 0); SB();
      mm(ac1[3][3], hw[1][3], hx[1][3]); SB();
      PUB2(); SB();
      asm volatile("s_nop 7" : "+v"(ac1[2][3]), "+v"(ac1[3][3]), "+v"(ac1[2][2]), "+v"(ac1[3][2])); SB();
      PUB3(2, 0); SB();
      PUB0(2, 1); SB();
      PUB1(2, 1); SB();
      PUB2(); SB();
      PUB3(2, 1); SB();
      PUB0(2, 2); SB();
      PUB1(2, 2); SB();
      PUB2(); SB();
      PUB3(2, 2); SB();
      PUB0(2, 3); SB();
      PUB1(2, 3); SB();
      PUB2(); SB();
      PUB3(2, 3); SB();
      OWN_SIG(1);
    }
    SPIN(tg_rd, 1);
    const unsigned tag = __builtin_amdgcn_readfirstlane(lflag[15]);
    tagv = tag;
    asm volatile("" : "+v"(tagv));
    f32x4 ac[4][2][2];
    const unsigned xg = (unsigned)((ti * 4 + ej) * 98304 + q * 24576 + wid * 2048 + lane * 16);
    const char* xb = ws + W_XB;
    const unsigned hw_ = sb + 147456 + (wid * 32 + fr) * 64 + (((fq >> 1) ^ (fr >> 2)) << 4) + (fq & 1) * 8;
    const unsigned tb[4] = {tag & 0x70007u, tag >> 3 & 0x70007u, tag >> 6 & 0x70007u, tag >> 9 & 0x70004u};
    unsigned pw[8], em[2], ek[2], msk = 0x70007u;
    u64 nz;
#define AV(G, i) ac[G][(i) >> 2 & 1][(i) >> 3][(i) & 3]
#define EM0(G, c) asm volatile("v_max3_f32 %0, |%1|, |%2|, |%3|" : "=v"(em[c]) : "v"(AV(G, 8 * (c))), "v"(AV(G, 8 * (c) + 1)), "v"(AV(G, 8 * (c) + 2)))
#define EM1(G, c, i, j) asm volatile("v_max3_f32 %0, %0, |%1|, |%2|" : "+v"(em[c]) : "v"(AV(G, i)), "v"(AV(G, j)))
#define EM2() asm volatile("v_max_f32 %0, %0, %1" : "+v"(em[0]) : "v"(em[1]))
#define ES0() asm volatile("v_subrev_u32 %0, 0x39800000, %1" : "=v"(em[1]) : "v"(em[0]))
#define ES1() asm volatile("v_cmp_ne_u32 %0, 0, %1" : "=s"(nz) : "v"(em[0]))
#define EK(c) asm volatile("v_or_b32 %0, %1, %2" : "=v"(ek[c]) : "i"(2 * (c)), "s"(tb[3]))
#define EH(G, P) asm volatile("v_cvt_pk_f16_f32 %0, %1, %2" : "=v"(pw[P]) : "v"(AV(G, 2 * (P))), "v"(AV(G, 2 * (P) + 1)))
#define ET(P) asm volatile("v_bfi_b32 %0, %1, %2, %0" : "+v"(pw[P]) : "v"(msk), "s"(tb[(P) & 3]))
#define EU(P) asm volatile("v_bfi_b32 %0, %1, %2, %0" : "+v"(pw[P]) : "v"(msk), "v"(ek[(P) >> 2]))
#define EX(G) asm volatile("v_cmp_le_u32 vcc, 0x0d800000, %17\n\ts_and_b64 vcc, vcc, %20\n\ts_cbranch_scc1 .Lxa%=\n.Lxb%=:\n\t.subsection 1\n.Lxa%=:\n\t"            \
      "v_lshrrev_b32 %17, 23, %16\n\tv_add_u32 %17, 6, %17\n\tv_lshrrev_b32 %17, 4, %17\n\tv_med3_u32 %17, %17, 1, 15\n\tv_lshlrev_b32 %16, 27, %17\n\t"              \
      "v_sub_u32 %16, 0x7f800000, %16\n\tv_and_or_b32 %18, %17, 3, %21\n\tv_lshrrev_b32 %17, 2, %17\n\tv_or_b32 %19, %21, %17\n\t"                                  \
      "v_mul_f32 %0, %0, %16\n\tv_mul_f32 %1, %1, %16\n\tv_mul_f32 %2, %2, %16\n\tv_mul_f32 %3, %3, %16\n\tv_mul_f32 %4, %4, %16\n\tv_mul_f32 %5, %5, %16\n\t"       \
      "v_mul_f32 %6, %6, %16\n\tv_mul_f32 %7, %7, %16\n\tv_mul_f32 %8, %8, %16\n\tv_mul_f32 %9, %9, %16\n\tv_mul_f32 %10, %10, %16\n\tv_mul_f32 %11, %11, %16\n\t"   \
      "v_mul_f32 %12, %12, %16\n\tv_mul_f32 %13, %13, %16\n\tv_mul_f32 %14, %14, %16\n\tv_mul_f32 %15, %15, %16\n\ts_branch .Lxb%=\n\t.subsection 0"               \
      : "+v"(AV(G, 0)), "+v"(AV(G, 1)), "+v"(AV(G, 2)), "+v"(AV(G, 3)), "+v"(AV(G, 4)), "+v"(AV(G, 5)), "+v"(AV(G, 6)), "+v"(AV(G, 7)), "+v"(AV(G, 8)), "+v"(AV(G, 9)),  \
        "+v"(AV(G, 10)), "+v"(AV(G, 11)), "+v"(AV(G, 12)), "+v"(AV(G, 13)), "+v"(AV(G, 14)), "+v"(AV(G, 15)), "+v"(em[0]), "+v"(em[1]), "+v"(ek[0]), "+v"(ek[1])        \
      : "s"(nz), "s"(tb[3]) : "vcc", "scc")
    const char *xd0 = xb + ((q ^ 3) - q) * 24576, *xd1 = xb + ((q ^ 2) - q) * 24576 + 8192, *xd2 = xb + ((q ^ 1) - q) * 24576 + 16384;
    const char *xr0 = xb, *xr1 = xb + 8192, *xr2 = xb + 16384;
    asm volatile("" : "+s"(xd0), "+s"(xd1), "+s"(xd2), "+s"(xr0), "+s"(xr1), "+s"(xr2));
#define SEND(G, Q) asm volatile("global_store_dwordx4 %0, %1, %2 offset:%3 sc1\n\ts_nop 1" ::"v"(xg), "v"(u32x4{pw[4 * (Q)], pw[4 * (Q) + 1], pw[4 * (Q) + 2], pw[4 * (Q) + 3]}), "s"(xd##G), "i"((Q) * 1024) : "memory")
    unsigned oo;
#define OO() do { oo = (unsigned)(((ti * 128 + wid * 32 + fr) * D_ + e0 + (fq & 1) * 16 + (fq >> 1) * 8) * 2); asm volatile("" : "+v"(oo)); } while (0)
    unsigned sn = 9 * 4u;
    float gs[2];
    u32x4 rv[3][2];
    u32x2 dq[4];
    unsigned f2;
    const unsigned d2_rd = best_rd;
#define RV_LD(S, tail) asm volatile("global_load_dwordx4 %0, %2, %3 sc1\n\tglobal_load_dwordx4 %1, %2, %3 offset:1024 sc1" tail \
                                    : "=&v"(rv[S][0]), "=&v"(rv[S][1]) : "v"(xg), "s"(xr##S) : "memory")
#define D_RD(i) asm volatile("ds_read_b64 %0, %1 offset:%2" : "=v"(dq[i]) : "v"(hw_ ^ ((i) & 1) * 32u), "i"(((i) >> 1) * 1024) : "memory")
#define G_RD(i) asm volatile("ds_read_b32 %0, %1 offset:%2" : "=v"(gs[i]) : "v"(sb + RING + L_G + (wid * 32 + fr) * 4), "i"((i) * 64) : "memory")
#define SIGQ(W) do { __builtin_amdgcn_s_waitcnt(0xc07f | ((W) << 8)); SB(); asm volatile("global_load_lds_dword %0, %1" ::"v"(sn), "s"(tab) : "memory"); } while (0)
#define SIGN() asm volatile("v_add_u32 %0, 4, %0" : "+v"(sn))
    asm volatile("s_mov_b32 m0, %2" : "+v"(sn), "+v"(msk) : "s"(row_s) : "memory");
    SPINF(lf_rd, 9, 0); SB();
    lds_rd128<49152>(wr[0][0], wa0_0); SB();
    lds_rd128<51200>(wr[0][1], wa0_0); SB();
    lds_rd128<49152>(wr[0][2], wa1_0); SB();
    lds_rd128<51200>(wr[0][3], wa1_0); SB();
    lds_rd128<0>(wr[1][0], wa0_1); SB();
    lds_rd128<2048>(wr[1][1], wa0_1); SB();
    lds_rd128<0>(wr[1][2], wa1_1); SB();
    lds_rd128<2048>(wr[1][3], wa1_1); SB();
    asm volatile("s_waitcnt lgkmcnt(0)" : "+v"(wr[0][0]), "+v"(wr[0][1]), "+v"(wr[0][2]), "+v"(wr[0][3]), "+v"(wr[1][0]), "+v"(wr[1][1]), "+v"(wr[1][2]), "+v"(wr[1][3])::"memory"); SB();
    OWN_SIG(2); SB();
    TRYFLAG(0); SB();
    SPINF(lf_rd, 10, 0); SB();
    lds_rd128<16384>(wr[2][0], wa0_1); SB();
    lds_rd128<18432>(wr[2][1], wa0_1); SB();
    lds_rd128<16384>(wr[2][2], wa1_1); SB();
    lds_rd128<18432>(wr[2][3], wa1_1); SB();
    lds_rd128<0>(wr[3][0], wa0_0); SB();
    lds_rd128<2048>(wr[3][1], wa0_0); SB();
    lds_rd128<0>(wr[3][2], wa1_0); SB();
    lds_rd128<2048>(wr[3][3], wa1_0); SB();
    asm volatile("s_waitcnt lgkmcnt(0)" : "+v"(wr[2][0]), "+v"(wr[2][1]), "+v"(wr[2][2]), "+v"(wr[2][3]), "+v"(wr[3][0]), "+v"(wr[3][1]), "+v"(wr[3][2]), "+v"(wr[3][3])::"memory"); SB();
    OWN_SIG(3); SB();
    TRYFLAG(0); SB();
    SPINF(lf_rd, 11, 0); SB();
    lds_rd128<32768>(wr[4][0], wa0_1); SB();
    lds_rd128<34816>(wr[4][1], wa0_1); SB();
    lds_rd128<32768>(wr[4][2], wa1_1); SB();
    lds_rd128<34816>(wr[4][3], wa1_1); SB();
    lds_rd128<16384>(wr[5][0], wa0_0); SB();
    lds_rd128<18432>(wr[5][1], wa0_0); SB();
    lds_rd128<16384>(wr[5][2], wa1_0); SB();
    lds_rd128<18432>(wr[5][3], wa1_0); SB();
    asm volatile("s_waitcnt lgkmcnt(0)" : "+v"(wr[4][0]), "+v"(wr[4][1]), "+v"(wr[4][2]), "+v"(wr[4][3]), "+v"(wr[5][0]), "+v"(wr[5][1]), "+v"(wr[5][2]), "+v"(wr[5][3])::"memory"); SB();
    OWN_SIG(4); SB();
    TRYFLAG(0); SB();
    SPINF(lf_rd, 12, 0); SB();
    lds_rd128<49152>(wr[6][0], wa0_1); SB();
    lds_rd128<51200>(wr[6][1], wa0_1); SB();
    lds_rd128<49152>(wr[6][2], wa1_1); SB();
    lds_rd128<51200>(wr[6][3], wa1_1); SB();
    lds_rd128<32768>(wr[7][0], wa0_0); SB();
    lds_rd128<34816>(wr[7][1], wa0_0); SB();
    lds_rd128<32768>(wr[7][2], wa1_0); SB();
    lds_rd128<34816>(wr[7][3], wa1_0); SB();
    asm volatile("s_waitcnt lgkmcnt(0)" : "+v"(wr[6][0]), "+v"(wr[6][1]), "+v"(wr[6][2]), "+v"(wr[6][3]), "+v"(wr[7][0]), "+v"(wr[7][1]), "+v"(wr[7][2]), "+v"(wr[7][3])::"memory"); SB();
    OWN_SIG(5); SB();
    TRYFLAG(0); SB();
    FLAG(0); SB();
    OO();
    SPIN(lf_rd, 13); SB();
    lds_rd128<0>(fb[0], xa_0); SB();
    lds_rd128<1024>(fb[1], xa_0); SB();
    lds_rd128<2048>(fb[2], xa_0); SB();
    lds_rd128<3072>(fb[3], xa_0); SB();
    lds_rd128<4096>(fb[4], xa_0); SB();
    lds_rd128<5120>(fb[5], xa_0); SB();
    lds_rd32(fl, lf_rd); SB();
    mm0w<6>(ac[0][0][0], fb[0], wr[0][0]); SB();
    lds_rd128<6144>(fb[6], xa_0); SB();
    mm0(ac[0][0][1], fb[0], wr[0][1]); SB();
    mm0w<6>(ac[0][1][1], fb[1], wr[0][1]); SB();
    lds_rd128<7168>(fb[7], xa_0); SB();
    mm0(ac[0][1][0], fb[1], wr[0][0]); SB();
    mm0w<2>(ac[1][0][0], fb[2], wr[0][0]); SB();
    chk<14>(fl, lf_rd, bud); SB();
    lds_rd128<8192>(fb[8], xa_0); SB();
    mm0(ac[1][0][1], fb[2], wr[0][1]); SB();
    mm0(ac[1][1][1], fb[3], wr[0][1]); SB();
    lds_rd128<9216>(fb[0], xa_0); SB();
    mm0(ac[1][1][0], fb[3], wr[0][0]); SB();
    mm(ac[0][0][0], fb[4], wr[0][2]); SB();
    lds_rd128<10240>(fb[1], xa_0); SB();
    SIGQ(3); SB();
    mm(ac[0][0][1], fb[4], wr[0][3]); SB();
    mm(ac[0][1][1], fb[5], wr[0][3]); SB();
    lds_rd128<11264>(fb[2], xa_0); SB();
    SIGN(); SB();
    mm(ac[0][1][0], fb[5], wr[0][2]); SB();
    mm(ac[1][0][0], fb[6], wr[0][2]); SB();
    lds_rd128<12288>(fb[3], xa_0); SB();
    mm(ac[1][0][1], fb[6], wr[0][3]); SB();
    mm(ac[1][1][1], fb[7], wr[0][3]); SB();
    lds_rd128<13312>(fb[4], xa_0); SB();
    lds_rd32(fl, lf_rd); SB();
    mm(ac[1][1][0], fb[7], wr[0][2]); SB();
    mm0w<5>(ac[2][0][0], fb[8], wr[0][0]); SB();
    lds_rd128<14336>(fb[5], xa_0); SB();
    mm0(ac[2][0][1], fb[8], wr[0][1]); SB();
    mm0(ac[2][1][1], fb[0], wr[0][1]); SB();
    lds_rd128<15360>(fb[6], xa_0); SB();
    mm0(ac[2][1][0], fb[0], wr[0][0]); SB();
    mm0w<2>(ac[3][0][0], fb[1], wr[0][0]); SB();
    chk<15>(fl, lf_rd, bud); SB();
    lds_rd128<32768>(fb[7], xa_1); SB();
    mm0(ac[3][0][1], fb[1], wr[0][1]); SB();
    mm0(ac[3][1][1], fb[2], wr[0][1]); SB();
    lds_rd128<33792>(fb[8], xa_1); SB();
    mm0(ac[3][1][0], fb[2], wr[0][0]); SB();
    mm(ac[2][0][0], fb[3], wr[0][2]); SB();
    lds_rd128<34816>(fb[0], xa_1); SB();
    SIGQ(3); SB();
    mm(ac[2][0][1], fb[3], wr[0][3]); SB();
    mm(ac[2][1][1], fb[4], wr[0][3]); SB();
    lds_rd128<35840>(fb[1], xa_1); SB();
    SIGN(); SB();
    mm(ac[2][1][0], fb[4], wr[0][2]); SB();
    mm(ac[3][0][0], fb[5], wr[0][2]); SB();
    lds_rd128<36864>(fb[2], xa_1); SB();
    mm(ac[3][0][1], fb[5], wr[0][3]); SB();
    mm(ac[3][1][1], fb[6], wr[0][3]); SB();
    lds_rd128<37888>(fb[3], xa_1); SB();
    lds_rd32(fl, lf_rd); SB();
    mm(ac[3][1][0], fb[6], wr[0][2]); SB();
    mmw<5>(ac[0][0][0], fb[7], wr[1][0]); SB();
    lds_rd128<38912>(fb[4], xa_1); SB();
    mm(ac[0][0][1], fb[7], wr[1][1]); SB();
    mm(ac[0][1][1], fb[8], wr[1][1]); SB();
    lds_rd128<39936>(fb[5], xa_1); SB();
    mm(ac[0][1][0], fb[8], wr[1][0]); SB();
    mmchkw<16, 2>(ac[1][0][0], fb[0], wr[1][0], fl, lf_rd, bud); SB();
    lds_rd128<40960>(fb[6], xa_1); SB();
    mm(ac[1][0][1], fb[0], wr[1][1]); SB();
    lds_rd128<0>(wr[8][0], wa0_2); SB();
    mm(ac[1][1][1], fb[1], wr[1][1]); SB();
    lds_rd128<41984>(fb[7], xa_1); SB();
    lds_rd128<2048>(wr[8][1], wa0_2); SB();
    mm(ac[1][1][0], fb[1], wr[1][0]); SB();
    lds_rd128<0>(wr[8][2], wa1_2); SB();
    mm(ac[0][0][0], fb[2], wr[1][2]); SB();
    lds_rd128<43008>(fb[8], xa_1); SB();
    lds_rd128<2048>(wr[8][3], wa1_2); SB();
    mm(ac[0][0][1], fb[2], wr[1][3]); SB();
    mm(ac[0][1][1], fb[3], wr[1][3]); SB();
    lds_rd128<44032>(fb[0], xa_1); SB();
    mm(ac[0][1][0], fb[3], wr[1][2]); SB();
    mmw<8>(ac[1][0][0], fb[4], wr[1][2]); SB();
    lds_rd128<45056>(fb[1], xa_1); SB();
    mm(ac[1][0][1], fb[4], wr[1][3]); SB();
    mm(ac[1][1][1], fb[5], wr[1][3]); SB();
    lds_rd128<46080>(fb[2], xa_1); SB();
    lds_rd32(fl, lf_rd); SB();
    mm(ac[1][1][0], fb[5], wr[1][2]); SB();
    mmw<9>(ac[2][0][0], fb[6], wr[1][0]); SB();
    lds_rd128<47104>(fb[3], xa_1); SB();
    mm(ac[2][0][1], fb[6], wr[1][1]); SB();
    mmw<8>(ac[2][1][1], fb[7], wr[1][1]); SB();
    lds_rd128<48128>(fb[4], xa_1); SB();
    mm(ac[2][1][0], fb[7], wr[1][0]); SB();
    mmchkw<17, 2>(ac[3][0][0], fb[8], wr[1][0], fl, lf_rd, bud); SB();
    lds_rd128<16384>(fb[5], xa_0); SB();
    mm(ac[3][0][1], fb[8], wr[1][1]); SB();
    mm(ac[3][1][1], fb[0], wr[1][1]); SB();
    lds_rd128<17408>(fb[6], xa_0); SB();
    mm(ac[3][1][0], fb[0], wr[1][0]); SB();
    mm(ac[2][0][0], fb[1], wr[1][2]); SB();
    lds_rd128<18432>(fb[7], xa_0); SB();
    SIGQ(3); SB();
    mm(ac[2][0][1], fb[1], wr[1][3]); SB();
    mm(ac[2][1][1], fb[2], wr[1][3]); SB();
    lds_rd128<19456>(fb[8], xa_0); SB();
    SIGN(); SB();
    mm(ac[2][1][0], fb[2], wr[1][2]); SB();
    mm(ac[3][0][0], fb[3], wr[1][2]); SB();
    lds_rd128<20480>(fb[0], xa_0); SB();
    mm(ac[3][0][1], fb[3], wr[1][3]); SB();
    mm(ac[3][1][1], fb[4], wr[1][3]); SB();
    lds_rd128<21504>(fb[1], xa_0); SB();
    mm(ac[3][1][0], fb[4], wr[1][2]); SB();
    mmw<4>(ac[0][0][0], fb[5], wr[2][0]); SB();
    lds_rd128<22528>(fb[2], xa_0); SB();
    mm(ac[0][0][1], fb[5], wr[2][1]); SB();
    mm(ac[0][1][1], fb[6], wr[2][1]); SB();
    lds_rd128<23552>(fb[3], xa_0); SB();
    mm(ac[0][1][0], fb[6], wr[2][0]); SB();
    mmw<4>(ac[1][0][0], fb[7], wr[2][0]); SB();
    lds_rd128<24576>(fb[4], xa_0); SB();
    mm(ac[1][0][1], fb[7], wr[2][1]); SB();
    lds_rd128<49152>(wr[9][0], wa0_0); SB();
    mm(ac[1][1][1], fb[8], wr[2][1]); SB();
    lds_rd128<25600>(fb[5], xa_0); SB();
    lds_rd128<51200>(wr[9][1], wa0_0); SB();
    mm(ac[1][1][0], fb[8], wr[2][0]); SB();
    lds_rd128<49152>(wr[9][2], wa1_0); SB();
    mmw<7>(ac[0][0][0], fb[0], wr[2][2]); SB();
    lds_rd128<26624>(fb[6], xa_0); SB();
    lds_rd128<51200>(wr[9][3], wa1_0); SB();
    mm(ac[0][0][1], fb[0], wr[2][3]); SB();
    mm(ac[0][1][1], fb[1], wr[2][3]); SB();
    lds_rd128<27648>(fb[7], xa_0); SB();
    mm(ac[0][1][0], fb[1], wr[2][2]); SB();
    mmw<8>(ac[1][0][0], fb[2], wr[2][2]); SB();
    lds_rd128<28672>(fb[8], xa_0); SB();
    mm(ac[1][0][1], fb[2], wr[2][3]); SB();
    mm(ac[1][1][1], fb[3], wr[2][3]); SB();
    lds_rd128<29696>(fb[0], xa_0); SB();
    lds_rd32(fl, lf_rd); SB();
    mm(ac[1][1][0], fb[3], wr[2][2]); SB();
    mmw<9>(ac[2][0][0], fb[4], wr[2][0]); SB();
    lds_rd128<30720>(fb[1], xa_0); SB();
    mm(ac[2][0][1], fb[4], wr[2][1]); SB();
    mmw<8>(ac[2][1][1], fb[5], wr[2][1]); SB();
    lds_rd128<31744>(fb[2], xa_0); SB();
    mm(ac[2][1][0], fb[5], wr[2][0]); SB();
    mmchkw<18, 2>(ac[3][0][0], fb[6], wr[2][0], fl, lf_rd, bud); SB();
    lds_rd128<49152>(fb[3], xa_1); SB();
    mm(ac[3][0][1], fb[6], wr[2][1]); SB();
    mm(ac[3][1][1], fb[7], wr[2][1]); SB();
    lds_rd128<50176>(fb[4], xa_1); SB();
    mm(ac[3][1][0], fb[7], wr[2][0]); SB();
    mm(ac[2][0][0], fb[8], wr[2][2]); SB();
    lds_rd128<51200>(fb[5], xa_1); SB();
    SIGQ(3); SB();
    mm(ac[2][0][1], fb[8], wr[2][3]); SB();
    mm(ac[2][1][1], fb[0], wr[2][3]); SB();
    lds_rd128<52224>(fb[6], xa_1); SB();
    SIGN(); SB();
    mm(ac[2][1][0], fb[0], wr[2][2]); SB();
    mm(ac[3][0][0], fb[1], wr[2][2]); SB();
    lds_rd128<53248>(fb[7], xa_1); SB();
    mm(ac[3][0][1], fb[1], wr[2][3]); SB();
    mm(ac[3][1][1], fb[2], wr[2][3]); SB();
    lds_rd128<54272>(fb[8], xa_1); SB();
    mm(ac[3][1][0], fb[2], wr[2][2]); SB();
    mmw<4>(ac[0][0][0], fb[3], wr[3][0]); SB();
    lds_rd128<55296>(fb[0], xa_1); SB();
    mm(ac[0][0][1], fb[3], wr[3][1]); SB();
    mm(ac[0][1][1], fb[4], wr[3][1]); SB();
    lds_rd128<56320>(fb[1], xa_1); SB();
    mm(ac[0][1][0], fb[4], wr[3][0]); SB();
    mmw<4>(ac[1][0][0], fb[5], wr[3][0]); SB();
    lds_rd128<57344>(fb[2], xa_1); SB();
    mm(ac[1][0][1], fb[5], wr[3][1]); SB();
    lds_rd128<0>(wr[10][0], wa0_1); SB();
    mm(ac[1][1][1], fb[6], wr[3][1]); SB();
    lds_rd128<58368>(fb[3], xa_1); SB();
    lds_rd128<2048>(wr[10][1], wa0_1); SB();
    mm(ac[1][1][0], fb[6], wr[3][0]); SB();
    lds_rd128<0>(wr[10][2], wa1_1); SB();
    mmw<7>(ac[0][0][0], fb[7], wr[3][2]); SB();
    lds_rd128<59392>(fb[4], xa_1); SB();
    lds_rd128<2048>(wr[10][3], wa1_1); SB();
    mm(ac[0][0][1], fb[7], wr[3][3]); SB();
    mm(ac[0][1][1], fb[8], wr[3][3]); SB();
    lds_rd128<60416>(fb[5], xa_1); SB();
    mm(ac[0][1][0], fb[8], wr[3][2]); SB();
    mmw<8>(ac[1][0][0], fb[0], wr[3][2]); SB();
    lds_rd128<61440>(fb[6], xa_1); SB();
    mm(ac[1][0][1], fb[0], wr[3][3]); SB();
    mm(ac[1][1][1], fb[1], wr[3][3]); SB();
    lds_rd128<62464>(fb[7], xa_1); SB();
    lds_rd32(fl, lf_rd); SB();
    mm(ac[1][1][0], fb[1], wr[3][2]); SB();
    mmw<9>(ac[2][0][0], fb[2], wr[3][0]); SB();
    lds_rd128<63488>(fb[8], xa_1); SB();
    mm(ac[2][0][1], fb[2], wr[3][1]); SB();
    mmw<8>(ac[2][1][1], fb[3], wr[3][1]); SB();
    lds_rd128<64512>(fb[0], xa_1); SB();
    mm(ac[2][1][0], fb[3], wr[3][0]); SB();
    mmchkw<19, 2>(ac[3][0][0], fb[4], wr[3][0], fl, lf_rd, bud); SB();
    lds_rd128<32768>(fb[1], xa_0); SB();
    mm(ac[3][0][1], fb[4], wr[3][1]); SB();
    mm(ac[3][1][1], fb[5], wr[3][1]); SB();
    lds_rd128<33792>(fb[2], xa_0); SB();
    mm(ac[3][1][0], fb[5], wr[3][0]); SB();
    mm(ac[2][0][0], fb[6], wr[3][2]); SB();
    lds_rd128<34816>(fb[3], xa_0); SB();
    SIGQ(3); SB();
    mm(ac[2][0][1], fb[6], wr[3][3]); SB();
    mm(ac[2][1][1], fb[7], wr[3][3]); SB();
    lds_rd128<35840>(fb[4], xa_0); SB();
    SIGN(); SB();
    mm(ac[2][1][0], fb[7], wr[3][2]); SB();
    mm(ac[3][0][0], fb[8], wr[3][2]); SB();
    lds_rd128<36864>(fb[5], xa_0); SB();
    mm(ac[3][0][1], fb[8], wr[3][3]); SB();
    mm(ac[3][1][1], fb[0], wr[3][3]); SB();
    lds_rd128<37888>(fb[6], xa_0); SB();
    mm(ac[3][1][0], fb[0], wr[3][2]); SB();
    mmw<4>(ac[0][0][0], fb[1], wr[4][0]); SB();
    lds_rd128<38912>(fb[7], xa_0); SB();
    mm(ac[0][0][1], fb[1], wr[4][1]); SB();
    mm(ac[0][1][1], fb[2], wr[4][1]); SB();
    lds_rd128<39936>(fb[8], xa_0); SB();
    mm(ac[0][1][0], fb[2], wr[4][0]); SB();
    mmw<4>(ac[1][0][0], fb[3], wr[4][0]); SB();
    lds_rd128<40960>(fb[0], xa_0); SB();
    mm(ac[1][0][1], fb[3], wr[4][1]); SB();
    lds_rd128<16384>(wr[11][0], wa0_1); SB();
    mm(ac[1][1][1], fb[4], wr[4][1]); SB();
    lds_rd128<41984>(fb[1], xa_0); SB();
    lds_rd128<18432>(wr[11][1], wa0_1); SB();
    mm(ac[1][1][0], fb[4], wr[4][0]); SB();
    lds_rd128<16384>(wr[11][2], wa1_1); SB();
    mmw<7>(ac[0][0][0], fb[5], wr[4][2]); SB();
    lds_rd128<43008>(fb[2], xa_0); SB();
    lds_rd128<18432>(wr[11][3], wa1_1); SB();
    mm(ac[0][0][1], fb[5], wr[4][3]); SB();
    mm(ac[0][1][1], fb[6], wr[4][3]); SB();
    lds_rd128<44032>(fb[3], xa_0); SB();
    mm(ac[0][1][0], fb[6], wr[4][2]); SB();
    mmw<8>(ac[1][0][0], fb[7], wr[4][2]); SB();
    lds_rd128<45056>(fb[4], xa_0); SB();
    mm(ac[1][0][1], fb[7], wr[4][3]); SB();
    mm(ac[1][1][1], fb[8], wr[4][3]); SB();
    lds_rd128<46080>(fb[5], xa_0); SB();
    lds_rd32(fl, lf_rd); SB();
    mm(ac[1][1][0], fb[8], wr[4][2]); SB();
    mmw<9>(ac[2][0][0], fb[0], wr[4][0]); SB();
    lds_rd128<47104>(fb[6], xa_0); SB();
    mm(ac[2][0][1], fb[0], wr[4][1]); SB();
    mmw<8>(ac[2][1][1], fb[1], wr[4][1]); SB();
    lds_rd128<48128>(fb[7], xa_0); SB();
    mm(ac[2][1][0], fb[1], wr[4][0]); SB();
    mmchkw<20, 2>(ac[3][0][0], fb[2], wr[4][0], fl, lf_rd, bud); SB();
    lds_rd128<0>(fb[8], xa_0); SB();
    mm(ac[3][0][1], fb[2], wr[4][1]); SB();
    mm(ac[3][1][1], fb[3], wr[4][1]); SB();
    lds_rd128<1024>(fb[0], xa_0); SB();
    mm(ac[3][1][0], fb[3], wr[4][0]); SB();
    mm(ac[2][0][0], fb[4], wr[4][2]); SB();
    lds_rd128<2048>(fb[1], xa_0); SB();
    SIGQ(3); SB();
    mm(ac[2][0][1], fb[4], wr[4][3]); SB();
    mm(ac[2][1][1], fb[5], wr[4][3]); SB();
    lds_rd128<3072>(fb[2], xa_0); SB();
    SIGN(); SB();
    mm(ac[2][1][0], fb[5], wr[4][2]); SB();
    mm(ac[3][0][0], fb[6], wr[4][2]); SB();
    lds_rd128<4096>(fb[3], xa_0); SB();
    mm(ac[3][0][1], fb[6], wr[4][3]); SB();
    mm(ac[3][1][1], fb[7], wr[4][3]); SB();
    lds_rd128<5120>(fb[4], xa_0); SB();
    mm(ac[3][1][0], fb[7], wr[4][2]); SB();
    mmw<4>(ac[0][0][0], fb[8], wr[5][0]); SB();
    lds_rd128<6144>(fb[5], xa_0); SB();
    mm(ac[0][0][1], fb[8], wr[5][1]); SB();
    mm(ac[0][1][1], fb[0], wr[5][1]); SB();
    lds_rd128<7168>(fb[6], xa_0); SB();
    mm(ac[0][1][0], fb[0], wr[5][0]); SB();
    mmw<4>(ac[1][0][0], fb[1], wr[5][0]); SB();
    lds_rd128<8192>(fb[7], xa_0); SB();
    mm(ac[1][0][1], fb[1], wr[5][1]); SB();
    mm(ac[1][1][1], fb[2], wr[5][1]); SB();
    lds_rd128<9216>(fb[8], xa_0); SB();
    mm(ac[1][1][0], fb[2], wr[5][0]); SB();
    mmw<4>(ac[0][0][0], fb[3], wr[5][2]); SB();
    lds_rd128<10240>(fb[0], xa_0); SB();
    mm(ac[0][0][1], fb[3], wr[5][3]); SB();
    mm(ac[0][1][1], fb[4], wr[5][3]); SB();
    lds_rd128<11264>(fb[1], xa_0); SB();
    mm(ac[0][1][0], fb[4], wr[5][2]); SB();
    mmw<4>(ac[1][0][0], fb[5], wr[5][2]); SB();
    lds_rd128<12288>(fb[2], xa_0); SB();
    mm(ac[1][0][1], fb[5], wr[5][3]); SB();
    mm(ac[1][1][1], fb[6], wr[5][3]); SB();
    lds_rd128<13312>(fb[3], xa_0); SB();
    lds_rd32(fl, lf_rd); SB();
    mm(ac[1][1][0], fb[6], wr[5][2]); SB();
    mmw<5>(ac[2][0][0], fb[7], wr[5][0]); SB();
    lds_rd128<14336>(fb[4], xa_0); SB();
    mm(ac[2][0][1], fb[7], wr[5][1]); SB();
    mm(ac[2][1][1], fb[8], wr[5][1]); SB();
    lds_rd128<15360>(fb[5], xa_0); SB();
    mm(ac[2][1][0], fb[8], wr[5][0]); SB();
    mmchkw<21, 2>(ac[3][0][0], fb[0], wr[5][0], fl, lf_rd, bud); SB();
    lds_rd128<0>(fb[6], xa_2); SB();
    mm(ac[3][0][1], fb[0], wr[5][1]); SB();
    mm(ac[3][1][1], fb[1], wr[5][1]); SB();
    lds_rd128<1024>(fb[7], xa_2); SB();
    mm(ac[3][1][0], fb[1], wr[5][0]); SB();
    mm(ac[2][0][0], fb[2], wr[5][2]); SB();
    lds_rd128<2048>(fb[8], xa_2); SB();
    SIGQ(3); SB();
    mm(ac[2][0][1], fb[2], wr[5][3]); SB();
    mm(ac[2][1][1], fb[3], wr[5][3]); SB();
    lds_rd128<3072>(fb[0], xa_2); SB();
    SIGN(); SB();
    mm(ac[2][1][0], fb[3], wr[5][2]); SB();
    mm(ac[3][0][0], fb[4], wr[5][2]); SB();
    lds_rd128<4096>(fb[1], xa_2); SB();
    mm(ac[3][0][1], fb[4], wr[5][3]); SB();
    mm(ac[3][1][1], fb[5], wr[5][3]); SB();
    lds_rd128<5120>(fb[2], xa_2); SB();
    mm(ac[3][1][0], fb[5], wr[5][2]); SB();
    mmw<4>(ac[0][0][0], fb[6], wr[6][0]); SB();
    lds_rd128<6144>(fb[3], xa_2); SB();
    mm(ac[0][0][1], fb[6], wr[6][1]); SB();
    mm(ac[0][1][1], fb[7], wr[6][1]); SB();
    lds_rd128<7168>(fb[4], xa_2); SB();
    mm(ac[0][1][0], fb[7], wr[6][0]); SB();
    mmw<4>(ac[1][0][0], fb[8], wr[6][0]); SB();
    lds_rd128<8192>(fb[5], xa_2); SB();
    mm(ac[1][0][1], fb[8], wr[6][1]); SB();
    lds_rd128<32768>(wr[12][0], wa0_1); SB();
    mm(ac[1][1][1], fb[0], wr[6][1]); SB();
    lds_rd128<9216>(fb[6], xa_2); SB();
    lds_rd128<34816>(wr[12][1], wa0_1); SB();
    mm(ac[1][1][0], fb[0], wr[6][0]); SB();
    lds_rd128<32768>(wr[12][2], wa1_1); SB();
    mmw<7>(ac[0][0][0], fb[1], wr[6][2]); SB();
    lds_rd128<10240>(fb[7], xa_2); SB();
    lds_rd128<34816>(wr[12][3], wa1_1); SB();
    mm(ac[0][0][1], fb[1], wr[6][3]); SB();
    mm(ac[0][1][1], fb[2], wr[6][3]); SB();
    lds_rd128<11264>(fb[8], xa_2); SB();
    mm(ac[0][1][0], fb[2], wr[6][2]); SB();
    mmw<8>(ac[1][0][0], fb[3], wr[6][2]); SB();
    lds_rd128<12288>(fb[0], xa_2); SB();
    mm(ac[1][0][1], fb[3], wr[6][3]); SB();
    mm(ac[1][1][1], fb[4], wr[6][3]); SB();
    lds_rd128<13312>(fb[1], xa_2); SB();
    lds_rd32(fl, lf_rd); SB();
    mm(ac[1][1][0], fb[4], wr[6][2]); SB();
    mmw<9>(ac[0][0][0], fb[5], wr[7][0]); SB();
    lds_rd128<14336>(fb[2], xa_2); SB();
    mm(ac[0][0][1], fb[5], wr[7][1]); SB();
    mmw<8>(ac[0][1][1], fb[6], wr[7][1]); SB();
    lds_rd128<15360>(fb[3], xa_2); SB();
    mm(ac[0][1][0], fb[6], wr[7][0]); SB();
    mmchkw<22, 2>(ac[1][0][0], fb[7], wr[7][0], fl, lf_rd, bud); SB();
    lds_rd128<49152>(fb[4], xa_0); SB();
    mm(ac[1][0][1], fb[7], wr[7][1]); SB();
    mm(ac[1][1][1], fb[8], wr[7][1]); SB();
    lds_rd128<50176>(fb[5], xa_0); SB();
    mm(ac[1][1][0], fb[8], wr[7][0]); SB();
    mm(ac[0][0][0], fb[0], wr[7][2]); SB();
    lds_rd128<51200>(fb[6], xa_0); SB();
    SIGQ(3); SB();
    mm(ac[0][0][1], fb[0], wr[7][3]); SB();
    mm(ac[0][1][1], fb[1], wr[7][3]); SB();
    lds_rd128<52224>(fb[7], xa_0); SB();
    SIGN(); SB();
    mm(ac[0][1][0], fb[1], wr[7][2]); SB();
    mm(ac[1][0][0], fb[2], wr[7][2]); SB();
    lds_rd128<53248>(fb[8], xa_0); SB();
    mm(ac[1][0][1], fb[2], wr[7][3]); SB();
    mm(ac[1][1][1], fb[3], wr[7][3]); SB();
    lds_rd128<54272>(fb[0], xa_0); SB();
    mm(ac[1][1][0], fb[3], wr[7][2]); SB();
    mmw<4>(ac[2][0][0], fb[4], wr[6][0]); SB();
    lds_rd128<55296>(fb[1], xa_0); SB();
    mm(ac[2][0][1], fb[4], wr[6][1]); SB();
    mm(ac[2][1][1], fb[5], wr[6][1]); SB();
    lds_rd128<56320>(fb[2], xa_0); SB();
    mm(ac[2][1][0], fb[5], wr[6][0]); SB();
    mmw<4>(ac[2][0][0], fb[6], wr[6][2]); SB();
    lds_rd128<57344>(fb[3], xa_0); SB();
    mm(ac[2][0][1], fb[6], wr[6][3]); SB();
    lds_rd128<16384>(wr[13][0], wa0_0); SB();
    mm(ac[2][1][1], fb[7], wr[6][3]); SB();
    lds_rd128<58368>(fb[4], xa_0); SB();
    lds_rd128<18432>(wr[13][1], wa0_0); SB();
    mm(ac[2][1][0], fb[7], wr[6][2]); SB();
    lds_rd128<16384>(wr[13][2], wa1_0); SB();
    mmw<7>(ac[2][0][0], fb[8], wr[7][0]); SB();
    lds_rd128<59392>(fb[5], xa_0); SB();
    lds_rd128<18432>(wr[13][3], wa1_0); SB();
    mm(ac[2][0][1], fb[8], wr[7][1]); SB();
    mm(ac[2][1][1], fb[0], wr[7][1]); SB();
    lds_rd128<60416>(fb[6], xa_0); SB();
    mm(ac[2][1][0], fb[0], wr[7][0]); SB();
    mmw<8>(ac[2][0][0], fb[1], wr[7][2]); SB();
    lds_rd128<61440>(fb[7], xa_0); SB();
    mm(ac[2][0][1], fb[1], wr[7][3]); SB();
    mm(ac[2][1][1], fb[2], wr[7][3]); SB();
    lds_rd128<62464>(fb[8], xa_0); SB();
    lds_rd32(fl, lf_rd); SB();
    mm(ac[2][1][0], fb[2], wr[7][2]); SB();
    mmw<9>(ac[0][0][0], fb[3], wr[8][0]); SB();
    lds_rd128<63488>(fb[0], xa_0); SB();
    mm(ac[0][0][1], fb[3], wr[8][1]); SB();
    mmw<8>(ac[0][1][1], fb[4], wr[8][1]); SB();
    lds_rd128<64512>(fb[1], xa_0); SB();
    mm(ac[0][1][0], fb[4], wr[8][0]); SB();
    mmchkw<23, 2>(ac[0][0][0], fb[5], wr[8][2], fl, lf_rd, bud); SB();
    lds_rd128<49152>(fb[2], xa_1); SB();
    mm(ac[0][0][1], fb[5], wr[8][3]); SB();
    mm(ac[0][1][1], fb[6], wr[8][3]); SB();
    lds_rd128<50176>(fb[3], xa_1); SB();
    mm(ac[0][1][0], fb[6], wr[8][2]); SB();
    mm(ac[0][0][0], fb[7], wr[9][0]); SB();
    lds_rd128<51200>(fb[4], xa_1); SB();
    SIGQ(3); SB();
    mm(ac[0][0][1], fb[7], wr[9][1]); SB();
    mm(ac[0][1][1], fb[8], wr[9][1]); SB();
    lds_rd128<52224>(fb[5], xa_1); SB();
    SIGN(); SB();
    mm(ac[0][1][0], fb[8], wr[9][0]); SB();
    mm(ac[0][0][0], fb[0], wr[9][2]); SB();
    lds_rd128<53248>(fb[6], xa_1); SB();
    mm(ac[0][0][1], fb[0], wr[9][3]); SB();
    mm(ac[0][1][1], fb[1], wr[9][3]); SB();
    lds_rd128<54272>(fb[7], xa_1); SB();
    mm(ac[0][1][0], fb[1], wr[9][2]); SB();
    mmw<4>(ac[0][0][0], fb[2], wr[10][0]); SB();
    lds_rd128<55296>(fb[8], xa_1); SB();
    mm(ac[0][0][1], fb[2], wr[10][1]); SB();
    mm(ac[0][1][1], fb[3], wr[10][1]); SB();
    lds_rd128<56320>(fb[0], xa_1); SB();
    mm(ac[0][1][0], fb[3], wr[10][0]); SB();
    mmw<4>(ac[0][0][0], fb[4], wr[10][2]); SB();
    lds_rd128<57344>(fb[1], xa_1); SB();
    mm(ac[0][0][1], fb[4], wr[10][3]); SB();
    mm(ac[0][1][1], fb[5], wr[10][3]); SB();
    lds_rd128<58368>(fb[2], xa_1); SB();
    mm(ac[0][1][0], fb[5], wr[10][2]); SB();
    mmw<4>(ac[0][0][0], fb[6], wr[11][0]); SB();
    lds_rd128<59392>(fb[3], xa_1); SB();
    mm(ac[0][0][1], fb[6], wr[11][1]); SB();
    mm(ac[0][1][1], fb[7], wr[11][1]); SB();
    lds_rd128<60416>(fb[4], xa_1); SB();
    mm(ac[0][1][0], fb[7], wr[11][0]); SB();
    mmw<4>(ac[0][0][0], fb[8], wr[11][2]); SB();
    lds_rd128<61440>(fb[5], xa_1); SB();
    mm(ac[0][0][1], fb[8], wr[11][3]); SB();
    mm(ac[0][1][1], fb[0], wr[11][3]); SB();
    lds_rd128<62464>(fb[6], xa_1); SB();
    mm(ac[0][1][0], fb[0], wr[11][2]); SB();
    lds_rd32(fl, lf_rd); SB();
    mmw<5>(ac[0][0][0], fb[1], wr[12][0]); SB();
    lds_rd128<63488>(fb[7], xa_1); SB();
    mm(ac[0][0][1], fb[1], wr[12][1]); SB();
    mm(ac[0][1][1], fb[2], wr[12][1]); SB();
    lds_rd128<64512>(fb[8], xa_1); SB();
    mm(ac[0][1][0], fb[2], wr[12][0]); SB();
    mmw<5>(ac[0][0][0], fb[3], wr[12][2]); SB();
    lds_rd128<0>(fb[0], wa0_1); SB();
    mmchkw<24, 3>(ac[0][0][1], fb[3], wr[12][3], fl, lf_rd, bud); SB();
    lds_rd128<16384>(fb[1], xa_1); SB();
    mm(ac[0][1][1], fb[4], wr[12][3]); SB();
    lds_rd128<2048>(fb[2], wa0_1); SB();
    mm(ac[0][1][0], fb[4], wr[12][2]); SB();
    lds_rd128<17408>(fb[3], xa_1); SB();
    mm(ac[0][0][0], fb[5], wr[13][0]); SB();
    lds_rd128<0>(fb[4], wa1_1); SB();
    mm(ac[0][0][1], fb[5], wr[13][1]); SB();
    lds_rd128<18432>(fb[5], xa_1); SB();
    mm(ac[0][1][1], fb[6], wr[13][1]); SB();
    SIGQ(6); SB();
    mm(ac[0][1][0], fb[6], wr[13][0]); SB();
    lds_rd128<2048>(fb[6], wa1_1); SB();
    mm(ac[0][0][0], fb[7], wr[13][2]); SB();
    SIGN(); SB();
    mm(ac[0][0][1], fb[7], wr[13][3]); SB();
    lds_rd128<19456>(fb[7], xa_1); SB();
    mm(ac[0][1][1], fb[8], wr[13][3]); SB();
    mm(ac[0][1][0], fb[8], wr[13][2]); SB();
    lds_rd128<32768>(fb[8], wa0_0); SB();
    mmw<6>(ac[0][0][0], fb[1], fb[0]); SB();
    mm(ac[0][0][1], fb[1], fb[2]); SB();
    lds_rd128<20480>(fb[1], xa_1); SB();
    mmw<5>(ac[0][1][1], fb[3], fb[2]); SB();
    lds_rd128<34816>(fb[2], wa0_0); SB();
    mm(ac[0][1][0], fb[3], fb[0]); SB();
    lds_rd128<21504>(fb[0], xa_1); SB();
    mmw<5>(ac[0][0][0], fb[5], fb[4]); SB();
    lds_rd128<32768>(fb[3], wa1_0); SB();
    mm(ac[0][0][1], fb[5], fb[6]); SB();
    lds_rd128<22528>(fb[5], xa_1); SB();
    mmw<6>(ac[0][1][1], fb[7], fb[6]); SB();
    lds_rd128<34816>(fb[6], wa1_0); SB();
    mm(ac[0][1][0], fb[7], fb[4]); SB();
    lds_rd128<23552>(fb[4], xa_1); SB();
    mmw<6>(ac[0][0][0], fb[1], fb[8]); SB();
    lds_rd128<24576>(fb[7], xa_1); SB();
    mmw<6>(ac[0][0][1], fb[1], fb[2]); SB();
    lds_rd128<25600>(fb[1], xa_1); SB();
    mmw<6>(ac[0][1][1], fb[0], fb[2]); SB();
    lds_rd128<26624>(fb[2], xa_1); SB();
    mm(ac[0][1][0], fb[0], fb[8]); SB();
    lds_rd128<27648>(fb[0], xa_1); SB();
    mmw<6>(ac[0][0][0], fb[5], fb[3]); SB();
    lds_rd128<28672>(fb[8], xa_1); SB();
    mmw<6>(ac[0][0][1], fb[5], fb[6]); SB();
    mmw<5>(ac[0][1][1], fb[4], fb[6]); SB();
    lds_rd128<29696>(fb[5], xa_1); SB();
    lds_rd32(fl, lf_rd); SB();
    mm(ac[0][1][0], fb[4], fb[3]); SB();
    mmw<6>(ac[1][0][0], fb[7], wr[8][0]); SB();
    lds_rd128<30720>(fb[6], xa_1); SB();
    mm(ac[1][0][1], fb[7], wr[8][1]); SB();
    mmw<6>(ac[1][1][1], fb[1], wr[8][1]); SB();
    lds_rd128<31744>(fb[3], xa_1); SB();
    mm(ac[1][1][0], fb[1], wr[8][0]); SB();
    EM0(0, 0); SB();
    mmchkw<25, 2>(ac[1][0][0], fb[2], wr[8][2], fl, lf_rd, bud); SB();
    lds_rd128<0>(fb[4], xa_0); SB();
    EM0(0, 1); SB();
    mm(ac[1][0][1], fb[2], wr[8][3]); SB();
    EM1(0, 0, 3, 4); SB();
    EM1(0, 1, 11, 12); SB();
    EM1(0, 0, 5, 6); SB();
    mm(ac[1][1][1], fb[0], wr[8][3]); SB();
    lds_rd128<1024>(fb[7], xa_0); SB();
    EM1(0, 1, 13, 14); SB();
    EM1(0, 0, 7, 15); SB();
    mm(ac[1][1][0], fb[0], wr[8][2]); SB();
    EK(0); SB();
    EM2(); SB();
    EK(1); SB();
    mm(ac[1][0][0], fb[8], wr[9][0]); SB();
    lds_rd128<2048>(fb[1], xa_0); SB();
    ES0(); SB();
    ES1(); SB();
    mm(ac[1][0][1], fb[8], wr[9][1]); SB();
    mm(ac[1][1][1], fb[5], wr[9][1]); SB();
    lds_rd128<3072>(fb[2], xa_0); SB();
    EX(0); SB();
    EH(0, 0); SB();
    mm(ac[1][1][0], fb[5], wr[9][0]); SB();
    EH(0, 1); SB();
    EH(0, 2); SB();
    mmw<4>(ac[1][0][0], fb[6], wr[9][2]); SB();
    lds_rd128<4096>(fb[0], xa_0); SB();
    EH(0, 3); SB();
    ET(0); SB();
    mm(ac[1][0][1], fb[6], wr[9][3]); SB();
    ET(1); SB();
    ET(2); SB();
    EU(3); SB();
    mm(ac[1][1][1], fb[3], wr[9][3]); SB();
    lds_rd128<5120>(fb[8], xa_0); SB();
    mm(ac[1][1][0], fb[3], wr[9][2]); SB();
    SEND(0, 0); SB();
    mmw<4>(ac[1][0][0], fb[4], wr[10][0]); SB();
    lds_rd128<6144>(fb[5], xa_0); SB();
    EH(0, 4); SB();
    EH(0, 5); SB();
    mm(ac[1][0][1], fb[4], wr[10][1]); SB();
    EH(0, 6); SB();
    EH(0, 7); SB();
    ET(4); SB();
    mm(ac[1][1][1], fb[7], wr[10][1]); SB();
    lds_rd128<7168>(fb[6], xa_0); SB();
    ET(5); SB();
    ET(6); SB();
    mm(ac[1][1][0], fb[7], wr[10][0]); SB();
    EU(7); SB();
    mmw<4>(ac[1][0][0], fb[1], wr[10][2]); SB();
    lds_rd128<8192>(fb[3], xa_0); SB();
    SEND(0, 1); SB();
    mm(ac[1][0][1], fb[1], wr[10][3]); SB();
    mm(ac[1][1][1], fb[2], wr[10][3]); SB();
    lds_rd128<9216>(fb[4], xa_0); SB();
    mm(ac[1][1][0], fb[2], wr[10][2]); SB();
    mmw<4>(ac[1][0][0], fb[0], wr[11][0]); SB();
    lds_rd128<10240>(fb[7], xa_0); SB();
    mm(ac[1][0][1], fb[0], wr[11][1]); SB();
    mm(ac[1][1][1], fb[8], wr[11][1]); SB();
    lds_rd128<11264>(fb[1], xa_0); SB();
    mm(ac[1][1][0], fb[8], wr[11][0]); SB();
    mmw<4>(ac[1][0][0], fb[5], wr[11][2]); SB();
    lds_rd128<12288>(fb[2], xa_0); SB();
    mm(ac[1][0][1], fb[5], wr[11][3]); SB();
    mm(ac[1][1][1], fb[6], wr[11][3]); SB();
    lds_rd128<13312>(fb[0], xa_0); SB();
    mm(ac[1][1][0], fb[6], wr[11][2]); SB();
    lds_rd32(fl, lf_rd); SB();
    mmw<5>(ac[1][0][0], fb[3], wr[12][0]); SB();
    lds_rd128<14336>(fb[8], xa_0); SB();
    mm(ac[1][0][1], fb[3], wr[12][1]); SB();
    mm(ac[1][1][1], fb[4], wr[12][1]); SB();
    lds_rd128<15360>(fb[5], xa_0); SB();
    mm(ac[1][1][0], fb[4], wr[12][0]); SB();
    mmw<5>(ac[1][0][0], fb[7], wr[12][2]); SB();
    lds_rd128<0>(fb[6], wa0_1); SB();
    mmchkw<26, 3>(ac[1][0][1], fb[7], wr[12][3], fl, lf_rd, bud); SB();
    lds_rd128<0>(fb[3], xa_2); SB();
    mm(ac[1][1][1], fb[1], wr[12][3]); SB();
    lds_rd128<2048>(fb[4], wa0_1); SB();
    mm(ac[1][1][0], fb[1], wr[12][2]); SB();
    lds_rd128<1024>(fb[7], xa_2); SB();
    mm(ac[1][0][0], fb[2], wr[13][0]); SB();
    lds_rd128<0>(fb[1], wa1_1); SB();
    mm(ac[1][0][1], fb[2], wr[13][1]); SB();
    lds_rd128<2048>(fb[2], xa_2); SB();
    mm(ac[1][1][1], fb[0], wr[13][1]); SB();
    mm(ac[1][1][0], fb[0], wr[13][0]); SB();
    lds_rd128<2048>(fb[0], wa1_1); SB();
    mmw<7>(ac[1][0][0], fb[8], wr[13][2]); SB();
    mm(ac[1][0][1], fb[8], wr[13][3]); SB();
    lds_rd128<3072>(fb[8], xa_2); SB();
    mm(ac[1][1][1], fb[5], wr[13][3]); SB();
    mm(ac[1][1][0], fb[5], wr[13][2]); SB();
    lds_rd128<32768>(fb[5], wa0_0); SB();
    mmw<6>(ac[1][0][0], fb[3], fb[6]); SB();
    mm(ac[1][0][1], fb[3], fb[4]); SB();
    lds_rd128<4096>(fb[3], xa_2); SB();
    mmw<5>(ac[1][1][1], fb[7], fb[4]); SB();
    lds_rd128<34816>(fb[4], wa0_0); SB();
    mm(ac[1][1][0], fb[7], fb[6]); SB();
    lds_rd128<5120>(fb[6], xa_2); SB();
    mmw<5>(ac[1][0][0], fb[2], fb[1]); SB();
    lds_rd128<32768>(fb[7], wa1_0); SB();
    mm(ac[1][0][1], fb[2], fb[0]); SB();
    lds_rd128<6144>(fb[2], xa_2); SB();
    mmw<6>(ac[1][1][1], fb[8], fb[0]); SB();
    lds_rd128<34816>(fb[0], wa1_0); SB();
    mm(ac[1][1][0], fb[8], fb[1]); SB();
    lds_rd128<7168>(fb[1], xa_2); SB();
    mmw<6>(ac[1][0][0], fb[3], fb[5]); SB();
    lds_rd128<8192>(fb[8], xa_2); SB();
    mmw<6>(ac[1][0][1], fb[3], fb[4]); SB();
    lds_rd128<9216>(fb[3], xa_2); SB();
    mmw<6>(ac[1][1][1], fb[6], fb[4]); SB();
    lds_rd128<10240>(fb[4], xa_2); SB();
    mm(ac[1][1][0], fb[6], fb[5]); SB();
    lds_rd128<11264>(fb[5], xa_2); SB();
    mmw<6>(ac[1][0][0], fb[2], fb[7]); SB();
    lds_rd128<12288>(fb[6], xa_2); SB();
    mmw<6>(ac[1][0][1], fb[2], fb[0]); SB();
    mmw<5>(ac[1][1][1], fb[1], fb[0]); SB();
    lds_rd128<13312>(fb[2], xa_2); SB();
    lds_rd32(fl, lf_rd); SB();
    mm(ac[1][1][0], fb[1], fb[7]); SB();
    mmw<6>(ac[2][0][0], fb[8], wr[8][0]); SB();
    lds_rd128<14336>(fb[0], xa_2); SB();
    mm(ac[2][0][1], fb[8], wr[8][1]); SB();
    mmw<6>(ac[2][1][1], fb[3], wr[8][1]); SB();
    lds_rd128<15360>(fb[1], xa_2); SB();
    mm(ac[2][1][0], fb[3], wr[8][0]); SB();
    EM0(1, 0); SB();
    mmchkw<27, 2>(ac[2][0][0], fb[4], wr[8][2], fl, lf_rd, bud); SB();
    lds_rd128<32768>(fb[7], xa_1); SB();
    EM0(1, 1); SB();
    mm(ac[2][0][1], fb[4], wr[8][3]); SB();
    EM1(1, 0, 3, 4); SB();
    EM1(1, 1, 11, 12); SB();
    EM1(1, 0, 5, 6); SB();
    mm(ac[2][1][1], fb[5], wr[8][3]); SB();
    lds_rd128<33792>(fb[8], xa_1); SB();
    EM1(1, 1, 13, 14); SB();
    EM1(1, 0, 7, 15); SB();
    mm(ac[2][1][0], fb[5], wr[8][2]); SB();
    EK(0); SB();
    EM2(); SB();
    EK(1); SB();
    mm(ac[2][0][0], fb[6], wr[9][0]); SB();
    lds_rd128<34816>(fb[3], xa_1); SB();
    ES0(); SB();
    ES1(); SB();
    mm(ac[2][0][1], fb[6], wr[9][1]); SB();
    mm(ac[2][1][1], fb[2], wr[9][1]); SB();
    lds_rd128<35840>(fb[4], xa_1); SB();
    EX(1); SB();
    EH(1, 0); SB();
    mm(ac[2][1][0], fb[2], wr[9][0]); SB();
    EH(1, 1); SB();
    EH(1, 2); SB();
    mmw<4>(ac[2][0][0], fb[0], wr[9][2]); SB();
    lds_rd128<36864>(fb[5], xa_1); SB();
    EH(1, 3); SB();
    ET(0); SB();
    mm(ac[2][0][1], fb[0], wr[9][3]); SB();
    ET(1); SB();
    ET(2); SB();
    EU(3); SB();
    mm(ac[2][1][1], fb[1], wr[9][3]); SB();
    lds_rd128<37888>(fb[6], xa_1); SB();
    mm(ac[2][1][0], fb[1], wr[9][2]); SB();
    SEND(1, 0); SB();
    mmw<4>(ac[2][0][0], fb[7], wr[10][0]); SB();
    lds_rd128<38912>(fb[2], xa_1); SB();
    EH(1, 4); SB();
    EH(1, 5); SB();
    mm(ac[2][0][1], fb[7], wr[10][1]); SB();
    EH(1, 6); SB();
    EH(1, 7); SB();
    ET(4); SB();
    mm(ac[2][1][1], fb[8], wr[10][1]); SB();
    lds_rd128<39936>(fb[0], xa_1); SB();
    ET(5); SB();
    ET(6); SB();
    mm(ac[2][1][0], fb[8], wr[10][0]); SB();
    EU(7); SB();
    mmw<4>(ac[2][0][0], fb[3], wr[10][2]); SB();
    lds_rd128<40960>(fb[1], xa_1); SB();
    SEND(1, 1); SB();
    mm(ac[2][0][1], fb[3], wr[10][3]); SB();
    mm(ac[2][1][1], fb[4], wr[10][3]); SB();
    lds_rd128<41984>(fb[7], xa_1); SB();
    mm(ac[2][1][0], fb[4], wr[10][2]); SB();
    mmw<4>(ac[2][0][0], fb[5], wr[11][0]); SB();
    lds_rd128<43008>(fb[8], xa_1); SB();
    mm(ac[2][0][1], fb[5], wr[11][1]); SB();
    mm(ac[2][1][1], fb[6], wr[11][1]); SB();
    lds_rd128<44032>(fb[3], xa_1); SB();
    mm(ac[2][1][0], fb[6], wr[11][0]); SB();
    mmw<4>(ac[2][0][0], fb[2], wr[11][2]); SB();
    lds_rd128<45056>(fb[4], xa_1); SB();
    mm(ac[2][0][1], fb[2], wr[11][3]); SB();
    mm(ac[2][1][1], fb[0], wr[11][3]); SB();
    lds_rd128<46080>(fb[5], xa_1); SB();
    mm(ac[2][1][0], fb[0], wr[11][2]); SB();
    lds_rd32(fl, lf_rd); SB();
    mmw<5>(ac[2][0][0], fb[1], wr[12][0]); SB();
    lds_rd128<47104>(fb[6], xa_1); SB();
    mm(ac[2][0][1], fb[1], wr[12][1]); SB();
    mm(ac[2][1][1], fb[7], wr[12][1]); SB();
    lds_rd128<48128>(fb[2], xa_1); SB();
    mm(ac[2][1][0], fb[7], wr[12][0]); SB();
    mmw<5>(ac[2][0][0], fb[8], wr[12][2]); SB();
    lds_rd128<0>(fb[0], wa0_1); SB();
    mmchkw<28, 3>(ac[2][0][1], fb[8], wr[12][3], fl, lf_rd, bud); SB();
    lds_rd128<49152>(fb[1], xa_0); SB();
    mm(ac[2][1][1], fb[3], wr[12][3]); SB();
    lds_rd128<2048>(fb[7], wa0_1); SB();
    mm(ac[2][1][0], fb[3], wr[12][2]); SB();
    lds_rd128<50176>(fb[8], xa_0); SB();
    mm(ac[2][0][0], fb[4], wr[13][0]); SB();
    lds_rd128<0>(fb[3], wa1_1); SB();
    mm(ac[2][0][1], fb[4], wr[13][1]); SB();
    lds_rd128<51200>(fb[4], xa_0); SB();
    mm(ac[2][1][1], fb[5], wr[13][1]); SB();
    mm(ac[2][1][0], fb[5], wr[13][0]); SB();
    lds_rd128<2048>(fb[5], wa1_1); SB();
    mmw<7>(ac[2][0][0], fb[6], wr[13][2]); SB();
    mm(ac[2][0][1], fb[6], wr[13][3]); SB();
    lds_rd128<52224>(fb[6], xa_0); SB();
    mm(ac[2][1][1], fb[2], wr[13][3]); SB();
    mm(ac[2][1][0], fb[2], wr[13][2]); SB();
    lds_rd128<32768>(fb[2], wa0_0); SB();
    mmw<6>(ac[2][0][0], fb[1], fb[0]); SB();
    mm(ac[2][0][1], fb[1], fb[7]); SB();
    lds_rd128<53248>(fb[1], xa_0); SB();
    mmw<5>(ac[2][1][1], fb[8], fb[7]); SB();
    lds_rd128<34816>(fb[7], wa0_0); SB();
    mm(ac[2][1][0], fb[8], fb[0]); SB();
    lds_rd128<54272>(fb[0], xa_0); SB();
    mmw<5>(ac[2][0][0], fb[4], fb[3]); SB();
    lds_rd128<32768>(fb[8], wa1_0); SB();
    mm(ac[2][0][1], fb[4], fb[5]); SB();
    lds_rd128<55296>(fb[4], xa_0); SB();
    mmw<6>(ac[2][1][1], fb[6], fb[5]); SB();
    lds_rd128<34816>(fb[5], wa1_0); SB();
    mm(ac[2][1][0], fb[6], fb[3]); SB();
    lds_rd128<56320>(fb[3], xa_0); SB();
    mmw<6>(ac[2][0][0], fb[1], fb[2]); SB();
    lds_rd128<57344>(fb[6], xa_0); SB();
    mmw<6>(ac[2][0][1], fb[1], fb[7]); SB();
    lds_rd128<58368>(fb[1], xa_0); SB();
    mmw<6>(ac[2][1][1], fb[0], fb[7]); SB();
    lds_rd128<59392>(fb[7], xa_0); SB();
    mm(ac[2][1][0], fb[0], fb[2]); SB();
    lds_rd128<60416>(fb[0], xa_0); SB();
    mmw<6>(ac[2][0][0], fb[4], fb[8]); SB();
    lds_rd128<61440>(fb[2], xa_0); SB();
    mmw<6>(ac[2][0][1], fb[4], fb[5]); SB();
    mmw<5>(ac[2][1][1], fb[3], fb[5]); SB();
    lds_rd128<62464>(fb[4], xa_0); SB();
    lds_rd32(fl, lf_rd); SB();
    mm(ac[2][1][0], fb[3], fb[8]); SB();
    mmw<6>(ac[3][0][0], fb[6], wr[6][0]); SB();
    lds_rd128<63488>(fb[5], xa_0); SB();
    mm(ac[3][0][1], fb[6], wr[6][1]); SB();
    mmw<6>(ac[3][1][1], fb[1], wr[6][1]); SB();
    lds_rd128<64512>(fb[3], xa_0); SB();
    mm(ac[3][1][0], fb[1], wr[6][0]); SB();
    EM0(2, 0); SB();
    EM0(2, 1); SB();
    mmchkw<29, 2>(ac[3][0][0], fb[7], wr[6][2], fl, lf_rd, bud); SB();
    lds_rd128<16384>(fb[8], xa_0); SB();
    EM1(2, 0, 3, 4); SB();
    EM1(2, 1, 11, 12); SB();
    mm(ac[3][0][1], fb[7], wr[6][3]); SB();
    EM1(2, 0, 5, 6); SB();
    EM1(2, 1, 13, 14); SB();
    EM1(2, 0, 7, 15); SB();
    EK(0); SB();
    mm(ac[3][1][1], fb[0], wr[6][3]); SB();
    lds_rd128<17408>(fb[6], xa_0); SB();
    EM2(); SB();
    EK(1); SB();
    ES0(); SB();
    mm(ac[3][1][0], fb[0], wr[6][2]); SB();
    ES1(); SB();
    mm(ac[3][0][0], fb[2], wr[7][0]); SB();
    lds_rd128<18432>(fb[1], xa_0); SB();
    EX(2); SB();
    EH(2, 0); SB();
    EH(2, 1); SB();
    mm(ac[3][0][1], fb[2], wr[7][1]); SB();
    EH(2, 2); SB();
    EH(2, 3); SB();
    ET(0); SB();
    ET(1); SB();
    mm(ac[3][1][1], fb[4], wr[7][1]); SB();
    lds_rd128<19456>(fb[7], xa_0); SB();
    ET(2); SB();
    EU(3); SB();
    mm(ac[3][1][0], fb[4], wr[7][0]); SB();
    SEND(2, 0); SB();
    mmw<4>(ac[3][0][0], fb[5], wr[7][2]); SB();
    lds_rd128<20480>(fb[0], xa_0); SB();
    EH(2, 4); SB();
    EH(2, 5); SB();
    EH(2, 6); SB();
    mm(ac[3][0][1], fb[5], wr[7][3]); SB();
    EH(2, 7); SB();
    ET(4); SB();
    ET(5); SB();
    ET(6); SB();
    mm(ac[3][1][1], fb[3], wr[7][3]); SB();
    lds_rd128<21504>(fb[2], xa_0); SB();
    EU(7); SB();
    mm(ac[3][1][0], fb[3], wr[7][2]); SB();
    SEND(2, 1); SB();
    mmw<4>(ac[3][0][0], fb[8], wr[8][0]); SB();
    lds_rd128<22528>(fb[4], xa_0); SB();
    mm(ac[3][0][1], fb[8], wr[8][1]); SB();
    mm(ac[3][1][1], fb[6], wr[8][1]); SB();
    lds_rd128<23552>(fb[5], xa_0); SB();
    mm(ac[3][1][0], fb[6], wr[8][0]); SB();
    mmw<4>(ac[3][0][0], fb[1], wr[8][2]); SB();
    lds_rd128<24576>(fb[3], xa_0); SB();
    mm(ac[3][0][1], fb[1], wr[8][3]); SB();
    mm(ac[3][1][1], fb[7], wr[8][3]); SB();
    lds_rd128<25600>(fb[8], xa_0); SB();
    mm(ac[3][1][0], fb[7], wr[8][2]); SB();
    mmw<4>(ac[3][0][0], fb[0], wr[9][0]); SB();
    lds_rd128<26624>(fb[6], xa_0); SB();
    mm(ac[3][0][1], fb[0], wr[9][1]); SB();
    mm(ac[3][1][1], fb[2], wr[9][1]); SB();
    lds_rd128<27648>(fb[1], xa_0); SB();
    mm(ac[3][1][0], fb[2], wr[9][0]); SB();
    mmw<4>(ac[3][0][0], fb[4], wr[9][2]); SB();
    lds_rd128<28672>(fb[7], xa_0); SB();
    mm(ac[3][0][1], fb[4], wr[9][3]); SB();
    mm(ac[3][1][1], fb[5], wr[9][3]); SB();
    lds_rd128<29696>(fb[0], xa_0); SB();
    lds_rd32(fl, lf_rd); SB();
    mm(ac[3][1][0], fb[5], wr[9][2]); SB();
    mmw<5>(ac[3][0][0], fb[3], wr[10][0]); SB();
    lds_rd128<30720>(fb[2], xa_0); SB();
    mm(ac[3][0][1], fb[3], wr[10][1]); SB();
    mm(ac[3][1][1], fb[8], wr[10][1]); SB();
    lds_rd128<31744>(fb[4], xa_0); SB();
    mm(ac[3][1][0], fb[8], wr[10][0]); SB();
    mmchkw<30, 2>(ac[3][0][0], fb[6], wr[10][2], fl, lf_rd, bud); SB();
    lds_rd128<49152>(fb[5], xa_1); SB();
    mm(ac[3][0][1], fb[6], wr[10][3]); SB();
    mm(ac[3][1][1], fb[1], wr[10][3]); SB();
    lds_rd128<50176>(fb[3], xa_1); SB();
    mm(ac[3][1][0], fb[1], wr[10][2]); SB();
    mm(ac[3][0][0], fb[7], wr[11][0]); SB();
    lds_rd128<51200>(fb[8], xa_1); SB();
    mm(ac[3][0][1], fb[7], wr[11][1]); SB();
    mm(ac[3][1][1], fb[0], wr[11][1]); SB();
    lds_rd128<52224>(fb[6], xa_1); SB();
    mm(ac[3][1][0], fb[0], wr[11][0]); SB();
    mmw<4>(ac[3][0][0], fb[2], wr[11][2]); SB();
    lds_rd128<53248>(fb[1], xa_1); SB();
    mm(ac[3][0][1], fb[2], wr[11][3]); SB();
    mm(ac[3][1][1], fb[4], wr[11][3]); SB();
    lds_rd128<54272>(fb[7], xa_1); SB();
    mm(ac[3][1][0], fb[4], wr[11][2]); SB();
    mmw<4>(ac[3][0][0], fb[5], wr[12][0]); SB();
    lds_rd128<55296>(fb[0], xa_1); SB();
    RV_LD(0, ""); SB();
    mm(ac[3][0][1], fb[5], wr[12][1]); SB();
    mm(ac[3][1][1], fb[3], wr[12][1]); SB();
    lds_rd128<56320>(fb[2], xa_1); SB();
    mm(ac[3][1][0], fb[3], wr[12][0]); SB();
    mmw<4>(ac[3][0][0], fb[8], wr[12][2]); SB();
    lds_rd128<0>(fb[4], wa0_1); SB();
    mm(ac[3][0][1], fb[8], wr[12][3]); SB();
    lds_rd128<57344>(fb[5], xa_1); SB();
    mm(ac[3][1][1], fb[6], wr[12][3]); SB();
    lds_rd128<2048>(fb[3], wa0_1); SB();
    mm(ac[3][1][0], fb[6], wr[12][2]); SB();
    lds_rd128<58368>(fb[8], xa_1); SB();
    mmw<6>(ac[3][0][0], fb[1], wr[13][0]); SB();
    lds_rd128<0>(fb[6], wa1_1); SB();
    mm(ac[3][0][1], fb[1], wr[13][1]); SB();
    lds_rd128<59392>(fb[1], xa_1); SB();
    mm(ac[3][1][1], fb[7], wr[13][1]); SB();
    mm(ac[3][1][0], fb[7], wr[13][0]); SB();
    lds_rd128<2048>(fb[7], wa1_1); SB();
    mmw<7>(ac[3][0][0], fb[0], wr[13][2]); SB();
    mm(ac[3][0][1], fb[0], wr[13][3]); SB();
    lds_rd128<60416>(fb[0], xa_1); SB();
    mm(ac[3][1][1], fb[2], wr[13][3]); SB();
    lds_rd32(f2, d2_rd); SB();
    mm(ac[3][1][0], fb[2], wr[13][2]); SB();
    lds_rd128<32768>(fb[2], wa0_0); SB();
    mmw<7>(ac[3][0][0], fb[5], fb[4]); SB();
    RV_LD(1, ""); SB();
    mm(ac[3][0][1], fb[5], fb[3]); SB();
    lds_rd128<61440>(fb[5], xa_1); SB();
    mmw<6>(ac[3][1][1], fb[8], fb[3]); SB();
    lds_rd128<34816>(fb[3], wa0_0); SB();
    mm(ac[3][1][0], fb[8], fb[4]); SB();
    lds_rd128<62464>(fb[4], xa_1); SB();
    mmw<6>(ac[3][0][0], fb[1], fb[6]); SB();
    lds_rd128<32768>(fb[8], wa1_0); SB();
    asm volatile("s_waitcnt lgkmcnt(5)" : "+v"(fb[0]), "+v"(f2)::"memory"); SB();
    chk<1>(f2, d2_rd, bud); SB();
    D_RD(0); SB();
    D_RD(1); SB();
    D_RD(2); SB();
    D_RD(3); SB();
    G_RD(0); SB();
    G_RD(1); SB();
    mm(ac[3][0][1], fb[1], fb[7]); SB();
    lds_rd128<63488>(fb[1], xa_1); SB();
    mm(ac[3][1][1], fb[0], fb[7]); SB();
    lds_rd128<34816>(fb[7], wa1_0); SB();
    mm(ac[3][1][0], fb[0], fb[6]); SB();
    lds_rd128<64512>(fb[0], xa_1); SB();
    RV_LD(2, ""); SB();
    mmw<12>(ac[3][0][0], fb[5], fb[2]); SB();
    mmw<11>(ac[3][0][1], fb[5], fb[3]); SB();
    mmw<10>(ac[3][1][1], fb[4], fb[3]); SB();
    mm(ac[3][1][0], fb[4], fb[2]); SB();
    mmw<2>(ac[3][0][0], fb[1], fb[8]); SB();
    mmw<1>(ac[3][0][1], fb[1], fb[7]); SB();
    mmw<0>(ac[3][1][1], fb[0], fb[7]); SB();
    mm(ac[3][1][0], fb[0], fb[8]); SB();
    asm volatile("s_nop 7" : "+v"(ac[3][0][0]), "+v"(ac[3][0][1]), "+v"(ac[3][1][0]), "+v"(ac[3][1][1])::"memory");
    const float om[2] = {rbf(1.0f - gs[0]), rbf(1.0f - gs[1])};
    gs[0] = h ? -gs[0] : gs[0];
    gs[1] = h ? -gs[1] : gs[1];
    float y[16];
#pragma unroll
    for (int p = 0; p < 8; ++p) {
      const unsigned o = dq[p >> 1][p & 1];
      const f32x2 v = f32x2{AV(3, 2 * p), AV(3, 2 * p + 1)} * gs[p >> 2] + f32x2{lo2f(o), hi2f(o)} * om[p >> 2];
      y[2 * p] = v[0];
      y[2 * p + 1] = v[1];
    }
    asm volatile("" : "+v"(oo), "+s"(out), "+v"(gs[0]), "+v"(gs[1]));
    auto strip = [&](u32x4 (&r)[2]) {
      const u32x4 t = {tb[0] ^ 0x40004u, tb[1] ^ 0x40004u, tb[2] ^ 0x40004u, tb[3] ^ 0x40004u};
      r[0] ^= t;
      r[1] ^= t;
      const unsigned o = ((r[0].x | r[0].y | r[0].z | r[1].x | r[1].y | r[1].z) & 0x30003u) | ((r[0].w | r[1].w) & 0x30000u);
      const unsigned a = r[0].x & r[0].y & r[0].z & r[0].w & r[1].x & r[1].y & r[1].z & r[1].w & 0x40004u;
      return (o | a) == 0x40004u;
    };
#define RV_FIN(S, Y)                                                                                                     \
  asm volatile("s_waitcnt vmcnt(%18)" : "+v"(rv[S][0]), "+v"(rv[S][1]), "+v"(y[0]), "+v"(y[1]), "+v"(y[2]), "+v"(y[3]), "+v"(y[4]), "+v"(y[5]), \
               "+v"(y[6]), "+v"(y[7]), "+v"(y[8]), "+v"(y[9]), "+v"(y[10]), "+v"(y[11]), "+v"(y[12]), "+v"(y[13]), "+v"(y[14]), "+v"(y[15])     \
               : "i"(Y) : "memory");                                                                                     \
  if (__builtin_expect(!__all(strip(rv[S])), 0)) {                                                                       \
    _Pragma("clang loop unroll(disable)") for (int it = 0; it < SPIN_MAX; ++it) {                                        \
      RV_LD(S, "\n\ts_waitcnt vmcnt(0)");                                                                                \
      if (__all(strip(rv[S]))) break;                                                                                    \
    }                                                                                                                    \
  }                                                                                                                      \
  {                                                                                                                      \
    const float sc = __uint_as_float((((rv[S][0].w & 3u) | (rv[S][1].w & 3u) << 2) << 27) + ((S) == 2 ? 0xff800000u : 0x7f800000u)); \
    rv[S][0].w &= ~3u;                                                                                                   \
    rv[S][1].w &= ~3u;                                                                                                   \
    _Pragma("unroll") for (int p = 0; p < 8; ++p) {                                                                      \
      const float f = sc * gs[p >> 2];                                                                                   \
      unsigned o = rv[S][p >> 2][p & 3];                                                                                 \
      asm("v_fma_mix_f32 %0, %2, %3, %0 op_sel_hi:[1,0,0]\n\tv_fma_mix_f32 %1, %2, %3, %1 op_sel:[1,0,0] op_sel_hi:[1,0,0]" \
          : "+v"(y[2 * p]), "+v"(y[2 * p + 1]) : "v"(o), "v"(f));                                                        \
    }                                                                                                                    \
  }
    RV_FIN(0, 4);
    RV_FIN(1, 2);
    RV_FIN(2, 0);
    const unsigned idx = (4095u - (__builtin_amdgcn_readfirstlane(f2) & 0xfffu)) & (S_ - 1);
    if (__builtin_expect(idx >> 5 == (unsigned)(ti * 4 + wid), 0)) {
      SPIN(d_rd, 1);
      u32x2 dn_[2];
      asm volatile("ds_read_b64 %0, %2\n\tds_read_b64 %1, %2 offset:32\n\ts_waitcnt lgkmcnt(0)" : "=&v"(dn_[0]), "=&v"(dn_[1]) : "v"(sb + RING + L_UPD + fq * 8) : "memory");
#pragma unroll
      for (int p = 0; p < 8; ++p)
        if ((idx & 31u) == (unsigned)((p >> 2) * 16 + fr)) {
          const unsigned o = dq[p >> 1][p & 1], n = dn_[p >> 1 & 1][p & 1];
          y[2 * p] += om[p >> 2] * (lo2f(n) - lo2f(o));
          y[2 * p + 1] += om[p >> 2] * (hi2f(n) - hi2f(o));
        }
    }
#pragma unroll
    for (int mi = 0; mi < 2; ++mi) {
      const float* z = y + mi * 8;
      const auto s0 = __builtin_amdgcn_permlane16_swap(pk2(z[0], z[1]), pk2(z[4], z[5]), false, false);
      const auto s1 = __builtin_amdgcn_permlane16_swap(pk2(z[2], z[3]), pk2(z[6], z[7]), false, false);
      asm volatile("global_store_dwordx4 %0, %1, %2\n\ts_nop 1" ::"v"(oo + mi * (16u * D_ * 2)), "v"(u32x4{s0[0], s1[0], s0[1], s1[1]}), "s"(out) : "memory");
    }
  }
}

extern "C" void kernel_launch(void* const* d_in, const int* in_sizes, int n_in, void* d_out, int out_size,
                              void* d_ws, size_t ws_size, hipStream_t stream) {
  const u16* x = (const u16*)d_in[0];
  const u16* wsre = (const u16*)d_in[1];
  const u16* wsim = (const u16*)d_in[2];
  const u16* wdre = (const u16*)d_in[3];
  const u16* wdim = (const u16*)d_in[4];
  const u16* w_r = (const u16*)d_in[6];
  const u16* w_gate = (const u16*)d_in[7];
  const u16* w_up = (const u16*)d_in[8];
  const u16* w_down = (const u16*)d_in[9];
  const u16* gate_w = (const u16*)d_in[10];
  u16* out = (u16*)d_out;
  fused_k<<<256, 512, 0, stream>>>(wsre, wsim, x, w_r, gate_w, wdre, wdim, w_gate, w_up, w_down, (char*)d_ws, out);
}
```

```cpp
#include <hip/hip_runtime.h>
#include <hip/hip_bf16.h>
#include <stdint.h>
#include <type_traits>

using bf16 = __hip_bfloat16;
using bf16x8 = __attribute__((ext_vector_type(8))) short;
using f32x4 = __attribute__((ext_vector_type(4))) float;
using f32x2 = __attribute__((ext_vector_type(2))) float;
using u32x4 = __attribute__((ext_vector_type(4))) unsigned;
using u32x2 = __attribute__((ext_vector_type(2))) unsigned;
typedef __attribute__((ext_vector_type(2))) __bf16 bf16x2;
typedef unsigned short u16;
typedef unsigned long long u64;
typedef __attribute__((address_space(3))) unsigned lds_u32;

constexpr int S_ = 2048, D_ = 512, F_ = 1792;

__device__ __forceinline__ float bf2f(u16 v) { return __uint_as_float(((unsigned)v) << 16); }
__device__ __forceinline__ u16 f2bf(float f) {
  bf16 h = __float2bfloat16(f);
  return *reinterpret_cast<u16*>(&h);
}
__device__ __forceinline__ float rbf(float f) { return bf2f(f2bf(f)); }
__device__ __forceinline__ unsigned pk2(float a, float b) {
  return __builtin_bit_cast(unsigned, __builtin_convertvector(f32x2{a, b}, bf16x2));
}
__device__ __forceinline__ float lo2f(unsigned v) { return __uint_as_float(v << 16); }
__device__ __forceinline__ float hi2f(unsigned v) { return __uint_as_float(v & 0xffff0000u); }
__device__ __forceinline__ float sigmoidf_(float v) { return 1.0f / (1.0f + __expf(-v)); }

template <int CTRL>
__device__ __forceinline__ unsigned dpp(unsigned v) { return __builtin_amdgcn_update_dpp(0, v, CTRL, 0xf, 0xf, false); }
template <int CTRL>
__device__ __forceinline__ float dpp_add(float v) { return v + __uint_as_float(dpp<CTRL>(__float_as_uint(v))); }
constexpr int DPP_X1 = 0xb1, DPP_X2 = 0x4e, DPP_HM = 0x141, DPP_M = 0x140;
__device__ __forceinline__ unsigned row_max(unsigned v) {
  v = max(v, dpp<DPP_X1>(v));
  v = max(v, dpp<DPP_X2>(v));
  v = max(v, dpp<DPP_HM>(v));
  return max(v, dpp<DPP_M>(v));
}
__device__ __forceinline__ float sw32(float a, float b) {
  const auto r = __builtin_amdgcn_permlane32_swap(__float_as_uint(a), __float_as_uint(b), false, false);
  return __uint_as_float(r[0]) + __uint_as_float(r[1]);
}
__device__ __forceinline__ float sw16(float a, float b) {
  const auto r = __builtin_amdgcn_permlane16_swap(__float_as_uint(a), __float_as_uint(b), false, false);
  return __uint_as_float(r[0]) + __uint_as_float(r[1]);
}
__device__ __forceinline__ unsigned wave_max(unsigned v) {
  v = row_max(v);
  const auto r = __builtin_amdgcn_permlane16_swap(v, v, false, false);
  v = max(r[0], r[1]);
  const auto q = __builtin_amdgcn_permlane32_swap(v, v, false, false);
  return max(q[0], q[1]);
}

#define WAIT_V(n) asm volatile("s_waitcnt vmcnt(%0)" ::"i"(n) : "memory")
#define WAIT_L(n) asm volatile("s_waitcnt lgkmcnt(%0)" ::"i"(n) : "memory")
#define SB() __builtin_amdgcn_sched_barrier(0)

constexpr int A_B = 128 * 64 * 2;
constexpr int RING = 155648;
constexpr int SCR = 8192;
constexpr int SPIN_MAX = 1 << 16;
constexpr int L_LF = 0;
constexpr int L_CP = 1024;
constexpr int L_RG = 2048;
constexpr int L_G = 4096;
constexpr int L_FLAG = 256;
constexpr int L_H = 4608;
constexpr int L_UPD = 4736;
constexpr int L_RF = 4992;
constexpr int L_X = 7168;

constexpr size_t W_AB = 0;
constexpr size_t W_XB = (size_t)4 << 20;
constexpr size_t W_E = (size_t)16 << 20;
constexpr size_t W_BEST = W_E + 4096;
constexpr size_t W_SEEN = W_E + 8192;
constexpr size_t W_KEY = W_E + 16384;
constexpr size_t W_PA = W_E + 65536;
constexpr size_t W_PB = W_E + 524288;
constexpr unsigned C_SEEN = W_SEEN - W_E, C_BEST = W_BEST - W_E, C_KEY = W_KEY - W_E, C_PA = W_PA - W_E, C_PB = W_PB - W_E;
constexpr int PSLEEP = 4, LSLEEP = 1, GSLEEP = 1, FSTR = 64;
constexpr unsigned C_FL2 = 1u << 20;

template <int OFF>
__device__ __forceinline__ void lds_rd128(bf16x8& d, unsigned a) {
  asm volatile("ds_read_b128 %0, %1 offset:%2" : "=v"(d) : "v"(a), "i"(OFF) : "memory");
}
__device__ __forceinline__ void glds(unsigned lds, unsigned voff, const void* sbase) {
  asm volatile("s_mov_b32 m0, %0\n\ts_nop 0\n\tglobal_load_lds_dwordx4 %1, %2" ::"s"(lds), "v"(voff), "s"(sbase) : "memory");
}
#define GLDS4(POL) asm volatile("s_add_u32 m0, %0, %6\n\ts_nop 0\n\tglobal_load_lds_dwordx4 %1, %5" POL "\n\t"                        \
                                "global_load_lds_dwordx4 %2, %5 offset:1024" POL "\n\tglobal_load_lds_dwordx4 %3, %5 offset:2048" POL "\n\t" \
                                "global_load_lds_dwordx4 %4, %5 offset:3072" POL                                                       \
                                ::"s"(lds), "v"(v0), "v"(v1), "v"(v2), "v"(v3), "s"(sbase), "i"(OFF) : "scc", "memory")
template <int OFF>
__device__ __forceinline__ void glds4(unsigned lds, unsigned v0, unsigned v1, unsigned v2, unsigned v3, const void* sbase) { GLDS4(""); }
template <int OFF>
__device__ __forceinline__ void glds4_sc1(unsigned lds, unsigned v0, unsigned v1, unsigned v2, unsigned v3, const void* sbase) { GLDS4(" sc1"); }
template <int ADD, int MASK, int SH>
__device__ __forceinline__ const char* padd(const char* base, unsigned o) {
  const u64 bb = (u64)base;
  unsigned lo, hi;
  asm volatile("s_add_u32 %0, %4, %5\n\ts_and_b32 %0, %0, %6\n\ts_lshl_b32 %0, %0, %7\n\ts_add_u32 %0, %2, %0\n\ts_addc_u32 %1, %3, 0"
               : "=&s"(lo), "=s"(hi)
               : "s"((unsigned)bb), "s"((unsigned)(bb >> 32)), "s"(o), "i"(ADD), "i"(MASK), "i"(SH)
               : "scc");
  return (const char*)(((u64)hi << 32) | lo);
}
__device__ __forceinline__ void gate(u64& seen, u64 want, unsigned sh, unsigned fo, unsigned tag, const void* wc, unsigned& bud) {
  u64 t, w;
  unsigned v;
  asm volatile("s_lshl_b64 %[w], %[want], %[sh]\n\ts_andn2_b64 %[t], %[w], %[seen]\n\ts_cbranch_scc0 .Lsb%=\n"
               ".Lsa%=:\n\tglobal_load_dword %[v], %[fo], %[wc] sc1\n\ts_waitcnt vmcnt(0)\n\tv_cmp_eq_u32 %[t], %[tag], %[v]\n\t"
               "s_or_b64 %[seen], %[seen], %[t]\n\ts_andn2_b64 %[t], %[w], %[seen]\n\ts_cbranch_scc0 .Lsb%=\n\ts_sleep %[sl]\n\t"
               "s_sub_u32 %[bud], %[bud], 16\n\ts_cbranch_scc0 .Lsa%=\n\ts_mov_b32 %[bud], 0\n.Lsb%=:"
               : [seen] "+s"(seen), [t] "=&s"(t), [w] "=&s"(w), [v] "=&v"(v), [bud] "+s"(bud)
               : [want] "s"(want), [sh] "s"(sh), [fo] "v"(fo), [tag] "s"(tag), [wc] "s"(wc), [sl] "i"(GSLEEP)
               : "scc", "memory");
}
template <int S>
__device__ __forceinline__ const char* k1tile(const char* base, unsigned rot) { return padd<S * 128, 0x380, 0>(base, rot << 7); }
__device__ __forceinline__ void gst128_sc1(unsigned voff, u32x4 v, const void* sbase) {
  asm volatile("global_store_dwordx4 %0, %1, %2 sc1\n\ts_nop 1" ::"v"(voff), "v"(v), "s"(sbase) : "memory");
}

__device__ __forceinline__ void lds_rd32(unsigned& d, unsigned a) { asm volatile("ds_read_b32 %0, %1" : "=v"(d) : "v"(a) : "memory"); }
__device__ __forceinline__ void sigv(unsigned row, const void* tab, unsigned n4) {
  asm volatile("s_mov_b32 m0, %0\n\ts_nop 0\n\tglobal_load_lds_dword %2, %1" ::"s"(row), "s"(tab), "v"(n4) : "memory");
}
template <int N>
__device__ __forceinline__ void sig(unsigned row, const void* tab) {
  unsigned t;
  asm volatile("s_mov_b32 m0, %1\n\tv_mov_b32 %0, %3\n\tglobal_load_lds_dword %0, %2" : "=&v"(t) : "s"(row), "s"(tab), "i"(N * 4) : "memory");
}
template <int N>
__device__ __forceinline__ void spin(unsigned a, unsigned& bud) {
  unsigned t;
  asm volatile(".Lsa%=:\n\tds_read_b32 %0, %2\n\ts_waitcnt lgkmcnt(0)\n\tv_cmp_gt_u32 vcc, %3, %0\n\ts_cbranch_vccz .Lsb%=\n\t"
               "s_sub_u32 %1, %1, 1\n\ts_cbranch_scc0 .Lsa%=\n\ts_mov_b32 %1, 0\n.Lsb%=:"
               : "=&v"(t), "+s"(bud) : "v"(a), "i"(N) : "vcc", "scc", "memory");
}
#define FT0 ""
#define FT1 "\n"
#define FLAG_ASM "s_getreg_b32 %[ib], hwreg(HW_REG_IB_STS)\n\ts_and_b32 %[ib], %[ib], 0xc0000f\n\ts_or_b32 %[ib], %[ib], %[done]\n\t"   \
                 "s_cmp_gt_u32 %[ib], %[y]\n\ts_cbranch_scc1 .Lsk%=\n\ts_waitcnt vmcnt(%[y])\n\ts_mov_b64 exec, 0xffff\n\t"                     \
                 FT0 "global_store_dword %[fo], %[tag], %[wc] sc1\n\ts_mov_b64 exec, -1\n\ts_mov_b32 %[done], 64\n\t" FT1 ".Lsk%=:\n\t"
template <int Y>
__device__ __forceinline__ void tryflag(unsigned& done, unsigned fo, unsigned tag, const void* wc) {
  static_assert(Y < 16, "");
  unsigned ib;
  asm volatile(FLAG_ASM : [ib] "=&s"(ib), [done] "+s"(done) : [fo] "v"(fo), [tag] "v"(tag), [wc] "s"(wc), [y] "i"(Y) : "scc", "memory");
}
template <int N, int Y>
__device__ __forceinline__ void spinf(unsigned a, unsigned& bud, unsigned& done, unsigned fo, unsigned tag, const void* wc) {
  static_assert(Y < 16, "");
  unsigned t, ib;
  asm volatile(".Lsa%=:\n\tds_read_b32 %[t], %[a]\n\t" FLAG_ASM "s_waitcnt lgkmcnt(0)\n\tv_cmp_gt_u32 vcc, %[n], %[t]\n\ts_cbranch_vccz .Lsb%=\n\t"
               "s_sub_u32 %[bud], %[bud], 1\n\ts_cbranch_scc0 .Lsa%=\n\ts_mov_b32 %[bud], 0\n.Lsb%=:"
               : [t] "=&v"(t), [bud] "+s"(bud), [ib] "=&s"(ib), [done] "+s"(done)
               : [a] "v"(a), [n] "i"(N), [fo] "v"(fo), [tag] "v"(tag), [wc] "s"(wc), [y] "i"(Y)
               : "vcc", "scc", "memory");
}
template <int N, int SL>
__device__ __forceinline__ void serve(unsigned a, unsigned& bud) {
  unsigned t;
  asm volatile(".Lsa%=:\n\tds_read_b32 %0, %2\n\ts_waitcnt lgkmcnt(0)\n\tv_cmp_gt_u32 vcc, %3, %0\n\ts_cbranch_vccz .Lsb%=\n\ts_sleep %4\n\t"
               "s_sub_u32 %1, %1, 1\n\ts_cbranch_scc0 .Lsa%=\n\ts_mov_b32 %1, 0\n.Lsb%=:"
               : "=&v"(t), "+s"(bud) : "v"(a), "i"(N), "i"(SL) : "vcc", "scc", "memory");
}
__device__ __forceinline__ void mm(f32x4& d, const bf16x8& a, const bf16x8& b) {
  asm volatile("v_mfma_f32_16x16x32_bf16 %0, %1, %2, %0" : "+v"(d) : "v"(a), "v"(b));
}
template <int N>
__device__ __forceinline__ void mmw(f32x4& d, const bf16x8& a, const bf16x8& b) {
  __builtin_amdgcn_s_waitcnt(0xc07f | (N << 8));
  SB();
  asm volatile("v_mfma_f32_16x16x32_bf16 %0, %1, %2, %0" : "+v"(d) : "v"(a), "v"(b) : "memory");
}
__device__ __forceinline__ void mm0(f32x4& d, const bf16x8& a, const bf16x8& b) {
  asm volatile("v_mfma_f32_16x16x32_bf16 %0, %1, %2, 0" : "=&v"(d) : "v"(a), "v"(b));
}
#define CHK_TAIL "s_cbranch_vccnz .Lsa%=\n.Lsb%=:\n\t.subsection 1\n.Lsa%=:\n\tds_read_b32 %[fl], %[a]\n\ts_waitcnt lgkmcnt(0)\n\t"        \
                 "v_cmp_gt_u32 vcc, %[n], %[fl]\n\ts_cbranch_vccz .Lsb%=\n\ts_sub_u32 %[bud], %[bud], 1\n\ts_cbranch_scc0 .Lsa%=\n\t"       \
                 "s_mov_b32 %[bud], 0\n\ts_branch .Lsb%=\n\t.subsection 0"
template <int N>
__device__ __forceinline__ void mmchk(f32x4& d, const bf16x8& x, const bf16x8& y, unsigned& fl, unsigned a, unsigned& bud) {
  asm volatile("v_cmp_gt_u32 vcc, %[n], %[fl]\n\tv_mfma_f32_16x16x32_bf16 %[d], %[x], %[y], %[d]\n\t" CHK_TAIL
               : [d] "+v"(d), [fl] "+v"(fl), [bud] "+s"(bud) : [x] "v"(x), [y] "v"(y), [a] "v"(a), [n] "i"(N) : "vcc", "scc", "memory");
}
template <int N, int W>
__device__ __forceinline__ void mmchkw(f32x4& d, const bf16x8& x, const bf16x8& y, unsigned& fl, unsigned a, unsigned& bud) {
  __builtin_amdgcn_s_waitcnt(0xc07f | (W << 8));
  SB();
  asm volatile("v_cmp_gt_u32 vcc, %[n], %[fl]\n\tv_mfma_f32_16x16x32_bf16 %[d], %[x], %[y], %[d]\n\t" CHK_TAIL
               : [d] "+v"(d), [fl] "+v"(fl), [bud] "+s"(bud) : [x] "v"(x), [y] "v"(y), [a] "v"(a), [n] "i"(N) : "vcc", "scc", "memory");
}
template <int N>
__device__ __forceinline__ void mm0w(f32x4& d, const bf16x8& a, const bf16x8& b) {
  asm volatile("s_waitcnt lgkmcnt(%3)\n\tv_mfma_f32_16x16x32_bf16 %0, %1, %2, 0" : "=&v"(d) : "v"(a), "v"(b), "i"(N) : "memory");
}
template <int N>
__device__ __forceinline__ void chk(unsigned& fl, unsigned a, unsigned& bud) {
  asm volatile("v_cmp_gt_u32 vcc, %[n], %[fl]\n\t" CHK_TAIL : [fl] "+v"(fl), [bud] "+s"(bud) : [a] "v"(a), [n] "i"(N) : "vcc", "scc", "memory");
}

__global__ __launch_bounds__(512, 2) void fused_k(const u16* __restrict__ wsre, const u16* __restrict__ wsim,
                                               const u16* __restrict__ x, const u16* __restrict__ w_r,
                                               const u16* __restrict__ gate_w, const u16* __restrict__ wdre,
                                               const u16* __restrict__ wdim, const u16* __restrict__ w_gate,
                                               const u16* __restrict__ w_up, const u16* __restrict__ w_down,
                                               char* ws, u16* __restrict__ out) {
  __shared__ __attribute__((aligned(16))) char smem[RING + SCR];
  static_assert(RING + SCR == 163840, "the whole LDS: one block per CU, the array starts at LDS address 0");
  asm volatile("ds_write2st64_b32 %0, %1, %1 offset1:8\n\ts_waitcnt lgkmcnt(0)\n\ts_barrier" ::"v"((unsigned)(size_t)smem + RING + ((threadIdx.x ^ 256u) << 2)), "v"(0u) : "memory");
  asm volatile("" ::"s"(wsre), "s"(wsim), "s"(x), "s"(w_r), "s"(gate_w), "s"(wdre), "s"(wdim), "s"(w_gate), "s"(w_up), "s"(w_down), "s"(ws));
  const char* wc = ws + W_E;
  const unsigned c_in = (unsigned)__builtin_amdgcn_s_memtime(), r_in = (unsigned)__builtin_amdgcn_s_memrealtime();
#define UNTIL_(now, t0, lim) do { _Pragma("clang loop unroll(disable)") for (int i_ = 0; i_ < 4096; ++i_) { if ((unsigned)(now) - (t0) >= (lim)) break; __builtin_amdgcn_s_sleep(1); } } while (0)
#define UNTIL(ticks, cyc) do { UNTIL_(__builtin_amdgcn_s_memrealtime(), r_in, ticks); UNTIL_(__builtin_amdgcn_s_memtime(), c_in, cyc); } while (0)
  const int tid = threadIdx.x, lane = tid & 63;
  const int wid = __builtin_amdgcn_readfirstlane(tid >> 6);
  const int b = blockIdx.x;
  const int xcd = b & 7, off = b >> 3;
  const int q = xcd >> 1;
  const int h = q >> 1, kh = q & 1;
  const int ej = off & 3, tl = off >> 2;
  const int e0 = (ej >> 1) * 256 + q * 64 + (ej & 1) * 32;
  const bool isq = ej & 1;
  unsigned isp = (unsigned)(~ej & 1);
  asm volatile("" : "+s"(isp));
  const int ti = (xcd & 1) * 8 + tl;
  const int si = (xcd & 3) * 4 + (tl & 3), cp1 = (tl >> 2) * 2 + (ej >> 1);
  const int fr = lane & 15, fq = lane >> 4;
  const int r8 = lane >> 3, c8 = lane & 7;
  const unsigned sb = (unsigned)(size_t)smem;
  volatile lds_u32* lflag = (volatile lds_u32*)(smem + RING + L_FLAG);
  const char* ab = ws + W_AB;
  const unsigned rot = (unsigned)(tl), flip = (unsigned)((ej & 2) * 4);
#define KADDR(P, SH, base) padd<(P) & 7, 7, SH>((base) + ((((P) & 8) ^ flip) << (SH)), rot)
  const unsigned rot1 = (unsigned)((2 * (cp1 + tl) + (xcd & 1)) & 7);
  const unsigned row_s = __builtin_amdgcn_readfirstlane(sb + RING + ((wid ^ 4) << 8));
  u64 pc;
  unsigned tab_o;
  asm volatile("s_getpc_b64 %0\n.Lfk_here:\n\ts_mov_b32 %1, .Lfk_tab-.Lfk_here\n\t.subsection 2\n\t.p2align 8\n.Lfk_tab:\n\t"
               ".set fk_i, 0\n\t.rept 128\n\t.long fk_i\n\t.set fk_i, fk_i+1\n\t.endr\n\t.subsection 0"
               : "=s"(pc), "=s"(tab_o));
  const char* tab = reinterpret_cast<const char*>(pc + tab_o);
#define SIG(n) sig<n>(row_s, tab)
  const unsigned lf_rd = sb + RING + L_LF + (lane & 1) * 516, cp_rd = sb + RING + ((lane & 7) == 4 ? L_LF + 784 : L_CP + (lane & 3) * 260);
  const unsigned la_rd = sb + RING + L_LF, lb_rd = sb + RING + L_LF + 512;
  const unsigned own_rd = sb + RING + L_FLAG + 32 + (lane & 3) * 4;
#define IC(k) std::integral_constant<int, k>{}

  unsigned bud = 1u << 20;
#define SERVE(ADDR, N) serve<N, LSLEEP>(ADDR, bud)
  const unsigned best_rd = sb + RING + L_FLAG + 12, d_rd = sb + RING + L_FLAG + 48;
  const unsigned rf_rd = sb + RING + L_RG + 1024;
  const unsigned tg_rd = sb + RING + L_FLAG + 56, gt_rd = sb + RING + L_FLAG + 4;
#define TAG_WAIT() do { SERVE(tg_rd, 1); tag = __builtin_amdgcn_readfirstlane(lflag[15]); } while (0)
  const unsigned lbase = __builtin_amdgcn_readfirstlane(sb);
  auto row_addr = [&](unsigned (&va)[8]) {
    unsigned l = (unsigned)lane;
    asm volatile("" : "+v"(l));
#pragma unroll
    for (int i = 0; i < 8; ++i) va[i] = (i * 8 + (l >> 3)) * (D_ * 2) + (((l & 7) ^ (l >> 3)) << 4) - (i & 3) * 1024;
  };
  const char* xs = (const char*)x + (size_t)si * 128 * D_ * 2;
  const char* wds = (const char*)(h ? wdim : wdre) + (size_t)cp1 * 128 * D_ * 2;
  if ((wid | 2) == 6) {
    unsigned va[8];
    row_addr(va);
    const bool a = wid == 4;
    const char* base = a ? xs : wds;
    const unsigned n = isq ? (a ? 1 * 2u : 1 * 2u) : 2u;
    const unsigned r0 = rot1 + (isq ? 1u : 0u);
    unsigned l0 = lbase + (a ? 0 : isq ? 131072 : 81920);
    if (!a && !isq) {
      glds(lbase + RING + L_RF, (unsigned)(lane * 16), w_r);
      glds(lbase + RING + L_RF + 1152, (unsigned)(lane * 16), gate_w);
      sig<1>(lbase + RING + L_RG + 1024, tab);
    }
    if (isq) __builtin_amdgcn_s_sleep(20);
#pragma clang loop unroll(disable)
    for (unsigned u = 0; u < n; ++u) {
      const char* p = base + (((((u >> 1) + r0) & 7u) << 7) + ((u & 1u) << 16));
      glds4<0>(l0, va[0], va[1], va[2], va[3], p);
      glds4<4096>(l0, va[4], va[5], va[6], va[7], p);
      sigv(row_s, tab, ((u + 1u) >> 1) << 2);
      l0 += 8192u;
    }
  } else {
    const unsigned len = tab_o + 512u;
    const unsigned po = (unsigned)((wid * 32 + off) * 1024 + lane * 16);
    if (po + 16u <= len) glds(lbase + RING + L_X, po, reinterpret_cast<const char*>(pc & ~63ull));
  }
  unsigned E;
  asm volatile("s_cmp_lg_u32 %3, 7\n\ts_cbranch_scc1 .Le%=\n\tglobal_load_dword %0, %1, %2 sc1\n.Le%=:" : "=&v"(E) : "v"(0u), "s"(wc), "s"(wid) : "scc", "memory");
  if (wid >= 4) {
  unsigned vw[16], vx[16];
  auto X_ADDR = [&]() {
    unsigned va[8];
    row_addr(va);
#pragma unroll
    for (int i = 0; i < 16; ++i) vx[i] = va[i & 7] + (unsigned)((i >> 3) * 64 * D_ * 2);
  };
  const char* Ws = (const char*)(h ? wsim : wsre) + (size_t)ti * 128 * S_ * 2 + kh * S_;
  auto tile16 = [&](auto O, const char* p, const unsigned (&v)[16]) {
    glds4<decltype(O)::value>(lbase, v[0], v[1], v[2], v[3], p);
    glds4<decltype(O)::value + 4096>(lbase, v[4], v[5], v[6], v[7], p);
    glds4<decltype(O)::value + 8192>(lbase, v[8], v[9], v[10], v[11], p);
    glds4<decltype(O)::value + 12288>(lbase, v[12], v[13], v[14], v[15], p);
  };
  auto W_ADDR = [&]() {
    unsigned r8_ = (unsigned)r8;
    asm volatile("" : "+v"(r8_));
#pragma unroll
    for (int i = 0; i < 16; ++i) vw[i] = (i * 8 + r8_) * (S_ * 2) + (((unsigned)c8 ^ r8_) << 4) - (i & 3) * 1024;
  };
  auto stageW = [&](auto P, auto O) { tile16(O, KADDR(decltype(P)::value, 7, Ws), vw); };
  const int qi = (off >> 1) * 8 + xcd;
  const unsigned g_o = (unsigned)(C_KEY + ti * 1024 + (lane >> 2 & 7) * 128 + (lane & 3) * 16);
#define G_PUT(gv, best) asm volatile("ds_write_b128 %0, %1\n\tds_write_b32 %2, %3" ::"v"(sb + RING + L_G + (lane & 31) * 16),                                    \
                                     "v"(u32x4{gv.x << 16, gv.x & 0xffff0000u, gv.y << 16, gv.y & 0xffff0000u}), "v"(best_rd), "v"(best) : "memory")
  if (wid == 4) {
    X_ADDR();
    auto stageX = [&](auto K, auto O) { tile16(O, k1tile<decltype(K)::value>(xs, rot1), vx); };
    W_ADDR();
    if (isp) {
    stageX(IC(1), IC(16384));
    SIG(2);
    stageX(IC(2), IC(32768));
    SIG(3);
    stageX(IC(3), IC(49152));
    SIG(4);
    stageX(IC(4), IC(65536));
    SIG(5);
    SERVE(cp_rd, 1);
    stageX(IC(5), IC(0));
    SIG(6);
    SERVE(cp_rd, 2);
    stageX(IC(6), IC(16384));
    SIG(7);
    SERVE(cp_rd, 3);
    stageX(IC(7), IC(32768));
    SIG(8);
    }
    if (isq) {
    }
    if (isp) SERVE(cp_rd, 4);
    stageW(IC(0), IC(49152));
    if (isp) SERVE(cp_rd, 5);
    stageW(IC(1), IC(65536));
    SIG(9);
    stageW(IC(2), IC(81920));
    if (isp) SERVE(cp_rd, 6);
    stageW(IC(3), IC(0));
    SIG(10);
    SERVE(own_rd, 1);
    stageW(IC(4), IC(98304));
    if (isp) SERVE(cp_rd, 7);
    stageW(IC(5), IC(16384));
    SIG(11);
    stageW(IC(6), IC(114688));
    if (isp) SERVE(cp_rd, 8);
    stageW(IC(7), IC(32768));
    SIG(15);
    if (isq) { SERVE(lb_rd, 12); }
    stageW(IC(8), IC(131072));
    SIG(16);
    SERVE(own_rd, 2);
    stageW(IC(9), IC(49152));
    SIG(17);
    stageW(IC(10), IC(65536));
    SIG(18);
    SERVE(own_rd, 3);
    stageW(IC(11), IC(81920));
    SIG(64);
    bud = 1u << 18;
    __builtin_amdgcn_s_setprio(3);
    unsigned tag;
    TAG_WAIT();
    if (isp) {
      UNTIL(855u, 17991u);
      u32x2 kb;
      u32x4 gv;
#pragma clang loop unroll(disable)
      for (int it = 0; it < SPIN_MAX; ++it) {
        asm volatile("global_load_dwordx2 %0, %2, %4 sc1\n\tglobal_load_dwordx4 %1, %3, %4 sc1\n\ts_waitcnt vmcnt(0)" : "=&v"(kb), "=&v"(gv) : "v"((unsigned)C_BEST), "v"(g_o), "s"(wc) : "memory");
        if (__all(kb.y == tag && gv.w == tag)) break;
        __builtin_amdgcn_s_sleep(PSLEEP);
      }
      G_PUT(gv, kb.x);
    }
    SERVE(best_rd, 1);
    const unsigned best = __builtin_amdgcn_readfirstlane(lflag[3]);
    const int idx = (4095 - (int)(best & 0xfffu)) & (S_ - 1);
    if ((idx >> 7) == ti) {
      const unsigned uo = best & 0xffff0000u;
      const float rsel = rbf(sigmoidf_(__uint_as_float((uo & 0x80000000u) ? (uo & 0x7fffffffu) : (~uo & 0xffff0000u))));
      const int col = (lane & 7) * 2 + (lane >> 5);
      unsigned xv[2];
      asm volatile("global_load_ushort %0, %2, %3\n\tglobal_load_ushort %1, %2, %3 offset:32\n\ts_waitcnt vmcnt(0)"
                   : "=&v"(xv[0]), "=&v"(xv[1]) : "v"((unsigned)((idx * D_ + e0 + col) * 2)), "s"(x) : "memory");
      float rs_ = rsel;
      unsigned pa = sb + RING + L_UPD + col * 2;
      u32x4 ov[8];
      unsigned go = (unsigned)(C_PB + r8 * 4096 + e0 * 8 + c8 * 16);
      unsigned g3 = lane < 32 ? go + 98304u : (unsigned)(C_SEEN + (lane & 7) * 256);
      asm volatile("" : "+v"(rs_), "+v"(pa), "+v"(go), "+v"(g3));
      SERVE(cp_rd, 18); UNTIL(1045u, 21373u);
#pragma clang loop unroll(disable)
      for (int it = 0; it < SPIN_MAX; ++it) {
        asm volatile("global_load_dwordx4 %0, %8, %12 sc1\n\tglobal_load_dwordx4 %1, %9, %12 sc1\n\t"
                     "global_load_dwordx4 %2, %10, %12 sc1\n\tglobal_load_dwordx4 %3, %11, %12 sc1\n\t"
                     "global_load_dwordx4 %4, %8, %12 offset:128 sc1\n\tglobal_load_dwordx4 %5, %9, %12 offset:128 sc1\n\t"
                     "global_load_dwordx4 %6, %10, %12 offset:128 sc1\n\tglobal_load_dwordx4 %7, %11, %12 offset:128 sc1\n\ts_waitcnt vmcnt(0)"
                     : "=&v"(ov[0]), "=&v"(ov[1]), "=&v"(ov[2]), "=&v"(ov[3]), "=&v"(ov[4]), "=&v"(ov[5]), "=&v"(ov[6]), "=&v"(ov[7])
                     : "v"(go), "v"(go + 32768u), "v"(go + 65536u), "v"(g3), "s"(wc)
                     : "memory");
        bool ok = true;
#pragma unroll
        for (int k = 0; k < 8; ++k) ok = ok && ov[k].y == tag && ov[k].w == tag;
        if (__all(ok)) break;
        __builtin_amdgcn_s_sleep(PSLEEP);
      }
      if (ej == 0 && q == 0 && lane == 0)
        asm volatile("global_store_dword %0, %1, %2 sc1\n\ts_nop 1" ::"v"(0u), "v"(tag), "s"(wc) : "memory");
      float u[2];
#pragma unroll
      for (int c = 0; c < 2; ++c) {
        if (lane >= 32) ov[c * 4 + 3].x = ov[c * 4 + 3].z = 0u;
        float s0 = (__uint_as_float(ov[c * 4].x) + __uint_as_float(ov[c * 4 + 1].x)) + (__uint_as_float(ov[c * 4 + 2].x) + __uint_as_float(ov[c * 4 + 3].x));
        float s1 = (__uint_as_float(ov[c * 4].z) + __uint_as_float(ov[c * 4 + 1].z)) + (__uint_as_float(ov[c * 4 + 2].z) + __uint_as_float(ov[c * 4 + 3].z));
        s0 = dpp_add<0x128>(s0); s1 = dpp_add<0x128>(s1);
        const float t2 = sw16(sw32(s0, s1), sw32(s0, s1));
        u[c] = rbf(rbf(t2) * rs_);
      }
      const unsigned p0 = f2bf(lo2f(xv[0]) + u[0]), p1 = f2bf(lo2f(xv[1]) + u[1]);
      if ((lane & 31) < 8)
        asm volatile("ds_write_b16 %0, %1\n\tds_write_b16 %0, %2 offset:32"
                     ::"v"(pa), "v"(p0), "v"(p1) : "memory");
    }
    asm volatile("ds_write_b32 %0, %1\n\ts_waitcnt vmcnt(0) lgkmcnt(0)" ::"v"(d_rd), "v"(1u) : "memory");
  } else if (wid == 6) {
    X_ADDR();
    const unsigned vb0 = (unsigned)(lane * 16), vb2 = vb0 + 2048u;
    const char* abg = ab + (size_t)((h * 4 + ej) * 32 + kh * 8) * A_B;
    const char* abA = abg + (h ^ 1) * 16 * A_B;
    const char* abH = abg + h * 16 * A_B;
    auto stageD = [&](auto K, auto O) { tile16(O, k1tile<decltype(K)::value>(wds, rot1), vx); };
    auto stageQ = [&](auto O) {
      const char* p = (const char*)x + (size_t)ti * 128 * D_ * 2 + e0 * 2;
      const unsigned v = (unsigned)((lane >> 2) * (D_ * 2) + ((lane & 3) ^ (lane >> 4)) * 16);
      glds4<decltype(O)::value>(lbase, v, v + 15360u, v + 30720u, v + 46080u, p);
      glds4<decltype(O)::value + 4096>(lbase, v, v + 15360u, v + 30720u, v + 46080u, p + 64 * (D_ * 2));
    };
    unsigned tag;
    u64 seen = 0ull;
    const unsigned flag_o = (unsigned)(C_FL2 + (((h * 4 + ej) * 2 + kh) * 32 + (lane & 31)) * FSTR);
    auto GATE_ALL = [&]() {
      gate(seen, ~0ull, 0u, flag_o, tag, wc, bud);
      if (isq) asm volatile("ds_write_b32 %0, %1" ::"v"(gt_rd), "v"(1u) : "memory");
    };
    const unsigned dl = (unsigned)(kh ^ 1) * 2048u;
    const unsigned vc0 = vb2 + dl, vc1 = vb2 - dl, vc2 = vc0 + 4096u, vc3 = vc1 + 4096u;
    auto stageA = [&](auto P, auto H, auto O, auto G) {
      constexpr int hf = decltype(H)::value;
      const char* p = KADDR(decltype(P)::value, 13, (hf ? abH : abA) - 2048);
      glds4_sc1<decltype(O)::value>(lbase, vc0, vc0, vc1, vc1, p);
      glds4_sc1<decltype(O)::value + 4096>(lbase, vc2, vc2, vc3, vc3, p);
    };
    auto stageN = [&](auto P, auto O, const char* base) {
      glds4_sc1<decltype(O)::value>(lbase, vb0, vb0, vb2, vb2, KADDR(decltype(P)::value, 13, base));
      glds4_sc1<decltype(O)::value + 4096>(lbase, vb0, vb0, vb2, vb2, KADDR(decltype(P)::value + 1, 13, base));
    };
    if (isp) {
    stageD(IC(1), IC(98304));
    SIG(2);
    WAIT_V(18);
    stageD(IC(2), IC(114688));
    SIG(3);
    WAIT_V(18);
    stageD(IC(3), IC(131072));
    SIG(4);
    SERVE(cp_rd, 1);
    WAIT_V(18);
    stageD(IC(4), IC(81920));
    SIG(5);
    SERVE(cp_rd, 2);
    WAIT_V(18);
    stageD(IC(5), IC(98304));
    SIG(6);
    SERVE(cp_rd, 3);
    WAIT_V(18);
    stageD(IC(6), IC(114688));
    SIG(7);
    SERVE(cp_rd, 4);
    WAIT_V(18);
    stageD(IC(7), IC(131072));
    SIG(8);
    }
    if (isq) UNTIL(144u, 3075u);
    stageQ(IC(147456));
    sig<12>(row_s, tab);
    W_ADDR();
    TAG_WAIT();
    SERVE(own_rd, 1);
    SERVE(cp_rd, 8);
    SERVE(la_rd, 9);
    SERVE(own_rd, 3); if (isq) SERVE(own_rd, 5);
    UNTIL(569u, 11787u);
    GATE_ALL();
    SERVE(own_rd, 3);
    stageA(IC(0), IC(0), IC(0), IC(0));
    SIG(13);
    stageA(IC(0), IC(1), IC(8192), IC(0));
    SIG(14);
    SERVE(own_rd, 4);
    stageA(IC(1), IC(0), IC(98304), IC(0));
    SIG(15);
    stageA(IC(1), IC(1), IC(106496), IC(0));
    SIG(16);
    stageA(IC(2), IC(0), IC(16384), IC(0));
    stageA(IC(2), IC(1), IC(24576), IC(0));
    SIG(17);
    SERVE(own_rd, 5);
    stageA(IC(3), IC(0), IC(114688), IC(0));
    stageA(IC(3), IC(1), IC(122880), IC(0));
    SIG(18);
    stageA(IC(4), IC(0), IC(32768), IC(0));
    stageA(IC(4), IC(1), IC(40960), IC(0));
    SIG(19);
    SERVE(cp_rd, 9);
    stageA(IC(5), IC(0), IC(0), IC(0));
    SERVE(cp_rd, 10);
    stageA(IC(5), IC(1), IC(8192), IC(0));
    SIG(20);
    SERVE(cp_rd, 11);
    stageW(IC(12), IC(98304));
    stageA(IC(6), IC(0), IC(131072), IC(0));
    stageA(IC(7), IC(0), IC(139264), IC(0));
    SIG(21);
    SERVE(cp_rd, 12);
    stageW(IC(13), IC(16384));
    stageN(IC(6), IC(49152), abH + dl);
    stageN(IC(8), IC(57344), abA + dl);
    SIG(22);
    SERVE(cp_rd, 13);
    stageW(IC(14), IC(65536));
    stageN(IC(10), IC(114688), abA + dl);
    SERVE(cp_rd, 14);
    stageW(IC(15), IC(32768));
    stageN(IC(12), IC(122880), abA + dl);
    SIG(23);
    stageN(IC(14), IC(81920), abA + dl);
    stageN(IC(8), IC(90112), abA + (dl ^ 2048u));
    SIG(24);
    SERVE(cp_rd, 15);
    stageN(IC(10), IC(0), abA + (dl ^ 2048u));
    stageN(IC(12), IC(8192), abA + (dl ^ 2048u));
    SIG(25);
    SERVE(cp_rd, 16);
    stageN(IC(14), IC(131072), abA + (dl ^ 2048u));
    stageN(IC(8), IC(139264), abH + dl);
    SIG(26);
    SERVE(cp_rd, 17);
    stageN(IC(10), IC(49152), abH + dl);
    stageN(IC(12), IC(57344), abH + dl);
    SIG(27);
    stageN(IC(14), IC(98304), abH + dl);
    stageN(IC(6), IC(106496), abH + (dl ^ 2048u));
    SIG(28);
    SERVE(cp_rd, 18);
    stageN(IC(8), IC(16384), abH + (dl ^ 2048u));
    stageN(IC(10), IC(24576), abH + (dl ^ 2048u));
    SIG(29);
    stageN(IC(12), IC(114688), abH + (dl ^ 2048u));
    stageN(IC(14), IC(122880), abH + (dl ^ 2048u));
    SIG(64);
    WAIT_V(0);
  } else {
    const int sw = wid >> 1 & 1;
    __builtin_amdgcn_s_setprio(2);
    unsigned tag;
    int idx = 0;
    auto TAG_PUT = [&]() {
      unsigned es;
      asm volatile("s_waitcnt vmcnt(0)\n\tv_readfirstlane_b32 %0, %1" : "=s"(es) : "v"(E) : "memory");
      asm volatile("ds_write2_b32 %0, %1, %2 offset1:1" ::"v"(tg_rd), "v"(1u), "v"(es * 0x9E3779B1u + 0x7F4A7C15u) : "memory");
    };
    auto ROUTER = [&]() {
      const unsigned ra = sb + ((h * 4 + cp1) * 16 + fr) * 128 + ((fq ^ (fr & 7)) << 4), rb = sb + RING + L_RF + (fr & 1) * 1152 + fq * 16;
      f32x4 d;
      SERVE(rf_rd, 1);
#define RSTEP(s, o) do { bf16x8 a0, a1, b0, b1; SERVE(la_rd, (s) + 1);                                                                       \
      asm volatile("ds_read_b128 %0, %4\n\tds_read_b128 %1, %5\n\tds_read_b128 %2, %6\n\tds_read_b128 %3, %6 offset:64\n\ts_waitcnt lgkmcnt(0)"  \
                   : "=&v"(a0), "=&v"(a1), "=&v"(b0), "=&v"(b1) : "v"(ra + (o)), "v"((ra ^ 64u) + (o)), "v"(rb + (((s) + rot1) & 7u) * 128u) : "memory"); \
      sig<(s) == 7 ? 64 : (s) + 1>(lbase + RING + L_LF + 768, tab); if ((s) == 0) mm0(d, a0, b0); else mm(d, a0, b0); mm(d, a1, b1); } while (0)
      RSTEP(0, 0u);
      RSTEP(1, 16384u);
      RSTEP(2, 32768u);
      RSTEP(3, 49152u);
      RSTEP(4, 65536u);
      RSTEP(5, 0u);
      RSTEP(6, 16384u);
      RSTEP(7, 32768u);
      asm volatile("s_nop 7" : "+v"(d));
      const int rid = si * 8 + h * 4 + cp1;
      unsigned key = 0u;
      float g[4];
#pragma unroll
      for (int i = 0; i < 4; ++i) {
        const float r = rbf(d[i]);
        g[i] = sigmoidf_(r);
        const unsigned u = __float_as_uint(r);
        const unsigned uo = (u & 0x80000000u) ? ~u : (u | 0x80000000u);
        key = max(key, (uo & 0xffff0000u) | (unsigned)(4095 - (rid * 16 + fq * 4 + i)));
      }
      key = dpp<0xa0>(key);
      const auto k1 = __builtin_amdgcn_permlane16_swap(key, key, false, false);
      key = max(k1[0], k1[1]);
      const auto k2 = __builtin_amdgcn_permlane32_swap(key, key, false, false);
      key = max(k2[0], k2[1]);
      if ((fr | 2) == 3) gst128_sc1((unsigned)(C_KEY + rid * 128 + (fr >> 1) * 64 + fq * 16), u32x4{pk2(g[0], g[1]), pk2(g[2], g[3]), key, tag}, wc);
    };
    auto POLL_KEYS = [&]() {
      u32x2 k0, k1;
      u32x4 gv;
#pragma clang loop unroll(disable)
      for (int it = 0; it < SPIN_MAX; ++it) {
        asm volatile("global_load_dwordx2 %0, %3, %6 sc1\n\tglobal_load_dwordx2 %1, %4, %6 sc1\n\tglobal_load_dwordx4 %2, %5, %6 sc1\n\ts_waitcnt vmcnt(0)"
                     : "=&v"(k0), "=&v"(k1), "=&v"(gv) : "v"((unsigned)(C_KEY + lane * 128 + 8)), "v"((unsigned)(C_KEY + lane * 128 + 8200)), "v"(g_o), "s"(wc) : "memory");
        if (__all(k0.y == tag && k1.y == tag && gv.w == tag)) break;
        __builtin_amdgcn_s_sleep(PSLEEP);
      }
      const unsigned best = wave_max(max(k0.x, k1.x));
      G_PUT(gv, best);
      if (b == 8 && lane < 8) gst128_sc1((unsigned)(C_BEST + lane * 16), u32x4{best, tag, best, tag}, wc);
    };
    auto IDX_READ = [&]() { idx = (4095 - (int)(__builtin_amdgcn_readfirstlane(lflag[3]) & 0xfffu)) & (S_ - 1); };
    const int on = 111 - qi;
    const bool ffn = on >= 0;
    const int fo = ffn ? qi >> 2 : 0, fp = qi & 3;
    u32x4 fg[16];
    const u16* wgu = sw ? w_up : w_gate;
    const unsigned vg = (unsigned)((fp * 128 + r8 * 8) * (F_ * 2) + fo * 128 + c8 * 16);
#define LOAD4(d, v, step, base, on)                                                                                        \
  asm volatile("s_cmp_lt_i32 %9, 0\n\ts_cbranch_scc1 .Lq%=\n\tglobal_load_dwordx4 %0, %4, %8\n\tglobal_load_dwordx4 %1, %5, %8\n\t" \
               "global_load_dwordx4 %2, %6, %8\n\tglobal_load_dwordx4 %3, %7, %8\n.Lq%=:"                                   \
               : "+v"((d)[0]), "+v"((d)[1]), "+v"((d)[2]), "+v"((d)[3])                                                    \
               : "v"(v), "v"((v) + (step)), "v"((v) + 2u * (step)), "v"((v) + 3u * (step)), "s"(base), "s"(on) : "scc", "memory")
#define ANY8(w) asm volatile("" : "=v"((w)[0]), "=v"((w)[1]), "=v"((w)[2]), "=v"((w)[3]), "=v"((w)[4]), "=v"((w)[5]), "=v"((w)[6]), "=v"((w)[7]))
    ANY8(fg);
    ANY8(fg + 8);
#define FFN_LOAD(p) LOAD4(fg + (p) * 4, vg + (((p) & 1) * 4u + ((p) >> 1) * 64u) * (F_ * 2), (unsigned)(F_ * 2), wgu, on)
    auto turn = [&](u32x4* w) {
      u32x4 t[8];
#pragma unroll
      for (int d = 0; d < 4; ++d)
#pragma unroll
        for (int j = 0; j < 4; ++j) {
          t[2 * d][j] = __builtin_amdgcn_perm(w[2 * j + 1][d], w[2 * j][d], 0x05040100u);
          t[2 * d + 1][j] = __builtin_amdgcn_perm(w[2 * j + 1][d], w[2 * j][d], 0x07060302u);
        }
#pragma unroll
      for (int i = 0; i < 8; ++i) w[i] = t[i];
      asm volatile("" : "+v"(w[0]), "+v"(w[1]), "+v"(w[2]), "+v"(w[3]), "+v"(w[4]), "+v"(w[5]), "+v"(w[6]), "+v"(w[7]));
    };
#define PIN8(w) asm volatile("s_waitcnt vmcnt(0)" : "+v"((w)[0]), "+v"((w)[1]), "+v"((w)[2]), "+v"((w)[3]), "+v"((w)[4]), "+v"((w)[5]), "+v"((w)[6]), "+v"((w)[7])::"memory")
    unsigned pa_o = (unsigned)(C_PA + qi * 1024 + (sw * 64 + c8 * 8 + fq * 2) * 8);
    auto FFN_RUN = [&]() {
      bf16x8 xa_[2];
      asm volatile("global_load_dwordx4 %0, %2, %3\n\tglobal_load_dwordx4 %1, %2, %3 offset:128\n\ts_waitcnt vmcnt(0)"
                   : "=&v"(xa_[0]), "=&v"(xa_[1]) : "v"((unsigned)(idx * (D_ * 2) + (fp * 128 + (fq * 2 + (fr & 1)) * 8) * 2)), "s"(x) : "memory");
      f32x4 d[8];
#pragma unroll
      for (int j = 0; j < 8; ++j) mm0(d[j], xa_[0], __builtin_bit_cast(bf16x8, fg[j]));
#pragma unroll
      for (int j = 0; j < 8; ++j) mm(d[j], xa_[1], __builtin_bit_cast(bf16x8, fg[8 + j]));
      asm volatile("s_nop 7" : "+v"(d[0]), "+v"(d[1]), "+v"(d[2]), "+v"(d[3]), "+v"(d[4]), "+v"(d[5]), "+v"(d[6]), "+v"(d[7]));
      float dp[8];
#pragma unroll
      for (int j = 0; j < 8; ++j) dp[j] = fr < 8 ? d[j][0] : d[j][1];
      const float s0 = dpp_add<0x128>(lane < 32 ? (fq & 1 ? dp[2] : dp[0]) : (fq & 1 ? dp[6] : dp[4]));
      const float s1 = dpp_add<0x128>(lane < 32 ? (fq & 1 ? dp[3] : dp[1]) : (fq & 1 ? dp[7] : dp[5]));
      const u32x4 o = {__float_as_uint(s0), tag, __float_as_uint(s1), tag};
      SERVE(gt_rd, 1);
      if ((lane & 8) == 0)
        gst128_sc1(pa_o, o, wc);
    };
    u32x4 fd[16];
    const unsigned vd = (unsigned)((fo * 64 + fq * 16) * (D_ * 2) + fp * 256 + fr * 16);
    ANY8(fd);
    ANY8(fd + 8);
#define DOWN_LOAD(p) LOAD4(fd + (p) * 4, vd + (p) * 4096u, 1024u, w_down, on | (sw - 1))
    unsigned pb_o = (unsigned)(C_PB + fo * 4096 + (fp * 128 + fr * 8 + fq * 2) * 8), pvo = (unsigned)(C_PA + fo * 4096 + lane * 16);
    auto PREP_F = [&]() { PIN8(fg); PIN8(fg + 8); turn(fg); turn(fg + 8); asm volatile("" : "+v"(pa_o)); };
    auto PREP_D = [&]() { PIN8(fd); PIN8(fd + 8); turn(fd); turn(fd + 8); asm volatile("" : "+v"(pb_o), "+v"(pvo)); };
    auto SEEN = [&]() { if (lane < 8) gst128_sc1((unsigned)(C_SEEN + (qi - 112) * 128 + lane * 16), u32x4{tag, tag, tag, tag}, wc); };
    auto RED_AB = [&]() {
      u32x4 pv[4];
#pragma clang loop unroll(disable)
      for (int it = 0; it < SPIN_MAX; ++it) {
        asm volatile("global_load_dwordx4 %0, %4, %5 sc1\n\tglobal_load_dwordx4 %1, %4, %5 offset:1024 sc1\n\t"
                     "global_load_dwordx4 %2, %4, %5 offset:2048 sc1\n\tglobal_load_dwordx4 %3, %4, %5 offset:3072 sc1\n\ts_waitcnt vmcnt(0)"
                     : "=&v"(pv[0]), "=&v"(pv[1]), "=&v"(pv[2]), "=&v"(pv[3]) : "v"(pvo), "s"(wc) : "memory");
        bool ok = true;
#pragma unroll
        for (int k = 0; k < 4; ++k) ok = ok && pv[k].y == tag && pv[k].w == tag;
        if (__all(ok)) break;
        __builtin_amdgcn_s_sleep(PSLEEP);
      }
      float s0 = 0.f, s1 = 0.f;
#pragma unroll
      for (int k = 0; k < 4; ++k) { s0 += __uint_as_float(pv[k].x); s1 += __uint_as_float(pv[k].z); }
      const auto r = __builtin_amdgcn_permlane32_swap(__float_as_uint(s0), __float_as_uint(s1), false, false);
      const float gt = rbf(__uint_as_float(r[0]));
      const unsigned hb = f2bf(rbf(gt * sigmoidf_(gt)) * rbf(__uint_as_float(r[1])));
      asm volatile("ds_write_b16 %0, %1" ::"v"(sb + RING + L_H + (lane & 31) * 4 + (lane >> 5) * 2), "v"(hb) : "memory");
    };
    auto RED_C = [&]() {
      bf16x8 hp[2];
      asm volatile("ds_read_b128 %0, %2\n\tds_read_b128 %1, %2 offset:16\n\ts_waitcnt lgkmcnt(0)" : "=&v"(hp[0]), "=&v"(hp[1]) : "v"(sb + RING + L_H + fq * 32) : "memory");
      f32x4 d[8];
#pragma unroll
      for (int j = 0; j < 8; ++j) mm0(d[j], hp[0], __builtin_bit_cast(bf16x8, fd[j]));
#pragma unroll
      for (int j = 0; j < 8; ++j) mm(d[j], hp[1], __builtin_bit_cast(bf16x8, fd[8 + j]));
      asm volatile("s_nop 7" : "+v"(d[0]), "+v"(d[1]), "+v"(d[2]), "+v"(d[3]), "+v"(d[4]), "+v"(d[5]), "+v"(d[6]), "+v"(d[7]));
      const float dp[8] = {d[0][0], d[1][0], d[2][0], d[3][0], d[4][0], d[5][0], d[6][0], d[7][0]};
      const float v0 = lane < 32 ? (fq & 1 ? dp[2] : dp[0]) : (fq & 1 ? dp[6] : dp[4]);
      const float v1 = lane < 32 ? (fq & 1 ? dp[3] : dp[1]) : (fq & 1 ? dp[7] : dp[5]);
      gst128_sc1(pb_o, u32x4{__float_as_uint(v0), tag, __float_as_uint(v1), tag}, wc);
    };
    if (sw) TAG_PUT();
    if (isp) {
      TAG_WAIT();
      if (sw == 0) ROUTER();
    SERVE(lb_rd, 16);
      DOWN_LOAD(0);
      DOWN_LOAD(1);
      DOWN_LOAD(2);
      DOWN_LOAD(3);
      if (sw) PREP_D();
    SERVE(cp_rd, 14);
    UNTIL(855u, 17991u);
      if (ffn && sw) RED_AB();
      if (ffn && sw) RED_C();
    }
    if (isq) {
      TAG_WAIT();
      if (!ffn && sw) SEEN();
      if (sw == 0) sig<64>(lbase + RING + L_LF + 768, tab);
    SERVE(la_rd, 11);
      FFN_LOAD(0);
      FFN_LOAD(1);
      FFN_LOAD(2);
      FFN_LOAD(3);
    SERVE(own_rd, 5);
    UNTIL(574u, 11890u);
      if (sw == 0) POLL_KEYS();
      PREP_F();
      SERVE(best_rd, 1);
      IDX_READ();
      if (ffn) FFN_RUN();
    }
  }
  } else {
    const int wm = wid >> 1, wn = wid & 1;
    unsigned fl;
    unsigned flagged = (unsigned)(ej & 1) * 64u;
    const unsigned flag_o = (unsigned)(C_FL2 + (((h * 4 + (cp1 & 2) + wn) * 2 + kh) * 32 + ((si & 7) * 2 + (cp1 & 1)) * 2 + wm) * FSTR + lane * 4);
    unsigned tagv;
#define TRYFLAG(Y) tryflag<Y>(flagged, flag_o, tagv, wc)
#define FLAG(Y) do { WAIT_V(Y); TRYFLAG(Y); } while (0)
    const unsigned own_wr = sb + RING + L_FLAG + 32 + wid * 4;
#define OWN_SIG(n) do { unsigned t_; asm volatile("v_mov_b32 %0, %2\n\tds_write_b32 %1, %0" : "=&v"(t_) : "v"(own_wr), "i"(n) : "memory"); } while (0)
#define SPIN(ADDR, N) spin<N>(ADDR, bud)
#define SPINF(ADDR, N, Y) spinf<N, Y>(ADDR, bud, flagged, flag_o, tagv, wc)
    const unsigned wa0 = sb + (wid * 32 + fr) * 128 + ((fq ^ (fr & 7)) << 4), wa1 = wa0 ^ 64u;
    const unsigned xa = sb + lane * 16;
    const unsigned wa0_0 = wa0 + 0u;
    const unsigned wa0_1 = wa0 + 65536u;
    const unsigned wa0_2 = wa0 + 131072u;
    const unsigned wa1_0 = wa1 + 0u;
    const unsigned wa1_1 = wa1 + 65536u;
    const unsigned wa1_2 = wa1 + 131072u;
    const unsigned xa_0 = xa + 0u;
    const unsigned xa_1 = xa + 65536u;
    const unsigned xa_2 = xa + 131072u;
    bf16x8 wr[16][4], fb[9];
    if (isq) {
      SIG(8);
      OWN_SIG(1);
    }
    if (isp) {
      const int row = wm * 64 + fr, xr = wn * 32 + fr;
      const unsigned pa0 = sb + row * 128 + ((fq ^ (row & 7)) << 4) + wn * 4096, pa1 = pa0 ^ 64u;
      const unsigned pb0 = sb + row * 128 + ((fq ^ (row & 7)) << 4) + (1 - wn) * 4096, pb1 = pb0 ^ 64u;
      const unsigned px0 = sb + xr * 128 + ((fq ^ (xr & 7)) << 4), px1 = px0 ^ 64u;
      f32x4 ac1[4][4];
      bf16x8 hw[2][4], hx[2][4];
      unsigned go[2];
      go[0] = (unsigned)((((h * 4 + (cp1 & 2) + wn) * 2 + (cp1 & 1)) * 16 + si) * A_B + wm * 8192 + wn * 4096 + (fq & 1) * 1024 + (fq >> 1) * 256 + fr * 16);
      go[1] = go[0] ^ 4096u;
#define P1_SETUP() asm volatile("" : "+v"(go[0]), "+v"(go[1]))
      unsigned pv[4];
#define PUB0(n, mi) asm volatile("v_cvt_pk_bf16_f32 %0, %2, %3\n\tv_cvt_pk_bf16_f32 %1, %4, %5" : "=&v"(pv[0]), "=&v"(pv[2])             \
                                 : "v"(ac1[n][mi][0]), "v"(ac1[n][mi][1]), "v"(ac1[(n) + 1][mi][0]), "v"(ac1[(n) + 1][mi][1]))
#define PUB1(n, mi) asm volatile("v_cvt_pk_bf16_f32 %0, %2, %3\n\tv_cvt_pk_bf16_f32 %1, %4, %5" : "=&v"(pv[1]), "=&v"(pv[3])             \
                                 : "v"(ac1[n][mi][2]), "v"(ac1[n][mi][3]), "v"(ac1[(n) + 1][mi][2]), "v"(ac1[(n) + 1][mi][3]))
#define PUB2() asm volatile("s_nop 1\n\tv_permlane16_swap_b32 %0, %2\n\tv_permlane16_swap_b32 %1, %3" : "+v"(pv[0]), "+v"(pv[1]), "+v"(pv[2]), "+v"(pv[3]))
#define PUB3(n, mi) asm volatile("global_store_dwordx4 %0, %1, %2 offset:%3 sc1\n\ts_nop 1" ::"v"(go[(mi) >> 1]), "v"(u32x4{pv[0], pv[1], pv[2], pv[3]}), "s"(ab), "i"(((mi) & 1) * 512 + (n) * 1024) : "memory")
      const unsigned pa0_0 = pa0 + 0u;
      const unsigned pa0_1 = pa0 + 65536u;
      const unsigned pa1_0 = pa1 + 0u;
      const unsigned pa1_1 = pa1 + 65536u;
      const unsigned pb0_0 = pb0 + 0u;
      const unsigned pb0_1 = pb0 + 65536u;
      const unsigned pb1_0 = pb1 + 0u;
      const unsigned pb1_1 = pb1 + 65536u;
      const unsigned px0_1 = px0 + 65536u;
      const unsigned px0_2 = px0 + 131072u;
      const unsigned px1_1 = px1 + 65536u;
      const unsigned px1_2 = px1 + 131072u;
      SPIN(lf_rd, 1); SB();
      lds_rd128<16384>(hx[0][0], px0_1); SB();
      lds_rd128<0>(hw[0][0], pa0_0); SB();
      lds_rd128<2048>(hw[0][1], pa0_0); SB();
      lds_rd128<0>(hw[0][2], pb0_0); SB();
      lds_rd128<2048>(hw[0][3], pb0_0); SB();
      lds_rd128<18432>(hx[0][1], px0_1); SB();
      lds_rd128<24576>(hx[0][2], px0_1); SB();
      lds_rd128<26624>(hx[0][3], px0_1); SB();
      mm0w<6>(ac1[0][0], hw[0][0], hx[0][0]); SB();
      lds_rd128<16384>(hx[1][0], px1_1); SB();
      mm0w<6>(ac1[0][1], hw[0][1], hx[0][0]); SB();
      lds_rd128<0>(hw[1][0], pa1_0); SB();
      mm0w<6>(ac1[0][2], hw[0][2], hx[0][0]); SB();
      lds_rd128<2048>(hw[1][1], pa1_0); SB();
      mm0w<6>(ac1[0][3], hw[0][3], hx[0][0]); SB();
      lds_rd128<0>(hw[1][2], pb1_0); SB();
      mm0w<6>(ac1[1][3], hw[0][3], hx[0][1]); SB();
      lds_rd128<2048>(hw[1][3], pb1_0); SB();
      mm0(ac1[1][2], hw[0][2], hx[0][1]); SB();
      lds_rd128<18432>(hx[1][1], px1_1); SB();
      mm0(ac1[1][1], hw[0][1], hx[0][1]); SB();
      lds_rd128<24576>(hx[1][2], px1_1); SB();
      mm0(ac1[1][0], hw[0][0], hx[0][1]); SB();
      lds_rd128<26624>(hx[1][3], px1_1); SB();
      mm0w<8>(ac1[2][0], hw[0][0], hx[0][2]); SB();
      mm0(ac1[2][1], hw[0][1], hx[0][2]); SB();
      mm0(ac1[2][2], hw[0][2], hx[0][2]); SB();
      mm0(ac1[2][3], hw[0][3], hx[0][2]); SB();
      mm0(ac1[3][3], hw[0][3], hx[0][3]); SB();
      mm0(ac1[3][2], hw[0][2], hx[0][3]); SB();
      asm volatile("s_waitcnt lgkmcnt(0)" : "+v"(hx[1][0]), "+v"(hw[1][0]), "+v"(hw[1][1]), "+v"(hw[1][2]), "+v"(hw[1][3]), "+v"(hx[1][1]), "+v"(hx[1][2]), "+v"(hx[1][3])::"memory"); SB();
      SIG(1); SB();
      mm0(ac1[3][1], hw[0][1], hx[0][3]); SB();
      mm0(ac1[3][0], hw[0][0], hx[0][3]); SB();
      mm(ac1[0][0], hw[1][0], hx[1][0]); SB();
      lds_rd32(fl, lf_rd); SB();
      mm(ac1[0][1], hw[1][1], hx[1][0]); SB();
      mm(ac1[0][2], hw[1][2], hx[1][0]); SB();
      mm(ac1[0][3], hw[1][3], hx[1][0]); SB();
      mm(ac1[1][3], hw[1][3], hx[1][1]); SB();
      mm(ac1[1][2], hw[1][2], hx[1][1]); SB();
      mmchkw<2, 0>(ac1[1][1], hw[1][1], hx[1][1], fl, lf_rd, bud); SB();
      lds_rd128<32768>(hx[0][0], px0_1); SB();
      mm(ac1[1][0], hw[1][0], hx[1][1]); SB();
      lds_rd128<16384>(hw[0][0], pa0_0); SB();
      mm(ac1[2][0], hw[1][0], hx[1][2]); SB();
      lds_rd128<18432>(hw[0][1], pa0_0); SB();
      mm(ac1[2][1], hw[1][1], hx[1][2]); SB();
      lds_rd128<16384>(hw[0][2], pb0_0); SB();
      mm(ac1[2][2], hw[1][2], hx[1][2]); SB();
      lds_rd128<18432>(hw[0][3], pb0_0); SB();
      mm(ac1[2][3], hw[1][3], hx[1][2]); SB();
      lds_rd128<34816>(hx[0][1], px0_1); SB();
      mm(ac1[3][3], hw[1][3], hx[1][3]); SB();
      lds_rd128<40960>(hx[0][2], px0_1); SB();
      mm(ac1[3][2], hw[1][2], hx[1][3]); SB();
      lds_rd128<43008>(hx[0][3], px0_1); SB();
      mm(ac1[3][1], hw[1][1], hx[1][3]); SB();
      mm(ac1[3][0], hw[1][0], hx[1][3]); SB();
      mmw<5>(ac1[0][0], hw[0][0], hx[0][0]); SB();
      lds_rd128<32768>(hx[1][0], px1_1); SB();
      mm(ac1[0][1], hw[0][1], hx[0][0]); SB();
      lds_rd128<16384>(hw[1][0], pa1_0); SB();
      mmw<5>(ac1[0][2], hw[0][2], hx[0][0]); SB();
      lds_rd128<18432>(hw[1][1], pa1_0); SB();
      mm(ac1[0][3], hw[0][3], hx[0][0]); SB();
      lds_rd128<16384>(hw[1][2], pb1_0); SB();
      mmw<5>(ac1[1][3], hw[0][3], hx[0][1]); SB();
      lds_rd128<18432>(hw[1][3], pb1_0); SB();
      mm(ac1[1][2], hw[0][2], hx[0][1]); SB();
      lds_rd128<34816>(hx[1][1], px1_1); SB();
      mm(ac1[1][1], hw[0][1], hx[0][1]); SB();
      lds_rd128<40960>(hx[1][2], px1_1); SB();
      mm(ac1[1][0], hw[0][0], hx[0][1]); SB();
      lds_rd128<43008>(hx[1][3], px1_1); SB();
      mm(ac1[2][0], hw[0][0], hx[0][2]); SB();
      mm(ac1[2][1], hw[0][1], hx[0][2]); SB();
      mm(ac1[2][2], hw[0][2], hx[0][2]); SB();
      mm(ac1[2][3], hw[0][3], hx[0][2]); SB();
      mmw<7>(ac1[3][3], hw[0][3], hx[0][3]); SB();
      mm(ac1[3][2], hw[0][2], hx[0][3]); SB();
      asm volatile("s_waitcnt lgkmcnt(0)" : "+v"(hw[1][0]), "+v"(hw[1][1]), "+v"(hw[1][2]), "+v"(hw[1][3]), "+v"(hx[1][1]), "+v"(hx[1][2]), "+v"(hx[1][3])::"memory"); SB();
      SIG(2); SB();
      mm(ac1[3][1], hw[0][1], hx[0][3]); SB();
      mm(ac1[3][0], hw[0][0], hx[0][3]); SB();
      mm(ac1[0][0], hw[1][0], hx[1][0]); SB();
      lds_rd32(fl, lf_rd); SB();
      mm(ac1[0][1], hw[1][1], hx[1][0]); SB();
      mm(ac1[0][2], hw[1][2], hx[1][0]); SB();
      mm(ac1[0][3], hw[1][3], hx[1][0]); SB();
      mm(ac1[1][3], hw[1][3], hx[1][1]); SB();
      mm(ac1[1][2], hw[1][2], hx[1][1]); SB();
      mmchkw<3, 0>(ac1[1][1], hw[1][1], hx[1][1], fl, lf_rd, bud); SB();
      lds_rd128<49152>(hx[0][0], px0_1); SB();
      mm(ac1[1][0], hw[1][0], hx[1][1]); SB();
      lds_rd128<32768>(hw[0][0], pa0_0); SB();
      mm(ac1[2][0], hw[1][0], hx[1][2]); SB();
      lds_rd128<34816>(hw[0][1], pa0_0); SB();
      mm(ac1[2][1], hw[1][1], hx[1][2]); SB();
      lds_rd128<32768>(hw[0][2], pb0_0); SB();
      mm(ac1[2][2], hw[1][2], hx[1][2]); SB();
      lds_rd128<34816>(hw[0][3], pb0_0); SB();
      mm(ac1[2][3], hw[1][3], hx[1][2]); SB();
      lds_rd128<51200>(hx[0][1], px0_1); SB();
      mm(ac1[3][3], hw[1][3], hx[1][3]); SB();
      lds_rd128<57344>(hx[0][2], px0_1); SB();
      mm(ac1[3][2], hw[1][2], hx[1][3]); SB();
      lds_rd128<59392>(hx[0][3], px0_1); SB();
      mm(ac1[3][1], hw[1][1], hx[1][3]); SB();
      mm(ac1[3][0], hw[1][0], hx[1][3]); SB();
      mmw<5>(ac1[0][0], hw[0][0], hx[0][0]); SB();
      lds_rd128<49152>(hx[1][0], px1_1); SB();
      mm(ac1[0][1], hw[0][1], hx[0][0]); SB();
      lds_rd128<32768>(hw[1][0], pa1_0); SB();
      mmw<5>(ac1[0][2], hw[0][2], hx[0][0]); SB();
      lds_rd128<34816>(hw[1][1], pa1_0); SB();
      mm(ac1[0][3], hw[0][3], hx[0][0]); SB();
      lds_rd128<32768>(hw[1][2], pb1_0); SB();
      mmw<5>(ac1[1][3], hw[0][3], hx[0][1]); SB();
      lds_rd128<34816>(hw[1][3], pb1_0); SB();
      mm(ac1[1][2], hw[0][2], hx[0][1]); SB();
      lds_rd128<51200>(hx[1][1], px1_1); SB();
      mm(ac1[1][1], hw[0][1], hx[0][1]); SB();
      lds_rd128<57344>(hx[1][2], px1_1); SB();
      mm(ac1[1][0], hw[0][0], hx[0][1]); SB();
      lds_rd128<59392>(hx[1][3], px1_1); SB();
      mm(ac1[2][0], hw[0][0], hx[0][2]); SB();
      mm(ac1[2][1], hw[0][1], hx[0][2]); SB();
      mm(ac1[2][2], hw[0][2], hx[0][2]); SB();
      mm(ac1[2][3], hw[0][3], hx[0][2]); SB();
      mmw<7>(ac1[3][3], hw[0][3], hx[0][3]); SB();
      mm(ac1[3][2], hw[0][2], hx[0][3]); SB();
      asm volatile("s_waitcnt lgkmcnt(0)" : "+v"(hw[1][0]), "+v"(hw[1][1]), "+v"(hw[1][2]), "+v"(hw[1][3]), "+v"(hx[1][1]), "+v"(hx[1][2]), "+v"(hx[1][3])::"memory"); SB();
      SIG(3); SB();
      mm(ac1[3][1], hw[0][1], hx[0][3]); SB();
      mm(ac1[3][0], hw[0][0], hx[0][3]); SB();
      mm(ac1[0][0], hw[1][0], hx[1][0]); SB();
      lds_rd32(fl, lf_rd); SB();
      mm(ac1[0][1], hw[1][1], hx[1][0]); SB();
      mm(ac1[0][2], hw[1][2], hx[1][0]); SB();
      mm(ac1[0][3], hw[1][3], hx[1][0]); SB();
      mm(ac1[1][3], hw[1][3], hx[1][1]); SB();
      mm(ac1[1][2], hw[1][2], hx[1][1]); SB();
      mmchkw<4, 0>(ac1[1][1], hw[1][1], hx[1][1], fl, lf_rd, bud); SB();
      lds_rd128<0>(hx[0][0], px0_2); SB();
      mm(ac1[1][0], hw[1][0], hx[1][1]); SB();
      lds_rd128<49152>(hw[0][0], pa0_0); SB();
      mm(ac1[2][0], hw[1][0], hx[1][2]); SB();
      lds_rd128<51200>(hw[0][1], pa0_0); SB();
      mm(ac1[2][1], hw[1][1], hx[1][2]); SB();
      lds_rd128<49152>(hw[0][2], pb0_0); SB();
      mm(ac1[2][2], hw[1][2], hx[1][2]); SB();
      lds_rd128<51200>(hw[0][3], pb0_0); SB();
      mm(ac1[2][3], hw[1][3], hx[1][2]); SB();
      lds_rd128<2048>(hx[0][1], px0_2); SB();
      mm(ac1[3][3], hw[1][3], hx[1][3]); SB();
      lds_rd128<8192>(hx[0][2], px0_2); SB();
      mm(ac1[3][2], hw[1][2], hx[1][3]); SB();
      lds_rd128<10240>(hx[0][3], px0_2); SB();
      mm(ac1[3][1], hw[1][1], hx[1][3]); SB();
      mm(ac1[3][0], hw[1][0], hx[1][3]); SB();
      mmw<5>(ac1[0][0], hw[0][0], hx[0][0]); SB();
      lds_rd128<0>(hx[1][0], px1_2); SB();
      mm(ac1[0][1], hw[0][1], hx[0][0]); SB();
      lds_rd128<49152>(hw[1][0], pa1_0); SB();
      mmw<5>(ac1[0][2], hw[0][2], hx[0][0]); SB();
      lds_rd128<51200>(hw[1][1], pa1_0); SB();
      mm(ac1[0][3], hw[0][3], hx[0][0]); SB();
      lds_rd128<49152>(hw[1][2], pb1_0); SB();
      mmw<5>(ac1[1][3], hw[0][3], hx[0][1]); SB();
      lds_rd128<51200>(hw[1][3], pb1_0); SB();
      mm(ac1[1][2], hw[0][2], hx[0][1]); SB();
      lds_rd128<2048>(hx[1][1], px1_2); SB();
      mm(ac1[1][1], hw[0][1], hx[0][1]); SB();
      lds_rd128<8192>(hx[1][2], px1_2); SB();
      mm(ac1[1][0], hw[0][0], hx[0][1]); SB();
      lds_rd128<10240>(hx[1][3], px1_2); SB();
      mm(ac1[2][0], hw[0][0], hx[0][2]); SB();
      mm(ac1[2][1], hw[0][1], hx[0][2]); SB();
      mm(ac1[2][2], hw[0][2], hx[0][2]); SB();
      mm(ac1[2][3], hw[0][3], hx[0][2]); SB();
      mmw<7>(ac1[3][3], hw[0][3], hx[0][3]); SB();
      mm(ac1[3][2], hw[0][2], hx[0][3]); SB();
      asm volatile("s_waitcnt lgkmcnt(0)" : "+v"(hw[1][0]), "+v"(hw[1][1]), "+v"(hw[1][2]), "+v"(hw[1][3]), "+v"(hx[1][1]), "+v"(hx[1][2]), "+v"(hx[1][3])::"memory"); SB();
      SIG(4); SB();
      mm(ac1[3][1], hw[0][1], hx[0][3]); SB();
      mm(ac1[3][0], hw[0][0], hx[0][3]); SB();
      mm(ac1[0][0], hw[1][0], hx[1][0]); SB();
      lds_rd32(fl, lf_rd); SB();
      mm(ac1[0][1], hw[1][1], hx[1][0]); SB();
      mm(ac1[0][2], hw[1][2], hx[1][0]); SB();
      mm(ac1[0][3], hw[1][3], hx[1][0]); SB();
      mm(ac1[1][3], hw[1][3], hx[1][1]); SB();
      mm(ac1[1][2], hw[1][2], hx[1][1]); SB();
      mmchkw<5, 0>(ac1[1][1], hw[1][1], hx[1][1], fl, lf_rd, bud); SB();
      lds_rd128<16384>(hx[0][0], px0_1); SB();
      mm(ac1[1][0], hw[1][0], hx[1][1]); SB();
      lds_rd128<0>(hw[0][0], pa0_1); SB();
      mm(ac1[2][0], hw[1][0], hx[1][2]); SB();
      lds_rd128<2048>(hw[0][1], pa0_1); SB();
      mm(ac1[2][1], hw[1][1], hx[1][2]); SB();
      lds_rd128<0>(hw[0][2], pb0_1); SB();
      mm(ac1[2][2], hw[1][2], hx[1][2]); SB();
      lds_rd128<2048>(hw[0][3], pb0_1); SB();
      mm(ac1[2][3], hw[1][3], hx[1][2]); SB();
      lds_rd128<18432>(hx[0][1], px0_1); SB();
      mm(ac1[3][3], hw[1][3], hx[1][3]); SB();
      lds_rd128<24576>(hx[0][2], px0_1); SB();
      mm(ac1[3][2], hw[1][2], hx[1][3]); SB();
      lds_rd128<26624>(hx[0][3], px0_1); SB();
      mm(ac1[3][1], hw[1][1], hx[1][3]); SB();
      mm(ac1[3][0], hw[1][0], hx[1][3]); SB();
      mmw<5>(ac1[0][0], hw[0][0], hx[0][0]); SB();
      lds_rd128<16384>(hx[1][0], px1_1); SB();
      mm(ac1[0][1], hw[0][1], hx[0][0]); SB();
      lds_rd128<0>(hw[1][0], pa1_1); SB();
      mmw<5>(ac1[0][2], hw[0][2], hx[0][0]); SB();
      lds_rd128<2048>(hw[1][1], pa1_1); SB();
      mm(ac1[0][3], hw[0][3], hx[0][0]); SB();
      lds_rd128<0>(hw[1][2], pb1_1); SB();
      mmw<5>(ac1[1][3], hw[0][3], hx[0][1]); SB();
      lds_rd128<2048>(hw[1][3], pb1_1); SB();
      mm(ac1[1][2], hw[0][2], hx[0][1]); SB();
      lds_rd128<18432>(hx[1][1], px1_1); SB();
      mm(ac1[1][1], hw[0][1], hx[0][1]); SB();
      lds_rd128<24576>(hx[1][2], px1_1); SB();
      mm(ac1[1][0], hw[0][0], hx[0][1]); SB();
      lds_rd128<26624>(hx[1][3], px1_1); SB();
      mm(ac1[2][0], hw[0][0], hx[0][2]); SB();
      mm(ac1[2][1], hw[0][1], hx[0][2]); SB();
      mm(ac1[2][2], hw[0][2], hx[0][2]); SB();
      mm(ac1[2][3], hw[0][3], hx[0][2]); SB();
      mmw<7>(ac1[3][3], hw[0][3], hx[0][3]); SB();
      mm(ac1[3][2], hw[0][2], hx[0][3]); SB();
      asm volatile("s_waitcnt lgkmcnt(0)" : "+v"(hw[1][0]), "+v"(hw[1][1]), "+v"(hw[1][2]), "+v"(hw[1][3]), "+v"(hx[1][1]), "+v"(hx[1][2]), "+v"(hx[1][3])::"memory"); SB();
      SIG(5); SB();
      mm(ac1[3][1], hw[0][1], hx[0][3]); SB();
      mm(ac1[3][0], hw[0][0], hx[0][3]); SB();
      P1_SETUP(); SB();
      mm(ac1[0][0], hw[1][0], hx[1][0]); SB();
      lds_rd32(fl, lf_rd); SB();
      mm(ac1[0][1], hw[1][1], hx[1][0]); SB();
      mm(ac1[0][2], hw[1][2], hx[1][0]); SB();
      mm(ac1[0][3], hw[1][3], hx[1][0]); SB();
      mm(ac1[1][3], hw[1][3], hx[1][1]); SB();
      mm(ac1[1][2], hw[1][2], hx[1][1]); SB();
      mmchkw<6, 0>(ac1[1][1], hw[1][1], hx[1][1], fl, lf_rd, bud); SB();
      lds_rd128<32768>(hx[0][0], px0_1); SB();
      mm(ac1[1][0], hw[1][0], hx[1][1]); SB();
      lds_rd128<0>(hw[0][0], pa0_0); SB();
      mm(ac1[2][0], hw[1][0], hx[1][2]); SB();
      lds_rd128<2048>(hw[0][1], pa0_0); SB();
      mm(ac1[2][1], hw[1][1], hx[1][2]); SB();
      lds_rd128<0>(hw[0][2], pb0_0); SB();
      mm(ac1[2][2], hw[1][2], hx[1][2]); SB();
      lds_rd128<2048>(hw[0][3], pb0_0); SB();
      mm(ac1[2][3], hw[1][3], hx[1][2]); SB();
      lds_rd128<34816>(hx[0][1], px0_1); SB();
      mm(ac1[3][3], hw[1][3], hx[1][3]); SB();
      lds_rd128<40960>(hx[0][2], px0_1); SB();
      mm(ac1[3][2], hw[1][2], hx[1][3]); SB();
      lds_rd128<43008>(hx[0][3], px0_1); SB();
      mm(ac1[3][1], hw[1][1], hx[1][3]); SB();
      mm(ac1[3][0], hw[1][0], hx[1][3]); SB();
      mmw<5>(ac1[0][0], hw[0][0], hx[0][0]); SB();
      lds_rd128<32768>(hx[1][0], px1_1); SB();
      mm(ac1[0][1], hw[0][1], hx[0][0]); SB();
      lds_rd128<0>(hw[1][0], pa1_0); SB();
      mmw<5>(ac1[0][2], hw[0][2], hx[0][0]); SB();
      lds_rd128<2048>(hw[1][1], pa1_0); SB();
      mm(ac1[0][3], hw[0][3], hx[0][0]); SB();
      lds_rd128<0>(hw[1][2], pb1_0); SB();
      mmw<5>(ac1[1][3], hw[0][3], hx[0][1]); SB();
      lds_rd128<2048>(hw[1][3], pb1_0); SB();
      mm(ac1[1][2], hw[0][2], hx[0][1]); SB();
      lds_rd128<34816>(hx[1][1], px1_1); SB();
      mm(ac1[1][1], hw[0][1], hx[0][1]); SB();
      lds_rd128<40960>(hx[1][2], px1_1); SB();
      mm(ac1[1][0], hw[0][0], hx[0][1]); SB();
      lds_rd128<43008>(hx[1][3], px1_1); SB();
      mm(ac1[2][0], hw[0][0], hx[0][2]); SB();
      mm(ac1[2][1], hw[0][1], hx[0][2]); SB();
      mm(ac1[2][2], hw[0][2], hx[0][2]); SB();
      mm(ac1[2][3], hw[0][3], hx[0][2]); SB();
      mmw<7>(ac1[3][3], hw[0][3], hx[0][3]); SB();
      mm(ac1[3][2], hw[0][2], hx[0][3]); SB();
      asm volatile("s_waitcnt lgkmcnt(0)" : "+v"(hw[1][0]), "+v"(hw[1][1]), "+v"(hw[1][2]), "+v"(hw[1][3]), "+v"(hx[1][1]), "+v"(hx[1][2]), "+v"(hx[1][3])::"memory"); SB();
      SIG(6); SB();
      mm(ac1[3][1], hw[0][1], hx[0][3]); SB();
      mm(ac1[3][0], hw[0][0], hx[0][3]); SB();
      mm(ac1[0][0], hw[1][0], hx[1][0]); SB();
      lds_rd32(fl, lf_rd); SB();
      mm(ac1[0][1], hw[1][1], hx[1][0]); SB();
      mm(ac1[0][2], hw[1][2], hx[1][0]); SB();
      mm(ac1[0][3], hw[1][3], hx[1][0]); SB();
      mm(ac1[1][3], hw[1][3], hx[1][1]); SB();
      mm(ac1[1][2], hw[1][2], hx[1][1]); SB();
      mmchkw<7, 0>(ac1[1][1], hw[1][1], hx[1][1], fl, lf_rd, bud); SB();
      lds_rd128<49152>(hx[0][0], px0_1); SB();
      mm(ac1[1][0], hw[1][0], hx[1][1]); SB();
      lds_rd128<16384>(hw[0][0], pa0_0); SB();
      mm(ac1[2][0], hw[1][0], hx[1][2]); SB();
      lds_rd128<18432>(hw[0][1], pa0_0); SB();
      mm(ac1[2][1], hw[1][1], hx[1][2]); SB();
      lds_rd128<16384>(hw[0][2], pb0_0); SB();
      mm(ac1[2][2], hw[1][2], hx[1][2]); SB();
      lds_rd128<18432>(hw[0][3], pb0_0); SB();
      mm(ac1[2][3], hw[1][3], hx[1][2]); SB();
      lds_rd128<51200>(hx[0][1], px0_1); SB();
      mm(ac1[3][3], hw[1][3], hx[1][3]); SB();
      lds_rd128<57344>(hx[0][2], px0_1); SB();
      mm(ac1[3][2], hw[1][2], hx[1][3]); SB();
      lds_rd128<59392>(hx[0][3], px0_1); SB();
      mm(ac1[3][1], hw[1][1], hx[1][3]); SB();
      mm(ac1[3][0], hw[1][0], hx[1][3]); SB();
      mmw<5>(ac1[0][0], hw[0][0], hx[0][0]); SB();
      lds_rd128<49152>(hx[1][0], px1_1); SB();
      mm(ac1[0][1], hw[0][1], hx[0][0]); SB();
      lds_rd128<16384>(hw[1][0], pa1_0); SB();
      mmw<5>(ac1[0][2], hw[0][2], hx[0][0]); SB();
      lds_rd128<18432>(hw[1][1], pa1_0); SB();
      mm(ac1[0][3], hw[0][3], hx[0][0]); SB();
      lds_rd128<16384>(hw[1][2], pb1_0); SB();
      mmw<5>(ac1[1][3], hw[0][3], hx[0][1]); SB();
      lds_rd128<18432>(hw[1][3], pb1_0); SB();
      mm(ac1[1][2], hw[0][2], hx[0][1]); SB();
      lds_rd128<51200>(hx[1][1], px1_1); SB();
      mm(ac1[1][1], hw[0][1], hx[0][1]); SB();
      lds_rd128<57344>(hx[1][2], px1_1); SB();
      mm(ac1[1][0], hw[0][0], hx[0][1]); SB();
      lds_rd128<59392>(hx[1][3], px1_1); SB();
      mm(ac1[2][0], hw[0][0], hx[0][2]); SB();
      mm(ac1[2][1], hw[0][1], hx[0][2]); SB();
      mm(ac1[2][2], hw[0][2], hx[0][2]); SB();
      mm(ac1[2][3], hw[0][3], hx[0][2]); SB();
      mmw<7>(ac1[3][3], hw[0][3], hx[0][3]); SB();
      mm(ac1[3][2], hw[0][2], hx[0][3]); SB();
      asm volatile("s_waitcnt lgkmcnt(0)" : "+v"(hw[1][0]), "+v"(hw[1][1]), "+v"(hw[1][2]), "+v"(hw[1][3]), "+v"(hx[1][1]), "+v"(hx[1][2]), "+v"(hx[1][3])::"memory"); SB();
      SIG(7); SB();
      mm(ac1[3][1], hw[0][1], hx[0][3]); SB();
      mm(ac1[3][0], hw[0][0], hx[0][3]); SB();
      mm(ac1[0][0], hw[1][0], hx[1][0]); SB();
      lds_rd32(fl, lf_rd); SB();
      mm(ac1[0][1], hw[1][1], hx[1][0]); SB();
      mm(ac1[0][2], hw[1][2], hx[1][0]); SB();
      mm(ac1[0][3], hw[1][3], hx[1][0]); SB();
      mm(ac1[1][3], hw[1][3], hx[1][1]); SB();
      mm(ac1[1][2], hw[1][2], hx[1][1]); SB();
      mmchkw<8, 0>(ac1[1][1], hw[1][1], hx[1][1], fl, lf_rd, bud); SB();
      lds_rd128<0>(hx[0][0], px0_2); SB();
      mm(ac1[1][0], hw[1][0], hx[1][1]); SB();
      lds_rd128<32768>(hw[0][0], pa0_0); SB();
      mm(ac1[2][0], hw[1][0], hx[1][2]); SB();
      lds_rd128<34816>(hw[0][1], pa0_0); SB();
      mm(ac1[2][1], hw[1][1], hx[1][2]); SB();
      lds_rd128<32768>(hw[0][2], pb0_0); SB();
      mm(ac1[2][2], hw[1][2], hx[1][2]); SB();
      lds_rd128<34816>(hw[0][3], pb0_0); SB();
      mm(ac1[2][3], hw[1][3], hx[1][2]); SB();
      lds_rd128<2048>(hx[0][1], px0_2); SB();
      mm(ac1[3][3], hw[1][3], hx[1][3]); SB();
      lds_rd128<8192>(hx[0][2], px0_2); SB();
      mm(ac1[3][2], hw[1][2], hx[1][3]); SB();
      lds_rd128<10240>(hx[0][3], px0_2); SB();
      mm(ac1[3][1], hw[1][1], hx[1][3]); SB();
      mm(ac1[3][0], hw[1][0], hx[1][3]); SB();
      mmw<5>(ac1[0][0], hw[0][0], hx[0][0]); SB();
      lds_rd128<0>(hx[1][0], px1_2); SB();
      mmw<3>(ac1[1][0], hw[0][0], hx[0][1]); SB();
      lds_rd128<32768>(hw[1][0], pa1_0); SB();
      mm(ac1[0][1], hw[0][1], hx[0][0]); SB();
      lds_rd128<34816>(hw[1][1], pa1_0); SB();
      mm(ac1[1][1], hw[0][1], hx[0][1]); SB();
      lds_rd128<32768>(hw[1][2], pb1_0); SB();
      mm(ac1[0][2], hw[0][2], hx[0][0]); SB();
      lds_rd128<34816>(hw[1][3], pb1_0); SB();
      mm(ac1[1][2], hw[0][2], hx[0][1]); SB();
      lds_rd128<2048>(hx[1][1], px1_2); SB();
      mm(ac1[0][3], hw[0][3], hx[0][0]); SB();
      lds_rd128<8192>(hx[1][2], px1_2); SB();
      mm(ac1[1][3], hw[0][3], hx[0][1]); SB();
      lds_rd128<10240>(hx[1][3], px1_2); SB();
      mmw<6>(ac1[0][0], hw[1][0], hx[1][0]); SB();
      mmw<2>(ac1[1][0], hw[1][0], hx[1][1]); SB();
      mm(ac1[0][1], hw[1][1], hx[1][0]); SB();
      mm(ac1[1][1], hw[1][1], hx[1][1]); SB();
      mm(ac1[0][2], hw[1][2], hx[1][0]); SB();
      asm volatile("s_waitcnt lgkmcnt(0)" : "+v"(hx[1][2]), "+v"(hx[1][3])::"memory"); SB();
      SIG(8); SB();
      mm(ac1[1][2], hw[1][2], hx[1][1]); SB();
      PUB0(0, 0); SB();
      mm(ac1[0][3], hw[1][3], hx[1][0]); SB();
      PUB1(0, 0); SB();
      mm(ac1[1][3], hw[1][3], hx[1][1]); SB();
      PUB2(); SB();
      mm(ac1[2][0], hw[0][0], hx[0][2]); SB();
      PUB3(0, 0); SB();
      mm(ac1[3][0], hw[0][0], hx[0][3]); SB();
      PUB0(0, 1); SB();
      mm(ac1[2][0], hw[1][0], hx[1][2]); SB();
      PUB1(0, 1); SB();
      mm(ac1[3][0], hw[1][0], hx[1][3]); SB();
      PUB2(); SB();
      mm(ac1[2][1], hw[0][1], hx[0][2]); SB();
      PUB3(0, 1); SB();
      mm(ac1[3][1], hw[0][1], hx[0][3]); SB();
      PUB0(0, 2); SB();
      mm(ac1[2][1], hw[1][1], hx[1][2]); SB();
      PUB1(0, 2); SB();
      mm(ac1[3][1], hw[1][1], hx[1][3]); SB();
      PUB2(); SB();
      mm(ac1[2][2], hw[0][2], hx[0][2]); SB();
      PUB3(0, 2); SB();
      mm(ac1[3][2], hw[0][2], hx[0][3]); SB();
      PUB0(0, 3); SB();
      mm(ac1[2][2], hw[1][2], hx[1][2]); SB();
      PUB1(0, 3); SB();
      mm(ac1[3][2], hw[1][2], hx[1][3]); SB();
      PUB2(); SB();
      mm(ac1[2][3], hw[0][3], hx[0][2]); SB();
      PUB3(0, 3); SB();
      mm(ac1[3][3], hw[0][3], hx[0][3]); SB();
      PUB0(2, 0); SB();
      mm(ac1[2][3], hw[1][3], hx[1][2]); SB();
      PUB1(2, 0); SB();
      mm(ac1[3][3], hw[1][3], hx[1][3]); SB();
      PUB2(); SB();
      asm volatile("s_nop 7" : "+v"(ac1[2][3]), "+v"(ac1[3][3]), "+v"(ac1[2][2]), "+v"(ac1[3][2])); SB();
      PUB3(2, 0); SB();
      PUB0(2, 1); SB();
      PUB1(2, 1); SB();
      PUB2(); SB();
      PUB3(2, 1); SB();
      PUB0(2, 2); SB();
      PUB1(2, 2); SB();
      PUB2(); SB();
      PUB3(2, 2); SB();
      PUB0(2, 3); SB();
      PUB1(2, 3); SB();
      PUB2(); SB();
      PUB3(2, 3); SB();
      OWN_SIG(1);
    }
    SPIN(tg_rd, 1);
    const unsigned tag = __builtin_amdgcn_readfirstlane(lflag[15]);
    tagv = tag;
    asm volatile("" : "+v"(tagv));
    f32x4 ac[4][2][2];
    const unsigned xg = (unsigned)((ti * 4 + ej) * 98304 + q * 24576 + wid * 2048 + lane * 16);
    const char* xb = ws + W_XB;
    const unsigned hw_ = sb + 147456 + (wid * 32 + fr) * 64 + (((fq >> 1) ^ (fr >> 2)) << 4) + (fq & 1) * 8;
    const unsigned tb[4] = {tag & 0x70007u, tag >> 3 & 0x70007u, tag >> 6 & 0x70007u, tag >> 9 & 0x70004u};
    unsigned pw[8], em[2], ek[2], msk = 0x70007u;
    u64 nz;
#define AV(G, i) ac[G][(i) >> 2 & 1][(i) >> 3][(i) & 3]
#define EM0(G, c) asm volatile("v_max3_f32 %0, |%1|, |%2|, |%3|" : "=v"(em[c]) : "v"(AV(G, 8 * (c))), "v"(AV(G, 8 * (c) + 1)), "v"(AV(G, 8 * (c) + 2)))
#define EM1(G, c, i, j) asm volatile("v_max3_f32 %0, %0, |%1|, |%2|" : "+v"(em[c]) : "v"(AV(G, i)), "v"(AV(G, j)))
#define EM2() asm volatile("v_max_f32 %0, %0, %1" : "+v"(em[0]) : "v"(em[1]))
#define ES0() asm volatile("v_subrev_u32 %0, 0x39800000, %1" : "=v"(em[1]) : "v"(em[0]))
#define ES1() asm volatile("v_cmp_ne_u32 %0, 0, %1" : "=s"(nz) : "v"(em[0]))
#define EK(c) asm volatile("v_or_b32 %0, %1, %2" : "=v"(ek[c]) : "i"(2 * (c)), "s"(tb[3]))
#define EH(G, P) asm volatile("v_cvt_pk_f16_f32 %0, %1, %2" : "=v"(pw[P]) : "v"(AV(G, 2 * (P))), "v"(AV(G, 2 * (P) + 1)))
#define ET(P) asm volatile("v_bfi_b32 %0, %1, %2, %0" : "+v"(pw[P]) : "v"(msk), "s"(tb[(P) & 3]))
#define EU(P) asm volatile("v_bfi_b32 %0, %1, %2, %0" : "+v"(pw[P]) : "v"(msk), "v"(ek[(P) >> 2]))
#define EX(G) asm volatile("v_cmp_le_u32 vcc, 0x0d800000, %17\n\ts_and_b64 vcc, vcc, %20\n\ts_cbranch_scc1 .Lxa%=\n.Lxb%=:\n\t.subsection 1\n.Lxa%=:\n\t"            \
      "v_lshrrev_b32 %17, 23, %16\n\tv_add_u32 %17, 6, %17\n\tv_lshrrev_b32 %17, 4, %17\n\tv_med3_u32 %17, %17, 1, 15\n\tv_lshlrev_b32 %16, 27, %17\n\t"              \
      "v_sub_u32 %16, 0x7f800000, %16\n\tv_and_or_b32 %18, %17, 3, %21\n\tv_lshrrev_b32 %17, 2, %17\n\tv_or_b32 %19, %21, %17\n\t"                                  \
      "v_mul_f32 %0, %0, %16\n\tv_mul_f32 %1, %1, %16\n\tv_mul_f32 %2, %2, %16\n\tv_mul_f32 %3, %3, %16\n\tv_mul_f32 %4, %4, %16\n\tv_mul_f32 %5, %5, %16\n\t"       \
      "v_mul_f32 %6, %6, %16\n\tv_mul_f32 %7, %7, %16\n\tv_mul_f32 %8, %8, %16\n\tv_mul_f32 %9, %9, %16\n\tv_mul_f32 %10, %10, %16\n\tv_mul_f32 %11, %11, %16\n\t"   \
      "v_mul_f32 %12, %12, %16\n\tv_mul_f32 %13, %13, %16\n\tv_mul_f32 %14, %14, %16\n\tv_mul_f32 %15, %15, %16\n\ts_branch .Lxb%=\n\t.subsection 0"               \
      : "+v"(AV(G, 0)), "+v"(AV(G, 1)), "+v"(AV(G, 2)), "+v"(AV(G, 3)), "+v"(AV(G, 4)), "+v"(AV(G, 5)), "+v"(AV(G, 6)), "+v"(AV(G, 7)), "+v"(AV(G, 8)), "+v"(AV(G, 9)),  \
        "+v"(AV(G, 10)), "+v"(AV(G, 11)), "+v"(AV(G, 12)), "+v"(AV(G, 13)), "+v"(AV(G, 14)), "+v"(AV(G, 15)), "+v"(em[0]), "+v"(em[1]), "+v"(ek[0]), "+v"(ek[1])        \
      : "s"(nz), "s"(tb[3]) : "vcc", "scc")
    const char *xd0 = xb + ((q ^ 3) - q) * 24576, *xd1 = xb + ((q ^ 2) - q) * 24576 + 8192, *xd2 = xb + ((q ^ 1) - q) * 24576 + 16384;
    const char *xr0 = xb, *xr1 = xb + 8192, *xr2 = xb + 16384;
    asm volatile("" : "+s"(xd0), "+s"(xd1), "+s"(xd2), "+s"(xr0), "+s"(xr1), "+s"(xr2));
#define SEND(G, Q) asm volatile("global_store_dwordx4 %0, %1, %2 offset:%3 sc1\n\ts_nop 1" ::"v"(xg), "v"(u32x4{pw[4 * (Q)], pw[4 * (Q) + 1], pw[4 * (Q) + 2], pw[4 * (Q) + 3]}), "s"(xd##G), "i"((Q) * 1024) : "memory")
    unsigned oo;
#define OO() do { oo = (unsigned)(((ti * 128 + wid * 32 + fr) * D_ + e0 + (fq & 1) * 16 + (fq >> 1) * 8) * 2); asm volatile("" : "+v"(oo)); } while (0)
    unsigned sn = 9 * 4u;
    float gs[2];
    u32x4 rv[3][2];
    u32x2 dq[4];
    unsigned f2;
    const unsigned d2_rd = best_rd;
#define RV_LD(S, tail) asm volatile("global_load_dwordx4 %0, %2, %3 sc1\n\tglobal_load_dwordx4 %1, %2, %3 offset:1024 sc1" tail \
                                    : "=&v"(rv[S][0]), "=&v"(rv[S][1]) : "v"(xg), "s"(xr##S) : "memory")
#define D_RD(i) asm volatile("ds_read_b64 %0, %1 offset:%2" : "=v"(dq[i]) : "v"(hw_ ^ ((i) & 1) * 32u), "i"(((i) >> 1) * 1024) : "memory")
#define G_RD(i) asm volatile("ds_read_b32 %0, %1 offset:%2" : "=v"(gs[i]) : "v"(sb + RING + L_G + (wid * 32 + fr) * 4), "i"((i) * 64) : "memory")
#define SIGQ(W) do { __builtin_amdgcn_s_waitcnt(0xc07f | ((W) << 8)); SB(); asm volatile("global_load_lds_dword %0, %1" ::"v"(sn), "s"(tab) : "memory"); } while (0)
#define SIGN() asm volatile("v_add_u32 %0, 4, %0" : "+v"(sn))
    asm volatile("s_mov_b32 m0, %2" : "+v"(sn), "+v"(msk) : "s"(row_s) : "memory");
    SPINF(lf_rd, 9, 0); SB();
    lds_rd128<49152>(wr[0][0], wa0_0); SB();
    lds_rd128<51200>(wr[0][1], wa0_0); SB();
    lds_rd128<49152>(wr[0][2], wa1_0); SB();
    lds_rd128<51200>(wr[0][3], wa1_0); SB();
    lds_rd128<0>(wr[1][0], wa0_1); SB();
    lds_rd128<2048>(wr[1][1], wa0_1); SB();
    lds_rd128<0>(wr[1][2], wa1_1); SB();
    lds_rd128<2048>(wr[1][3], wa1_1); SB();
    asm volatile("s_waitcnt lgkmcnt(0)" : "+v"(wr[0][0]), "+v"(wr[0][1]), "+v"(wr[0][2]), "+v"(wr[0][3]), "+v"(wr[1][0]), "+v"(wr[1][1]), "+v"(wr[1][2]), "+v"(wr[1][3])::"memory"); SB();
    OWN_SIG(2); SB();
    TRYFLAG(0); SB();
    SPINF(lf_rd, 10, 0); SB();
    lds_rd128<16384>(wr[2][0], wa0_1); SB();
    lds_rd128<18432>(wr[2][1], wa0_1); SB();
    lds_rd128<16384>(wr[2][2], wa1_1); SB();
    lds_rd128<18432>(wr[2][3], wa1_1); SB();
    lds_rd128<0>(wr[3][0], wa0_0); SB();
    lds_rd128<2048>(wr[3][1], wa0_0); SB();
    lds_rd128<0>(wr[3][2], wa1_0); SB();
    lds_rd128<2048>(wr[3][3], wa1_0); SB();
    asm volatile("s_waitcnt lgkmcnt(0)" : "+v"(wr[2][0]), "+v"(wr[2][1]), "+v"(wr[2][2]), "+v"(wr[2][3]), "+v"(wr[3][0]), "+v"(wr[3][1]), "+v"(wr[3][2]), "+v"(wr[3][3])::"memory"); SB();
    OWN_SIG(3); SB();
    TRYFLAG(0); SB();
    SPINF(lf_rd, 11, 0); SB();
    lds_rd128<32768>(wr[4][0], wa0_1); SB();
    lds_rd128<34816>(wr[4][1], wa0_1); SB();
    lds_rd128<32768>(wr[4][2], wa1_1); SB();
    lds_rd128<34816>(wr[4][3], wa1_1); SB();
    lds_rd128<16384>(wr[5][0], wa0_0); SB();
    lds_rd128<18432>(wr[5][1], wa0_0); SB();
    lds_rd128<16384>(wr[5][2], wa1_0); SB();
    lds_rd128<18432>(wr[5][3], wa1_0); SB();
    asm volatile("s_waitcnt lgkmcnt(0)" : "+v"(wr[4][0]), "+v"(wr[4][1]), "+v"(wr[4][2]), "+v"(wr[4][3]), "+v"(wr[5][0]), "+v"(wr[5][1]), "+v"(wr[5][2]), "+v"(wr[5][3])::"memory"); SB();
    OWN_SIG(4); SB();
    TRYFLAG(0); SB();
    SPINF(lf_rd, 12, 0); SB();
    lds_rd128<49152>(wr[6][0], wa0_1); SB();
    lds_rd128<51200>(wr[6][1], wa0_1); SB();
    lds_rd128<49152>(wr[6][2], wa1_1); SB();
    lds_rd128<51200>(wr[6][3], wa1_1); SB();
    lds_rd128<32768>(wr[7][0], wa0_0); SB();
    lds_rd128<34816>(wr[7][1], wa0_0); SB();
    lds_rd128<32768>(wr[7][2], wa1_0); SB();
    lds_rd128<34816>(wr[7][3], wa1_0); SB();
    asm volatile("s_waitcnt lgkmcnt(0)" : "+v"(wr[6][0]), "+v"(wr[6][1]), "+v"(wr[6][2]), "+v"(wr[6][3]), "+v"(wr[7][0]), "+v"(wr[7][1]), "+v"(wr[7][2]), "+v"(wr[7][3])::"memory"); SB();
    OWN_SIG(5); SB();
    TRYFLAG(0); SB();
    FLAG(0); SB();
    OO();
    SPIN(lf_rd, 13); SB();
    lds_rd128<0>(fb[0], xa_0); SB();
    lds_rd128<1024>(fb[1], xa_0); SB();
    lds_rd128<2048>(fb[2], xa_0); SB();
    lds_rd128<3072>(fb[3], xa_0); SB();
    lds_rd128<4096>(fb[4], xa_0); SB();
    lds_rd128<5120>(fb[5], xa_0); SB();
    lds_rd32(fl, lf_rd); SB();
    mm0w<6>(ac[0][0][0], fb[0], wr[0][0]); SB();
    lds_rd128<6144>(fb[6], xa_0); SB();
    mm0(ac[0][0][1], fb[0], wr[0][1]); SB();
    mm0w<6>(ac[0][1][1], fb[1], wr[0][1]); SB();
    lds_rd128<7168>(fb[7], xa_0); SB();
    mm0(ac[0][1][0], fb[1], wr[0][0]); SB();
    mm0w<2>(ac[1][0][0], fb[2], wr[0][0]); SB();
    chk<14>(fl, lf_rd, bud); SB();
    lds_rd128<8192>(fb[8], xa_0); SB();
    mm0(ac[1][0][1], fb[2], wr[0][1]); SB();
    mm0(ac[1][1][1], fb[3], wr[0][1]); SB();
    lds_rd128<9216>(fb[0], xa_0); SB();
    mm0(ac[1][1][0], fb[3], wr[0][0]); SB();
    mm(ac[0][0][0], fb[4], wr[0][2]); SB();
    lds_rd128<10240>(fb[1], xa_0); SB();
    SIGQ(3); SB();
    mm(ac[0][0][1], fb[4], wr[0][3]); SB();
    mm(ac[0][1][1], fb[5], wr[0][3]); SB();
    lds_rd128<11264>(fb[2], xa_0); SB();
    SIGN(); SB();
    mm(ac[0][1][0], fb[5], wr[0][2]); SB();
    mm(ac[1][0][0], fb[6], wr[0][2]); SB();
    lds_rd128<12288>(fb[3], xa_0); SB();
    mm(ac[1][0][1], fb[6], wr[0][3]); SB();
    mm(ac[1][1][1], fb[7], wr[0][3]); SB();
    lds_rd128<13312>(fb[4], xa_0); SB();
    lds_rd32(fl, lf_rd); SB();
    mm(ac[1][1][0], fb[7], wr[0][2]); SB();
    mm0w<5>(ac[2][0][0], fb[8], wr[0][0]); SB();
    lds_rd128<14336>(fb[5], xa_0); SB();
    mm0(ac[2][0][1], fb[8], wr[0][1]); SB();
    mm0(ac[2][1][1], fb[0], wr[0][1]); SB();
    lds_rd128<15360>(fb[6], xa_0); SB();
    mm0(ac[2][1][0], fb[0], wr[0][0]); SB();
    mm0w<2>(ac[3][0][0], fb[1], wr[0][0]); SB();
    chk<15>(fl, lf_rd, bud); SB();
    lds_rd128<32768>(fb[7], xa_1); SB();
    mm0(ac[3][0][1], fb[1], wr[0][1]); SB();
    mm0(ac[3][1][1], fb[2], wr[0][1]); SB();
    lds_rd128<33792>(fb[8], xa_1); SB();
    mm0(ac[3][1][0], fb[2], wr[0][0]); SB();
    mm(ac[2][0][0], fb[3], wr[0][2]); SB();
    lds_rd128<34816>(fb[0], xa_1); SB();
    SIGQ(3); SB();
    mm(ac[2][0][1], fb[3], wr[0][3]); SB();
    mm(ac[2][1][1], fb[4], wr[0][3]); SB();
    lds_rd128<35840>(fb[1], xa_1); SB();
    SIGN(); SB();
    mm(ac[2][1][0], fb[4], wr[0][2]); SB();
    mm(ac[3][0][0], fb[5], wr[0][2]); SB();
    lds_rd128<36864>(fb[2], xa_1); SB();
    mm(ac[3][0][1], fb[5], wr[0][3]); SB();
    mm(ac[3][1][1], fb[6], wr[0][3]); SB();
    lds_rd128<37888>(fb[3], xa_1); SB();
    lds_rd32(fl, lf_rd); SB();
    mm(ac[3][1][0], fb[6], wr[0][2]); SB();
    mmw<5>(ac[0][0][0], fb[7], wr[1][0]); SB();
    lds_rd128<38912>(fb[4], xa_1); SB();
    mm(ac[0][0][1], fb[7], wr[1][1]); SB();
    mm(ac[0][1][1], fb[8], wr[1][1]); SB();
    lds_rd128<39936>(fb[5], xa_1); SB();
    mm(ac[0][1][0], fb[8], wr[1][0]); SB();
    mmchkw<16, 2>(ac[1][0][0], fb[0], wr[1][0], fl, lf_rd, bud); SB();
    lds_rd128<40960>(fb[6], xa_1); SB();
    mm(ac[1][0][1], fb[0], wr[1][1]); SB();
    lds_rd128<0>(wr[8][0], wa0_2); SB();
    mm(ac[1][1][1], fb[1], wr[1][1]); SB();
    lds_rd128<41984>(fb[7], xa_1); SB();
    lds_rd128<2048>(wr[8][1], wa0_2); SB();
    mm(ac[1][1][0], fb[1], wr[1][0]); SB();
    lds_rd128<0>(wr[8][2], wa1_2); SB();
    mm(ac[0][0][0], fb[2], wr[1][2]); SB();
    lds_rd128<43008>(fb[8], xa_1); SB();
    lds_rd128<2048>(wr[8][3], wa1_2); SB();
    mm(ac[0][0][1], fb[2], wr[1][3]); SB();
    mm(ac[0][1][1], fb[3], wr[1][3]); SB();
    lds_rd128<44032>(fb[0], xa_1); SB();
    mm(ac[0][1][0], fb[3], wr[1][2]); SB();
    mmw<8>(ac[1][0][0], fb[4], wr[1][2]); SB();
    lds_rd128<45056>(fb[1], xa_1); SB();
    mm(ac[1][0][1], fb[4], wr[1][3]); SB();
    mm(ac[1][1][1], fb[5], wr[1][3]); SB();
    lds_rd128<46080>(fb[2], xa_1); SB();
    lds_rd32(fl, lf_rd); SB();
    mm(ac[1][1][0], fb[5], wr[1][2]); SB();
    mmw<9>(ac[2][0][0], fb[6], wr[1][0]); SB();
    lds_rd128<47104>(fb[3], xa_1); SB();
    mm(ac[2][0][1], fb[6], wr[1][1]); SB();
    mmw<8>(ac[2][1][1], fb[7], wr[1][1]); SB();
    lds_rd128<48128>(fb[4], xa_1); SB();
    mm(ac[2][1][0], fb[7], wr[1][0]); SB();
    mmchkw<17, 2>(ac[3][0][0], fb[8], wr[1][0], fl, lf_rd, bud); SB();
    lds_rd128<16384>(fb[5], xa_0); SB();
    mm(ac[3][0][1], fb[8], wr[1][1]); SB();
    mm(ac[3][1][1], fb[0], wr[1][1]); SB();
    lds_rd128<17408>(fb[6], xa_0); SB();
    mm(ac[3][1][0], fb[0], wr[1][0]); SB();
    mm(ac[2][0][0], fb[1], wr[1][2]); SB();
    lds_rd128<18432>(fb[7], xa_0); SB();
    SIGQ(3); SB();
    mm(ac[2][0][1], fb[1], wr[1][3]); SB();
    mm(ac[2][1][1], fb[2], wr[1][3]); SB();
    lds_rd128<19456>(fb[8], xa_0); SB();
    SIGN(); SB();
    mm(ac[2][1][0], fb[2], wr[1][2]); SB();
    mm(ac[3][0][0], fb[3], wr[1][2]); SB();
    lds_rd128<20480>(fb[0], xa_0); SB();
    mm(ac[3][0][1], fb[3], wr[1][3]); SB();
    mm(ac[3][1][1], fb[4], wr[1][3]); SB();
    lds_rd128<21504>(fb[1], xa_0); SB();
    mm(ac[3][1][0], fb[4], wr[1][2]); SB();
    mmw<4>(ac[0][0][0], fb[5], wr[2][0]); SB();
    lds_rd128<22528>(fb[2], xa_0); SB();
    mm(ac[0][0][1], fb[5], wr[2][1]); SB();
    mm(ac[0][1][1], fb[6], wr[2][1]); SB();
    lds_rd128<23552>(fb[3], xa_0); SB();
    mm(ac[0][1][0], fb[6], wr[2][0]); SB();
    mmw<4>(ac[1][0][0], fb[7], wr[2][0]); SB();
    lds_rd128<24576>(fb[4], xa_0); SB();
    mm(ac[1][0][1], fb[7], wr[2][1]); SB();
    lds_rd128<49152>(wr[9][0], wa0_0); SB();
    mm(ac[1][1][1], fb[8], wr[2][1]); SB();
    lds_rd128<25600>(fb[5], xa_0); SB();
    lds_rd128<51200>(wr[9][1], wa0_0); SB();
    mm(ac[1][1][0], fb[8], wr[2][0]); SB();
    lds_rd128<49152>(wr[9][2], wa1_0); SB();
    mmw<7>(ac[0][0][0], fb[0], wr[2][2]); SB();
    lds_rd128<26624>(fb[6], xa_0); SB();
    lds_rd128<51200>(wr[9][3], wa1_0); SB();
    mm(ac[0][0][1], fb[0], wr[2][3]); SB();
    mm(ac[0][1][1], fb[1], wr[2][3]); SB();
    lds_rd128<27648>(fb[7], xa_0); SB();
    mm(ac[0][1][0], fb[1], wr[2][2]); SB();
    mmw<8>(ac[1][0][0], fb[2], wr[2][2]); SB();
    lds_rd128<28672>(fb[8], xa_0); SB();
    mm(ac[1][0][1], fb[2], wr[2][3]); SB();
    mm(ac[1][1][1], fb[3], wr[2][3]); SB();
    lds_rd128<29696>(fb[0], xa_0); SB();
    lds_rd32(fl, lf_rd); SB();
    mm(ac[1][1][0], fb[3], wr[2][2]); SB();
    mmw<9>(ac[2][0][0], fb[4], wr[2][0]); SB();
    lds_rd128<30720>(fb[1], xa_0); SB();
    mm(ac[2][0][1], fb[4], wr[2][1]); SB();
    mmw<8>(ac[2][1][1], fb[5], wr[2][1]); SB();
    lds_rd128<31744>(fb[2], xa_0); SB();
    mm(ac[2][1][0], fb[5], wr[2][0]); SB();
    mmchkw<18, 2>(ac[3][0][0], fb[6], wr[2][0], fl, lf_rd, bud); SB();
    lds_rd128<49152>(fb[3], xa_1); SB();
    mm(ac[3][0][1], fb[6], wr[2][1]); SB();
    mm(ac[3][1][1], fb[7], wr[2][1]); SB();
    lds_rd128<50176>(fb[4], xa_1); SB();
    mm(ac[3][1][0], fb[7], wr[2][0]); SB();
    mm(ac[2][0][0], fb[8], wr[2][2]); SB();
    lds_rd128<51200>(fb[5], xa_1); SB();
    SIGQ(3); SB();
    mm(ac[2][0][1], fb[8], wr[2][3]); SB();
    mm(ac[2][1][1], fb[0], wr[2][3]); SB();
    lds_rd128<52224>(fb[6], xa_1); SB();
    SIGN(); SB();
    mm(ac[2][1][0], fb[0], wr[2][2]); SB();
    mm(ac[3][0][0], fb[1], wr[2][2]); SB();
    lds_rd128<53248>(fb[7], xa_1); SB();
    mm(ac[3][0][1], fb[1], wr[2][3]); SB();
    mm(ac[3][1][1], fb[2], wr[2][3]); SB();
    lds_rd128<54272>(fb[8], xa_1); SB();
    mm(ac[3][1][0], fb[2], wr[2][2]); SB();
    mmw<4>(ac[0][0][0], fb[3], wr[3][0]); SB();
    lds_rd128<55296>(fb[0], xa_1); SB();
    mm(ac[0][0][1], fb[3], wr[3][1]); SB();
    mm(ac[0][1][1], fb[4], wr[3][1]); SB();
    lds_rd128<56320>(fb[1], xa_1); SB();
    mm(ac[0][1][0], fb[4], wr[3][0]); SB();
    mmw<4>(ac[1][0][0], fb[5], wr[3][0]); SB();
    lds_rd128<57344>(fb[2], xa_1); SB();
    mm(ac[1][0][1], fb[5], wr[3][1]); SB();
    lds_rd128<0>(wr[10][0], wa0_1); SB();
    mm(ac[1][1][1], fb[6], wr[3][1]); SB();
    lds_rd128<58368>(fb[3], xa_1); SB();
    lds_rd128<2048>(wr[10][1], wa0_1); SB();
    mm(ac[1][1][0], fb[6], wr[3][0]); SB();
    lds_rd128<0>(wr[10][2], wa1_1); SB();
    mmw<7>(ac[0][0][0], fb[7], wr[3][2]); SB();
    lds_rd128<59392>(fb[4], xa_1); SB();
    lds_rd128<2048>(wr[10][3], wa1_1); SB();
    mm(ac[0][0][1], fb[7], wr[3][3]); SB();
    mm(ac[0][1][1], fb[8], wr[3][3]); SB();
    lds_rd128<60416>(fb[5], xa_1); SB();
    mm(ac[0][1][0], fb[8], wr[3][2]); SB();
    mmw<8>(ac[1][0][0], fb[0], wr[3][2]); SB();
    lds_rd128<61440>(fb[6], xa_1); SB();
    mm(ac[1][0][1], fb[0], wr[3][3]); SB();
    mm(ac[1][1][1], fb[1], wr[3][3]); SB();
    lds_rd128<62464>(fb[7], xa_1); SB();
    lds_rd32(fl, lf_rd); SB();
    mm(ac[1][1][0], fb[1], wr[3][2]); SB();
    mmw<9>(ac[2][0][0], fb[2], wr[3][0]); SB();
    lds_rd128<63488>(fb[8], xa_1); SB();
    mm(ac[2][0][1], fb[2], wr[3][1]); SB();
    mmw<8>(ac[2][1][1], fb[3], wr[3][1]); SB();
    lds_rd128<64512>(fb[0], xa_1); SB();
    mm(ac[2][1][0], fb[3], wr[3][0]); SB();
    mmchkw<19, 2>(ac[3][0][0], fb[4], wr[3][0], fl, lf_rd, bud); SB();
    lds_rd128<32768>(fb[1], xa_0); SB();
    mm(ac[3][0][1], fb[4], wr[3][1]); SB();
    mm(ac[3][1][1], fb[5], wr[3][1]); SB();
    lds_rd128<33792>(fb[2], xa_0); SB();
    mm(ac[3][1][0], fb[5], wr[3][0]); SB();
    mm(ac[2][0][0], fb[6], wr[3][2]); SB();
    lds_rd128<34816>(fb[3], xa_0); SB();
    SIGQ(3); SB();
    mm(ac[2][0][1], fb[6], wr[3][3]); SB();
    mm(ac[2][1][1], fb[7], wr[3][3]); SB();
    lds_rd128<35840>(fb[4], xa_0); SB();
    SIGN(); SB();
    mm(ac[2][1][0], fb[7], wr[3][2]); SB();
    mm(ac[3][0][0], fb[8], wr[3][2]); SB();
    lds_rd128<36864>(fb[5], xa_0); SB();
    mm(ac[3][0][1], fb[8], wr[3][3]); SB();
    mm(ac[3][1][1], fb[0], wr[3][3]); SB();
    lds_rd128<37888>(fb[6], xa_0); SB();
    mm(ac[3][1][0], fb[0], wr[3][2]); SB();
    mmw<4>(ac[0][0][0], fb[1], wr[4][0]); SB();
    lds_rd128<38912>(fb[7], xa_0); SB();
    mm(ac[0][0][1], fb[1], wr[4][1]); SB();
    mm(ac[0][1][1], fb[2], wr[4][1]); SB();
    lds_rd128<39936>(fb[8], xa_0); SB();
    mm(ac[0][1][0], fb[2], wr[4][0]); SB();
    mmw<4>(ac[1][0][0], fb[3], wr[4][0]); SB();
    lds_rd128<40960>(fb[0], xa_0); SB();
    mm(ac[1][0][1], fb[3], wr[4][1]); SB();
    lds_rd128<16384>(wr[11][0], wa0_1); SB();
    mm(ac[1][1][1], fb[4], wr[4][1]); SB();
    lds_rd128<41984>(fb[1], xa_0); SB();
    lds_rd128<18432>(wr[11][1], wa0_1); SB();
    mm(ac[1][1][0], fb[4], wr[4][0]); SB();
    lds_rd128<16384>(wr[11][2], wa1_1); SB();
    mmw<7>(ac[0][0][0], fb[5], wr[4][2]); SB();
    lds_rd128<43008>(fb[2], xa_0); SB();
    lds_rd128<18432>(wr[11][3], wa1_1); SB();
    mm(ac[0][0][1], fb[5], wr[4][3]); SB();
    mm(ac[0][1][1], fb[6], wr[4][3]); SB();
    lds_rd128<44032>(fb[3], xa_0); SB();
    mm(ac[0][1][0], fb[6], wr[4][2]); SB();
    mmw<8>(ac[1][0][0], fb[7], wr[4][2]); SB();
    lds_rd128<45056>(fb[4], xa_0); SB();
    mm(ac[1][0][1], fb[7], wr[4][3]); SB();
    mm(ac[1][1][1], fb[8], wr[4][3]); SB();
    lds_rd128<46080>(fb[5], xa_0); SB();
    lds_rd32(fl, lf_rd); SB();
    mm(ac[1][1][0], fb[8], wr[4][2]); SB();
    mmw<9>(ac[2][0][0], fb[0], wr[4][0]); SB();
    lds_rd128<47104>(fb[6], xa_0); SB();
    mm(ac[2][0][1], fb[0], wr[4][1]); SB();
    mmw<8>(ac[2][1][1], fb[1], wr[4][1]); SB();
    lds_rd128<48128>(fb[7], xa_0); SB();
    mm(ac[2][1][0], fb[1], wr[4][0]); SB();
    mmchkw<20, 2>(ac[3][0][0], fb[2], wr[4][0], fl, lf_rd, bud); SB();
    lds_rd128<0>(fb[8], xa_0); SB();
    mm(ac[3][0][1], fb[2], wr[4][1]); SB();
    mm(ac[3][1][1], fb[3], wr[4][1]); SB();
    lds_rd128<1024>(fb[0], xa_0); SB();
    mm(ac[3][1][0], fb[3], wr[4][0]); SB();
    mm(ac[2][0][0], fb[4], wr[4][2]); SB();
    lds_rd128<2048>(fb[1], xa_0); SB();
    SIGQ(3); SB();
    mm(ac[2][0][1], fb[4], wr[4][3]); SB();
    mm(ac[2][1][1], fb[5], wr[4][3]); SB();
    lds_rd128<3072>(fb[2], xa_0); SB();
    SIGN(); SB();
    mm(ac[2][1][0], fb[5], wr[4][2]); SB();
    mm(ac[3][0][0], fb[6], wr[4][2]); SB();
    lds_rd128<4096>(fb[3], xa_0); SB();
    mm(ac[3][0][1], fb[6], wr[4][3]); SB();
    mm(ac[3][1][1], fb[7], wr[4][3]); SB();
    lds_rd128<5120>(fb[4], xa_0); SB();
    mm(ac[3][1][0], fb[7], wr[4][2]); SB();
    mmw<4>(ac[0][0][0], fb[8], wr[5][0]); SB();
    lds_rd128<6144>(fb[5], xa_0); SB();
    mm(ac[0][0][1], fb[8], wr[5][1]); SB();
    mm(ac[0][1][1], fb[0], wr[5][1]); SB();
    lds_rd128<7168>(fb[6], xa_0); SB();
    mm(ac[0][1][0], fb[0], wr[5][0]); SB();
    mmw<4>(ac[1][0][0], fb[1], wr[5][0]); SB();
    lds_rd128<8192>(fb[7], xa_0); SB();
    mm(ac[1][0][1], fb[1], wr[5][1]); SB();
    mm(ac[1][1][1], fb[2], wr[5][1]); SB();
    lds_rd128<9216>(fb[8], xa_0); SB();
    mm(ac[1][1][0], fb[2], wr[5][0]); SB();
    mmw<4>(ac[0][0][0], fb[3], wr[5][2]); SB();
    lds_rd128<10240>(fb[0], xa_0); SB();
    mm(ac[0][0][1], fb[3], wr[5][3]); SB();
    mm(ac[0][1][1], fb[4], wr[5][3]); SB();
    lds_rd128<11264>(fb[1], xa_0); SB();
    mm(ac[0][1][0], fb[4], wr[5][2]); SB();
    mmw<4>(ac[1][0][0], fb[5], wr[5][2]); SB();
    lds_rd128<12288>(fb[2], xa_0); SB();
    mm(ac[1][0][1], fb[5], wr[5][3]); SB();
    mm(ac[1][1][1], fb[6], wr[5][3]); SB();
    lds_rd128<13312>(fb[3], xa_0); SB();
    lds_rd32(fl, lf_rd); SB();
    mm(ac[1][1][0], fb[6], wr[5][2]); SB();
    mmw<5>(ac[2][0][0], fb[7], wr[5][0]); SB();
    lds_rd128<14336>(fb[4], xa_0); SB();
    mm(ac[2][0][1], fb[7], wr[5][1]); SB();
    mm(ac[2][1][1], fb[8], wr[5][1]); SB();
    lds_rd128<15360>(fb[5], xa_0); SB();
    mm(ac[2][1][0], fb[8], wr[5][0]); SB();
    mmchkw<21, 2>(ac[3][0][0], fb[0], wr[5][0], fl, lf_rd, bud); SB();
    lds_rd128<0>(fb[6], xa_2); SB();
    mm(ac[3][0][1], fb[0], wr[5][1]); SB();
    mm(ac[3][1][1], fb[1], wr[5][1]); SB();
    lds_rd128<1024>(fb[7], xa_2); SB();
    mm(ac[3][1][0], fb[1], wr[5][0]); SB();
    mm(ac[2][0][0], fb[2], wr[5][2]); SB();
    lds_rd128<2048>(fb[8], xa_2); SB();
    SIGQ(3); SB();
    mm(ac[2][0][1], fb[2], wr[5][3]); SB();
    mm(ac[2][1][1], fb[3], wr[5][3]); SB();
    lds_rd128<3072>(fb[0], xa_2); SB();
    SIGN(); SB();
    mm(ac[2][1][0], fb[3], wr[5][2]); SB();
    mm(ac[3][0][0], fb[4], wr[5][2]); SB();
    lds_rd128<4096>(fb[1], xa_2); SB();
    mm(ac[3][0][1], fb[4], wr[5][3]); SB();
    mm(ac[3][1][1], fb[5], wr[5][3]); SB();
    lds_rd128<5120>(fb[2], xa_2); SB();
    mm(ac[3][1][0], fb[5], wr[5][2]); SB();
    mmw<4>(ac[0][0][0], fb[6], wr[6][0]); SB();
    lds_rd128<6144>(fb[3], xa_2); SB();
    mm(ac[0][0][1], fb[6], wr[6][1]); SB();
    mm(ac[0][1][1], fb[7], wr[6][1]); SB();
    lds_rd128<7168>(fb[4], xa_2); SB();
    mm(ac[0][1][0], fb[7], wr[6][0]); SB();
    mmw<4>(ac[1][0][0], fb[8], wr[6][0]); SB();
    lds_rd128<8192>(fb[5], xa_2); SB();
    mm(ac[1][0][1], fb[8], wr[6][1]); SB();
    mm(ac[1][1][1], fb[0], wr[6][1]); SB();
    lds_rd128<9216>(fb[6], xa_2); SB();
    mm(ac[1][1][0], fb[0], wr[6][0]); SB();
    mmw<4>(ac[0][0][0], fb[1], wr[6][2]); SB();
    lds_rd128<10240>(fb[7], xa_2); SB();
    mm(ac[0][0][1], fb[1], wr[6][3]); SB();
    mm(ac[0][1][1], fb[2], wr[6][3]); SB();
    lds_rd128<11264>(fb[8], xa_2); SB();
    mm(ac[0][1][0], fb[2], wr[6][2]); SB();
    mmw<4>(ac[1][0][0], fb[3], wr[6][2]); SB();
    lds_rd128<12288>(fb[0], xa_2); SB();
    mm(ac[1][0][1], fb[3], wr[6][3]); SB();
    mm(ac[1][1][1], fb[4], wr[6][3]); SB();
    lds_rd128<13312>(fb[1], xa_2); SB();
    lds_rd32(fl, lf_rd); SB();
    mm(ac[1][1][0], fb[4], wr[6][2]); SB();
    mmw<5>(ac[0][0][0], fb[5], wr[7][0]); SB();
    lds_rd128<14336>(fb[2], xa_2); SB();
    mm(ac[0][0][1], fb[5], wr[7][1]); SB();
    mm(ac[0][1][1], fb[6], wr[7][1]); SB();
    lds_rd128<15360>(fb[3], xa_2); SB();
    mm(ac[0][1][0], fb[6], wr[7][0]); SB();
    mmchkw<22, 2>(ac[1][0][0], fb[7], wr[7][0], fl, lf_rd, bud); SB();
    lds_rd128<49152>(fb[4], xa_0); SB();
    mm(ac[1][0][1], fb[7], wr[7][1]); SB();
    mm(ac[1][1][1], fb[8], wr[7][1]); SB();
    lds_rd128<50176>(fb[5], xa_0); SB();
    mm(ac[1][1][0], fb[8], wr[7][0]); SB();
    mm(ac[0][0][0], fb[0], wr[7][2]); SB();
    lds_rd128<51200>(fb[6], xa_0); SB();
    SIGQ(3); SB();
    mm(ac[0][0][1], fb[0], wr[7][3]); SB();
    mm(ac[0][1][1], fb[1], wr[7][3]); SB();
    lds_rd128<52224>(fb[7], xa_0); SB();
    SIGN(); SB();
    mm(ac[0][1][0], fb[1], wr[7][2]); SB();
    mm(ac[1][0][0], fb[2], wr[7][2]); SB();
    lds_rd128<53248>(fb[8], xa_0); SB();
    mm(ac[1][0][1], fb[2], wr[7][3]); SB();
    mm(ac[1][1][1], fb[3], wr[7][3]); SB();
    lds_rd128<54272>(fb[0], xa_0); SB();
    mm(ac[1][1][0], fb[3], wr[7][2]); SB();
    mmw<4>(ac[2][0][0], fb[4], wr[6][0]); SB();
    lds_rd128<55296>(fb[1], xa_0); SB();
    mm(ac[2][0][1], fb[4], wr[6][1]); SB();
    mm(ac[2][1][1], fb[5], wr[6][1]); SB();
    lds_rd128<56320>(fb[2], xa_0); SB();
    mm(ac[2][1][0], fb[5], wr[6][0]); SB();
    mmw<4>(ac[2][0][0], fb[6], wr[6][2]); SB();
    lds_rd128<57344>(fb[3], xa_0); SB();
    mm(ac[2][0][1], fb[6], wr[6][3]); SB();
    lds_rd128<32768>(wr[12][0], wa0_1); SB();
    mm(ac[2][1][1], fb[7], wr[6][3]); SB();
    lds_rd128<58368>(fb[4], xa_0); SB();
    lds_rd128<34816>(wr[12][1], wa0_1); SB();
    mm(ac[2][1][0], fb[7], wr[6][2]); SB();
    lds_rd128<32768>(wr[12][2], wa1_1); SB();
    mmw<7>(ac[2][0][0], fb[8], wr[7][0]); SB();
    lds_rd128<59392>(fb[5], xa_0); SB();
    lds_rd128<34816>(wr[12][3], wa1_1); SB();
    mm(ac[2][0][1], fb[8], wr[7][1]); SB();
    mm(ac[2][1][1], fb[0], wr[7][1]); SB();
    lds_rd128<60416>(fb[6], xa_0); SB();
    mm(ac[2][1][0], fb[0], wr[7][0]); SB();
    mmw<8>(ac[2][0][0], fb[1], wr[7][2]); SB();
    lds_rd128<61440>(fb[7], xa_0); SB();
    mm(ac[2][0][1], fb[1], wr[7][3]); SB();
    mm(ac[2][1][1], fb[2], wr[7][3]); SB();
    lds_rd128<62464>(fb[8], xa_0); SB();
    lds_rd32(fl, lf_rd); SB();
    mm(ac[2][1][0], fb[2], wr[7][2]); SB();
    mmw<9>(ac[0][0][0], fb[3], wr[8][0]); SB();
    lds_rd128<63488>(fb[0], xa_0); SB();
    mm(ac[0][0][1], fb[3], wr[8][1]); SB();
    mmw<8>(ac[0][1][1], fb[4], wr[8][1]); SB();
    lds_rd128<64512>(fb[1], xa_0); SB();
    mm(ac[0][1][0], fb[4], wr[8][0]); SB();
    mmchkw<23, 2>(ac[0][0][0], fb[5], wr[8][2], fl, lf_rd, bud); SB();
    lds_rd128<49152>(fb[2], xa_1); SB();
    mm(ac[0][0][1], fb[5], wr[8][3]); SB();
    lds_rd128<16384>(wr[13][0], wa0_0); SB();
    mm(ac[0][1][1], fb[6], wr[8][3]); SB();
    lds_rd128<50176>(fb[3], xa_1); SB();
    lds_rd128<18432>(wr[13][1], wa0_0); SB();
    mm(ac[0][1][0], fb[6], wr[8][2]); SB();
    lds_rd128<16384>(wr[13][2], wa1_0); SB();
    mm(ac[0][0][0], fb[7], wr[9][0]); SB();
    lds_rd128<51200>(fb[4], xa_1); SB();
    lds_rd128<18432>(wr[13][3], wa1_0); SB();
    mm(ac[0][0][1], fb[7], wr[9][1]); SB();
    SIGQ(7); SB();
    mm(ac[0][1][1], fb[8], wr[9][1]); SB();
    lds_rd128<52224>(fb[5], xa_1); SB();
    mm(ac[0][1][0], fb[8], wr[9][0]); SB();
    SIGN(); SB();
    mm(ac[0][0][0], fb[0], wr[9][2]); SB();
    lds_rd128<53248>(fb[6], xa_1); SB();
    mm(ac[0][0][1], fb[0], wr[9][3]); SB();
    mm(ac[0][1][1], fb[1], wr[9][3]); SB();
    lds_rd128<54272>(fb[7], xa_1); SB();
    mm(ac[0][1][0], fb[1], wr[9][2]); SB();
    mmw<8>(ac[0][0][0], fb[2], wr[10][0]); SB();
    lds_rd128<55296>(fb[8], xa_1); SB();
    mm(ac[0][0][1], fb[2], wr[10][1]); SB();
    mmw<7>(ac[0][1][1], fb[3], wr[10][1]); SB();
    lds_rd128<56320>(fb[0], xa_1); SB();
    mm(ac[0][1][0], fb[3], wr[10][0]); SB();
    mmw<5>(ac[0][0][0], fb[4], wr[10][2]); SB();
    lds_rd128<57344>(fb[1], xa_1); SB();
    mm(ac[0][0][1], fb[4], wr[10][3]); SB();
    mmw<4>(ac[0][1][1], fb[5], wr[10][3]); SB();
    lds_rd128<58368>(fb[2], xa_1); SB();
    mm(ac[0][1][0], fb[5], wr[10][2]); SB();
    mm(ac[0][0][0], fb[6], wr[11][0]); SB();
    lds_rd128<59392>(fb[3], xa_1); SB();
    mm(ac[0][0][1], fb[6], wr[11][1]); SB();
    mmw<4>(ac[0][1][1], fb[7], wr[11][1]); SB();
    lds_rd128<60416>(fb[4], xa_1); SB();
    mm(ac[0][1][0], fb[7], wr[11][0]); SB();
    mm(ac[0][0][0], fb[8], wr[11][2]); SB();
    lds_rd128<61440>(fb[5], xa_1); SB();
    mm(ac[0][0][1], fb[8], wr[11][3]); SB();
    mmw<4>(ac[0][1][1], fb[0], wr[11][3]); SB();
    lds_rd128<62464>(fb[6], xa_1); SB();
    mm(ac[0][1][0], fb[0], wr[11][2]); SB();
    lds_rd32(fl, lf_rd); SB();
    mm(ac[0][0][0], fb[1], wr[12][0]); SB();
    lds_rd128<63488>(fb[7], xa_1); SB();
    mm(ac[0][0][1], fb[1], wr[12][1]); SB();
    mmw<5>(ac[0][1][1], fb[2], wr[12][1]); SB();
    lds_rd128<64512>(fb[8], xa_1); SB();
    mm(ac[0][1][0], fb[2], wr[12][0]); SB();
    mm(ac[0][0][0], fb[3], wr[12][2]); SB();
    lds_rd128<0>(fb[0], wa0_1); SB();
    mmchkw<24, 3>(ac[0][0][1], fb[3], wr[12][3], fl, lf_rd, bud); SB();
    lds_rd128<16384>(fb[1], xa_1); SB();
    mm(ac[0][1][1], fb[4], wr[12][3]); SB();
    lds_rd128<2048>(fb[2], wa0_1); SB();
    mm(ac[0][1][0], fb[4], wr[12][2]); SB();
    lds_rd128<17408>(fb[3], xa_1); SB();
    mm(ac[0][0][0], fb[5], wr[13][0]); SB();
    lds_rd128<0>(fb[4], wa1_1); SB();
    mm(ac[0][0][1], fb[5], wr[13][1]); SB();
    lds_rd128<18432>(fb[5], xa_1); SB();
    mm(ac[0][1][1], fb[6], wr[13][1]); SB();
    SIGQ(6); SB();
    mm(ac[0][1][0], fb[6], wr[13][0]); SB();
    lds_rd128<2048>(fb[6], wa1_1); SB();
    mm(ac[0][0][0], fb[7], wr[13][2]); SB();
    SIGN(); SB();
    mm(ac[0][0][1], fb[7], wr[13][3]); SB();
    lds_rd128<19456>(fb[7], xa_1); SB();
    mm(ac[0][1][1], fb[8], wr[13][3]); SB();
    mm(ac[0][1][0], fb[8], wr[13][2]); SB();
    lds_rd128<32768>(fb[8], wa0_0); SB();
    mmw<6>(ac[0][0][0], fb[1], fb[0]); SB();
    mm(ac[0][0][1], fb[1], fb[2]); SB();
    lds_rd128<20480>(fb[1], xa_1); SB();
    mmw<5>(ac[0][1][1], fb[3], fb[2]); SB();
    lds_rd128<34816>(fb[2], wa0_0); SB();
    mm(ac[0][1][0], fb[3], fb[0]); SB();
    lds_rd128<21504>(fb[0], xa_1); SB();
    mmw<5>(ac[0][0][0], fb[5], fb[4]); SB();
    lds_rd128<32768>(fb[3], wa1_0); SB();
    mm(ac[0][0][1], fb[5], fb[6]); SB();
    lds_rd128<22528>(fb[5], xa_1); SB();
    mmw<6>(ac[0][1][1], fb[7], fb[6]); SB();
    lds_rd128<34816>(fb[6], wa1_0); SB();
    mm(ac[0][1][0], fb[7], fb[4]); SB();
    lds_rd128<23552>(fb[4], xa_1); SB();
    mmw<6>(ac[0][0][0], fb[1], fb[8]); SB();
    lds_rd128<24576>(fb[7], xa_1); SB();
    mmw<6>(ac[0][0][1], fb[1], fb[2]); SB();
    lds_rd128<25600>(fb[1], xa_1); SB();
    mmw<6>(ac[0][1][1], fb[0], fb[2]); SB();
    lds_rd128<26624>(fb[2], xa_1); SB();
    mm(ac[0][1][0], fb[0], fb[8]); SB();
    lds_rd128<27648>(fb[0], xa_1); SB();
    mmw<6>(ac[0][0][0], fb[5], fb[3]); SB();
    lds_rd128<28672>(fb[8], xa_1); SB();
    mmw<6>(ac[0][0][1], fb[5], fb[6]); SB();
    mmw<5>(ac[0][1][1], fb[4], fb[6]); SB();
    lds_rd128<29696>(fb[5], xa_1); SB();
    lds_rd32(fl, lf_rd); SB();
    mm(ac[0][1][0], fb[4], fb[3]); SB();
    mmw<6>(ac[1][0][0], fb[7], wr[8][0]); SB();
    lds_rd128<30720>(fb[6], xa_1); SB();
    mm(ac[1][0][1], fb[7], wr[8][1]); SB();
    mmw<6>(ac[1][1][1], fb[1], wr[8][1]); SB();
    lds_rd128<31744>(fb[3], xa_1); SB();
    mm(ac[1][1][0], fb[1], wr[8][0]); SB();
    EM0(0, 0); SB();
    mmchkw<25, 2>(ac[1][0][0], fb[2], wr[8][2], fl, lf_rd, bud); SB();
    lds_rd128<0>(fb[4], xa_0); SB();
    EM0(0, 1); SB();
    mm(ac[1][0][1], fb[2], wr[8][3]); SB();
    EM1(0, 0, 3, 4); SB();
    EM1(0, 1, 11, 12); SB();
    EM1(0, 0, 5, 6); SB();
    mm(ac[1][1][1], fb[0], wr[8][3]); SB();
    lds_rd128<1024>(fb[7], xa_0); SB();
    EM1(0, 1, 13, 14); SB();
    EM1(0, 0, 7, 15); SB();
    mm(ac[1][1][0], fb[0], wr[8][2]); SB();
    EK(0); SB();
    EM2(); SB();
    EK(1); SB();
    mm(ac[1][0][0], fb[8], wr[9][0]); SB();
    lds_rd128<2048>(fb[1], xa_0); SB();
    ES0(); SB();
    ES1(); SB();
    mm(ac[1][0][1], fb[8], wr[9][1]); SB();
    mm(ac[1][1][1], fb[5], wr[9][1]); SB();
    lds_rd128<3072>(fb[2], xa_0); SB();
    EX(0); SB();
    EH(0, 0); SB();
    mm(ac[1][1][0], fb[5], wr[9][0]); SB();
    EH(0, 1); SB();
    EH(0, 2); SB();
    mmw<4>(ac[1][0][0], fb[6], wr[9][2]); SB();
    lds_rd128<4096>(fb[0], xa_0); SB();
    EH(0, 3); SB();
    ET(0); SB();
    mm(ac[1][0][1], fb[6], wr[9][3]); SB();
    ET(1); SB();
    ET(2); SB();
    EU(3); SB();
    mm(ac[1][1][1], fb[3], wr[9][3]); SB();
    lds_rd128<5120>(fb[8], xa_0); SB();
    mm(ac[1][1][0], fb[3], wr[9][2]); SB();
    SEND(0, 0); SB();
    mmw<4>(ac[1][0][0], fb[4], wr[10][0]); SB();
    lds_rd128<6144>(fb[5], xa_0); SB();
    EH(0, 4); SB();
    EH(0, 5); SB();
    mm(ac[1][0][1], fb[4], wr[10][1]); SB();
    EH(0, 6); SB();
    EH(0, 7); SB();
    ET(4); SB();
    mm(ac[1][1][1], fb[7], wr[10][1]); SB();
    lds_rd128<7168>(fb[6], xa_0); SB();
    ET(5); SB();
    ET(6); SB();
    mm(ac[1][1][0], fb[7], wr[10][0]); SB();
    EU(7); SB();
    mmw<4>(ac[1][0][0], fb[1], wr[10][2]); SB();
    lds_rd128<8192>(fb[3], xa_0); SB();
    SEND(0, 1); SB();
    mm(ac[1][0][1], fb[1], wr[10][3]); SB();
    mm(ac[1][1][1], fb[2], wr[10][3]); SB();
    lds_rd128<9216>(fb[4], xa_0); SB();
    mm(ac[1][1][0], fb[2], wr[10][2]); SB();
    mmw<4>(ac[1][0][0], fb[0], wr[11][0]); SB();
    lds_rd128<10240>(fb[7], xa_0); SB();
    mm(ac[1][0][1], fb[0], wr[11][1]); SB();
    mm(ac[1][1][1], fb[8], wr[11][1]); SB();
    lds_rd128<11264>(fb[1], xa_0); SB();
    mm(ac[1][1][0], fb[8], wr[11][0]); SB();
    mmw<4>(ac[1][0][0], fb[5], wr[11][2]); SB();
    lds_rd128<12288>(fb[2], xa_0); SB();
    mm(ac[1][0][1], fb[5], wr[11][3]); SB();
    mm(ac[1][1][1], fb[6], wr[11][3]); SB();
    lds_rd128<13312>(fb[0], xa_0); SB();
    mm(ac[1][1][0], fb[6], wr[11][2]); SB();
    lds_rd32(fl, lf_rd); SB();
    mmw<5>(ac[1][0][0], fb[3], wr[12][0]); SB();
    lds_rd128<14336>(fb[8], xa_0); SB();
    mm(ac[1][0][1], fb[3], wr[12][1]); SB();
    mm(ac[1][1][1], fb[4], wr[12][1]); SB();
    lds_rd128<15360>(fb[5], xa_0); SB();
    mm(ac[1][1][0], fb[4], wr[12][0]); SB();
    mmw<5>(ac[1][0][0], fb[7], wr[12][2]); SB();
    lds_rd128<0>(fb[6], wa0_1); SB();
    mmchkw<26, 3>(ac[1][0][1], fb[7], wr[12][3], fl, lf_rd, bud); SB();
    lds_rd128<0>(fb[3], xa_2); SB();
    mm(ac[1][1][1], fb[1], wr[12][3]); SB();
    lds_rd128<2048>(fb[4], wa0_1); SB();
    mm(ac[1][1][0], fb[1], wr[12][2]); SB();
    lds_rd128<1024>(fb[7], xa_2); SB();
    mm(ac[1][0][0], fb[2], wr[13][0]); SB();
    lds_rd128<0>(fb[1], wa1_1); SB();
    mm(ac[1][0][1], fb[2], wr[13][1]); SB();
    lds_rd128<2048>(fb[2], xa_2); SB();
    mm(ac[1][1][1], fb[0], wr[13][1]); SB();
    mm(ac[1][1][0], fb[0], wr[13][0]); SB();
    lds_rd128<2048>(fb[0], wa1_1); SB();
    mmw<7>(ac[1][0][0], fb[8], wr[13][2]); SB();
    mm(ac[1][0][1], fb[8], wr[13][3]); SB();
    lds_rd128<3072>(fb[8], xa_2); SB();
    mm(ac[1][1][1], fb[5], wr[13][3]); SB();
    mm(ac[1][1][0], fb[5], wr[13][2]); SB();
    lds_rd128<32768>(fb[5], wa0_0); SB();
    mmw<6>(ac[1][0][0], fb[3], fb[6]); SB();
    mm(ac[1][0][1], fb[3], fb[4]); SB();
    lds_rd128<4096>(fb[3], xa_2); SB();
    mmw<5>(ac[1][1][1], fb[7], fb[4]); SB();
    lds_rd128<34816>(fb[4], wa0_0); SB();
    mm(ac[1][1][0], fb[7], fb[6]); SB();
    lds_rd128<5120>(fb[6], xa_2); SB();
    mmw<5>(ac[1][0][0], fb[2], fb[1]); SB();
    lds_rd128<32768>(fb[7], wa1_0); SB();
    mm(ac[1][0][1], fb[2], fb[0]); SB();
    lds_rd128<6144>(fb[2], xa_2); SB();
    mmw<6>(ac[1][1][1], fb[8], fb[0]); SB();
    lds_rd128<34816>(fb[0], wa1_0); SB();
    mm(ac[1][1][0], fb[8], fb[1]); SB();
    lds_rd128<7168>(fb[1], xa_2); SB();
    mmw<6>(ac[1][0][0], fb[3], fb[5]); SB();
    lds_rd128<8192>(fb[8], xa_2); SB();
    mmw<6>(ac[1][0][1], fb[3], fb[4]); SB();
    lds_rd128<9216>(fb[3], xa_2); SB();
    mmw<6>(ac[1][1][1], fb[6], fb[4]); SB();
    lds_rd128<10240>(fb[4], xa_2); SB();
    mm(ac[1][1][0], fb[6], fb[5]); SB();
    lds_rd128<11264>(fb[5], xa_2); SB();
    mmw<6>(ac[1][0][0], fb[2], fb[7]); SB();
    lds_rd128<12288>(fb[6], xa_2); SB();
    mmw<6>(ac[1][0][1], fb[2], fb[0]); SB();
    mmw<5>(ac[1][1][1], fb[1], fb[0]); SB();
    lds_rd128<13312>(fb[2], xa_2); SB();
    lds_rd32(fl, lf_rd); SB();
    mm(ac[1][1][0], fb[1], fb[7]); SB();
    mmw<6>(ac[2][0][0], fb[8], wr[8][0]); SB();
    lds_rd128<14336>(fb[0], xa_2); SB();
    mm(ac[2][0][1], fb[8], wr[8][1]); SB();
    mmw<6>(ac[2][1][1], fb[3], wr[8][1]); SB();
    lds_rd128<15360>(fb[1], xa_2); SB();
    mm(ac[2][1][0], fb[3], wr[8][0]); SB();
    EM0(1, 0); SB();
    mmchkw<27, 2>(ac[2][0][0], fb[4], wr[8][2], fl, lf_rd, bud); SB();
    lds_rd128<49152>(fb[7], xa_0); SB();
    EM0(1, 1); SB();
    mm(ac[2][0][1], fb[4], wr[8][3]); SB();
    EM1(1, 0, 3, 4); SB();
    EM1(1, 1, 11, 12); SB();
    EM1(1, 0, 5, 6); SB();
    mm(ac[2][1][1], fb[5], wr[8][3]); SB();
    lds_rd128<50176>(fb[8], xa_0); SB();
    EM1(1, 1, 13, 14); SB();
    EM1(1, 0, 7, 15); SB();
    mm(ac[2][1][0], fb[5], wr[8][2]); SB();
    EK(0); SB();
    EM2(); SB();
    EK(1); SB();
    mm(ac[2][0][0], fb[6], wr[9][0]); SB();
    lds_rd128<51200>(fb[3], xa_0); SB();
    ES0(); SB();
    ES1(); SB();
    mm(ac[2][0][1], fb[6], wr[9][1]); SB();
    mm(ac[2][1][1], fb[2], wr[9][1]); SB();
    lds_rd128<52224>(fb[4], xa_0); SB();
    EX(1); SB();
    EH(1, 0); SB();
    mm(ac[2][1][0], fb[2], wr[9][0]); SB();
    EH(1, 1); SB();
    EH(1, 2); SB();
    mmw<4>(ac[2][0][0], fb[0], wr[9][2]); SB();
    lds_rd128<53248>(fb[5], xa_0); SB();
    EH(1, 3); SB();
    ET(0); SB();
    mm(ac[2][0][1], fb[0], wr[9][3]); SB();
    ET(1); SB();
    ET(2); SB();
    EU(3); SB();
    mm(ac[2][1][1], fb[1], wr[9][3]); SB();
    lds_rd128<54272>(fb[6], xa_0); SB();
    mm(ac[2][1][0], fb[1], wr[9][2]); SB();
    SEND(1, 0); SB();
    mmw<4>(ac[2][0][0], fb[7], wr[10][0]); SB();
    lds_rd128<55296>(fb[2], xa_0); SB();
    EH(1, 4); SB();
    EH(1, 5); SB();
    mm(ac[2][0][1], fb[7], wr[10][1]); SB();
    EH(1, 6); SB();
    EH(1, 7); SB();
    ET(4); SB();
    mm(ac[2][1][1], fb[8], wr[10][1]); SB();
    lds_rd128<56320>(fb[0], xa_0); SB();
    ET(5); SB();
    ET(6); SB();
    mm(ac[2][1][0], fb[8], wr[10][0]); SB();
    EU(7); SB();
    mmw<4>(ac[2][0][0], fb[3], wr[10][2]); SB();
    lds_rd128<57344>(fb[1], xa_0); SB();
    SEND(1, 1); SB();
    mm(ac[2][0][1], fb[3], wr[10][3]); SB();
    mm(ac[2][1][1], fb[4], wr[10][3]); SB();
    lds_rd128<58368>(fb[7], xa_0); SB();
    mm(ac[2][1][0], fb[4], wr[10][2]); SB();
    mmw<4>(ac[2][0][0], fb[5], wr[11][0]); SB();
    lds_rd128<59392>(fb[8], xa_0); SB();
    mm(ac[2][0][1], fb[5], wr[11][1]); SB();
    mm(ac[2][1][1], fb[6], wr[11][1]); SB();
    lds_rd128<60416>(fb[3], xa_0); SB();
    mm(ac[2][1][0], fb[6], wr[11][0]); SB();
    mmw<4>(ac[2][0][0], fb[2], wr[11][2]); SB();
    lds_rd128<61440>(fb[4], xa_0); SB();
    mm(ac[2][0][1], fb[2], wr[11][3]); SB();
    mm(ac[2][1][1], fb[0], wr[11][3]); SB();
    lds_rd128<62464>(fb[5], xa_0); SB();
    mm(ac[2][1][0], fb[0], wr[11][2]); SB();
    lds_rd32(fl, lf_rd); SB();
    mmw<5>(ac[2][0][0], fb[1], wr[12][0]); SB();
    lds_rd128<63488>(fb[6], xa_0); SB();
    mm(ac[2][0][1], fb[1], wr[12][1]); SB();
    mm(ac[2][1][1], fb[7], wr[12][1]); SB();
    lds_rd128<64512>(fb[2], xa_0); SB();
    mm(ac[2][1][0], fb[7], wr[12][0]); SB();
    mmw<5>(ac[2][0][0], fb[8], wr[12][2]); SB();
    lds_rd128<0>(fb[0], wa0_1); SB();
    mmchkw<28, 3>(ac[2][0][1], fb[8], wr[12][3], fl, lf_rd, bud); SB();
    lds_rd128<32768>(fb[1], xa_1); SB();
    mm(ac[2][1][1], fb[3], wr[12][3]); SB();
    lds_rd128<2048>(fb[7], wa0_1); SB();
    mm(ac[2][1][0], fb[3], wr[12][2]); SB();
    lds_rd128<33792>(fb[8], xa_1); SB();
    mm(ac[2][0][0], fb[4], wr[13][0]); SB();
    lds_rd128<0>(fb[3], wa1_1); SB();
    mm(ac[2][0][1], fb[4], wr[13][1]); SB();
    lds_rd128<34816>(fb[4], xa_1); SB();
    mm(ac[2][1][1], fb[5], wr[13][1]); SB();
    mm(ac[2][1][0], fb[5], wr[13][0]); SB();
    lds_rd128<2048>(fb[5], wa1_1); SB();
    mmw<7>(ac[2][0][0], fb[6], wr[13][2]); SB();
    mm(ac[2][0][1], fb[6], wr[13][3]); SB();
    lds_rd128<35840>(fb[6], xa_1); SB();
    mm(ac[2][1][1], fb[2], wr[13][3]); SB();
    mm(ac[2][1][0], fb[2], wr[13][2]); SB();
    lds_rd128<32768>(fb[2], wa0_0); SB();
    mmw<6>(ac[2][0][0], fb[1], fb[0]); SB();
    mm(ac[2][0][1], fb[1], fb[7]); SB();
    lds_rd128<36864>(fb[1], xa_1); SB();
    mmw<5>(ac[2][1][1], fb[8], fb[7]); SB();
    lds_rd128<34816>(fb[7], wa0_0); SB();
    mm(ac[2][1][0], fb[8], fb[0]); SB();
    lds_rd128<37888>(fb[0], xa_1); SB();
    mmw<5>(ac[2][0][0], fb[4], fb[3]); SB();
    lds_rd128<32768>(fb[8], wa1_0); SB();
    mm(ac[2][0][1], fb[4], fb[5]); SB();
    lds_rd128<38912>(fb[4], xa_1); SB();
    mmw<6>(ac[2][1][1], fb[6], fb[5]); SB();
    lds_rd128<34816>(fb[5], wa1_0); SB();
    mm(ac[2][1][0], fb[6], fb[3]); SB();
    lds_rd128<39936>(fb[3], xa_1); SB();
    mmw<6>(ac[2][0][0], fb[1], fb[2]); SB();
    lds_rd128<40960>(fb[6], xa_1); SB();
    mmw<6>(ac[2][0][1], fb[1], fb[7]); SB();
    lds_rd128<41984>(fb[1], xa_1); SB();
    mmw<6>(ac[2][1][1], fb[0], fb[7]); SB();
    lds_rd128<43008>(fb[7], xa_1); SB();
    mm(ac[2][1][0], fb[0], fb[2]); SB();
    lds_rd128<44032>(fb[0], xa_1); SB();
    mmw<6>(ac[2][0][0], fb[4], fb[8]); SB();
    lds_rd128<45056>(fb[2], xa_1); SB();
    mmw<6>(ac[2][0][1], fb[4], fb[5]); SB();
    mmw<5>(ac[2][1][1], fb[3], fb[5]); SB();
    lds_rd128<46080>(fb[4], xa_1); SB();
    lds_rd32(fl, lf_rd); SB();
    mm(ac[2][1][0], fb[3], fb[8]); SB();
    mmw<6>(ac[3][0][0], fb[6], wr[6][0]); SB();
    lds_rd128<47104>(fb[5], xa_1); SB();
    mm(ac[3][0][1], fb[6], wr[6][1]); SB();
    mmw<6>(ac[3][1][1], fb[1], wr[6][1]); SB();
    lds_rd128<48128>(fb[3], xa_1); SB();
    mm(ac[3][1][0], fb[1], wr[6][0]); SB();
    EM0(2, 0); SB();
    EM0(2, 1); SB();
    mmchkw<29, 2>(ac[3][0][0], fb[7], wr[6][2], fl, lf_rd, bud); SB();
    lds_rd128<16384>(fb[8], xa_0); SB();
    EM1(2, 0, 3, 4); SB();
    EM1(2, 1, 11, 12); SB();
    mm(ac[3][0][1], fb[7], wr[6][3]); SB();
    EM1(2, 0, 5, 6); SB();
    EM1(2, 1, 13, 14); SB();
    EM1(2, 0, 7, 15); SB();
    EK(0); SB();
    mm(ac[3][1][1], fb[0], wr[6][3]); SB();
    lds_rd128<17408>(fb[6], xa_0); SB();
    EM2(); SB();
    EK(1); SB();
    ES0(); SB();
    mm(ac[3][1][0], fb[0], wr[6][2]); SB();
    ES1(); SB();
    mm(ac[3][0][0], fb[2], wr[7][0]); SB();
    lds_rd128<18432>(fb[1], xa_0); SB();
    EX(2); SB();
    EH(2, 0); SB();
    EH(2, 1); SB();
    mm(ac[3][0][1], fb[2], wr[7][1]); SB();
    EH(2, 2); SB();
    EH(2, 3); SB();
    ET(0); SB();
    ET(1); SB();
    mm(ac[3][1][1], fb[4], wr[7][1]); SB();
    lds_rd128<19456>(fb[7], xa_0); SB();
    ET(2); SB();
    EU(3); SB();
    mm(ac[3][1][0], fb[4], wr[7][0]); SB();
    SEND(2, 0); SB();
    mmw<4>(ac[3][0][0], fb[5], wr[7][2]); SB();
    lds_rd128<20480>(fb[0], xa_0); SB();
    EH(2, 4); SB();
    EH(2, 5); SB();
    EH(2, 6); SB();
    mm(ac[3][0][1], fb[5], wr[7][3]); SB();
    EH(2, 7); SB();
    ET(4); SB();
    ET(5); SB();
    ET(6); SB();
    mm(ac[3][1][1], fb[3], wr[7][3]); SB();
    lds_rd128<21504>(fb[2], xa_0); SB();
    EU(7); SB();
    mm(ac[3][1][0], fb[3], wr[7][2]); SB();
    SEND(2, 1); SB();
    mmw<4>(ac[3][0][0], fb[8], wr[8][0]); SB();
    lds_rd128<22528>(fb[4], xa_0); SB();
    mm(ac[3][0][1], fb[8], wr[8][1]); SB();
    mm(ac[3][1][1], fb[6], wr[8][1]); SB();
    lds_rd128<23552>(fb[5], xa_0); SB();
    mm(ac[3][1][0], fb[6], wr[8][0]); SB();
    mmw<4>(ac[3][0][0], fb[1], wr[8][2]); SB();
    lds_rd128<24576>(fb[3], xa_0); SB();
    mm(ac[3][0][1], fb[1], wr[8][3]); SB();
    mm(ac[3][1][1], fb[7], wr[8][3]); SB();
    lds_rd128<25600>(fb[8], xa_0); SB();
    mm(ac[3][1][0], fb[7], wr[8][2]); SB();
    mmw<4>(ac[3][0][0], fb[0], wr[9][0]); SB();
    lds_rd128<26624>(fb[6], xa_0); SB();
    mm(ac[3][0][1], fb[0], wr[9][1]); SB();
    mm(ac[3][1][1], fb[2], wr[9][1]); SB();
    lds_rd128<27648>(fb[1], xa_0); SB();
    mm(ac[3][1][0], fb[2], wr[9][0]); SB();
    mmw<4>(ac[3][0][0], fb[4], wr[9][2]); SB();
    lds_rd128<28672>(fb[7], xa_0); SB();
    mm(ac[3][0][1], fb[4], wr[9][3]); SB();
    mm(ac[3][1][1], fb[5], wr[9][3]); SB();
    lds_rd128<29696>(fb[0], xa_0); SB();
    lds_rd32(fl, lf_rd); SB();
    mm(ac[3][1][0], fb[5], wr[9][2]); SB();
    mmw<5>(ac[3][0][0], fb[3], wr[10][0]); SB();
    lds_rd128<30720>(fb[2], xa_0); SB();
    mm(ac[3][0][1], fb[3], wr[10][1]); SB();
    mm(ac[3][1][1], fb[8], wr[10][1]); SB();
    lds_rd128<31744>(fb[4], xa_0); SB();
    mm(ac[3][1][0], fb[8], wr[10][0]); SB();
    mmchkw<30, 2>(ac[3][0][0], fb[6], wr[10][2], fl, lf_rd, bud); SB();
    lds_rd128<49152>(fb[5], xa_1); SB();
    mm(ac[3][0][1], fb[6], wr[10][3]); SB();
    mm(ac[3][1][1], fb[1], wr[10][3]); SB();
    lds_rd128<50176>(fb[3], xa_1); SB();
    mm(ac[3][1][0], fb[1], wr[10][2]); SB();
    mm(ac[3][0][0], fb[7], wr[11][0]); SB();
    lds_rd128<51200>(fb[8], xa_1); SB();
    mm(ac[3][0][1], fb[7], wr[11][1]); SB();
    mm(ac[3][1][1], fb[0], wr[11][1]); SB();
    lds_rd128<52224>(fb[6], xa_1); SB();
    mm(ac[3][1][0], fb[0], wr[11][0]); SB();
    mmw<4>(ac[3][0][0], fb[2], wr[11][2]); SB();
    lds_rd128<53248>(fb[1], xa_1); SB();
    mm(ac[3][0][1], fb[2], wr[11][3]); SB();
    mm(ac[3][1][1], fb[4], wr[11][3]); SB();
    lds_rd128<54272>(fb[7], xa_1); SB();
    mm(ac[3][1][0], fb[4], wr[11][2]); SB();
    mmw<4>(ac[3][0][0], fb[5], wr[12][0]); SB();
    lds_rd128<55296>(fb[0], xa_1); SB();
    RV_LD(0, ""); SB();
    mm(ac[3][0][1], fb[5], wr[12][1]); SB();
    mm(ac[3][1][1], fb[3], wr[12][1]); SB();
    lds_rd128<56320>(fb[2], xa_1); SB();
    mm(ac[3][1][0], fb[3], wr[12][0]); SB();
    mmw<4>(ac[3][0][0], fb[8], wr[12][2]); SB();
    lds_rd128<0>(fb[4], wa0_1); SB();
    mm(ac[3][0][1], fb[8], wr[12][3]); SB();
    lds_rd128<57344>(fb[5], xa_1); SB();
    mm(ac[3][1][1], fb[6], wr[12][3]); SB();
    lds_rd128<2048>(fb[3], wa0_1); SB();
    mm(ac[3][1][0], fb[6], wr[12][2]); SB();
    lds_rd128<58368>(fb[8], xa_1); SB();
    mmw<6>(ac[3][0][0], fb[1], wr[13][0]); SB();
    lds_rd128<0>(fb[6], wa1_1); SB();
    mm(ac[3][0][1], fb[1], wr[13][1]); SB();
    lds_rd128<59392>(fb[1], xa_1); SB();
    mm(ac[3][1][1], fb[7], wr[13][1]); SB();
    mm(ac[3][1][0], fb[7], wr[13][0]); SB();
    lds_rd128<2048>(fb[7], wa1_1); SB();
    mmw<7>(ac[3][0][0], fb[0], wr[13][2]); SB();
    mm(ac[3][0][1], fb[0], wr[13][3]); SB();
    lds_rd128<60416>(fb[0], xa_1); SB();
    mm(ac[3][1][1], fb[2], wr[13][3]); SB();
    lds_rd32(f2, d2_rd); SB();
    mm(ac[3][1][0], fb[2], wr[13][2]); SB();
    lds_rd128<32768>(fb[2], wa0_0); SB();
    mmw<7>(ac[3][0][0], fb[5], fb[4]); SB();
    RV_LD(1, ""); SB();
    mm(ac[3][0][1], fb[5], fb[3]); SB();
    lds_rd128<61440>(fb[5], xa_1); SB();
    mmw<6>(ac[3][1][1], fb[8], fb[3]); SB();
    lds_rd128<34816>(fb[3], wa0_0); SB();
    mm(ac[3][1][0], fb[8], fb[4]); SB();
    lds_rd128<62464>(fb[4], xa_1); SB();
    mmw<6>(ac[3][0][0], fb[1], fb[6]); SB();
    lds_rd128<32768>(fb[8], wa1_0); SB();
    asm volatile("s_waitcnt lgkmcnt(5)" : "+v"(fb[0]), "+v"(f2)::"memory"); SB();
    chk<1>(f2, d2_rd, bud); SB();
    D_RD(0); SB();
    D_RD(1); SB();
    D_RD(2); SB();
    D_RD(3); SB();
    G_RD(0); SB();
    G_RD(1); SB();
    mm(ac[3][0][1], fb[1], fb[7]); SB();
    lds_rd128<63488>(fb[1], xa_1); SB();
    mm(ac[3][1][1], fb[0], fb[7]); SB();
    lds_rd128<34816>(fb[7], wa1_0); SB();
    mm(ac[3][1][0], fb[0], fb[6]); SB();
    lds_rd128<64512>(fb[0], xa_1); SB();
    RV_LD(2, ""); SB();
    mmw<12>(ac[3][0][0], fb[5], fb[2]); SB();
    mmw<11>(ac[3][0][1], fb[5], fb[3]); SB();
    mmw<10>(ac[3][1][1], fb[4], fb[3]); SB();
    mm(ac[3][1][0], fb[4], fb[2]); SB();
    mmw<2>(ac[3][0][0], fb[1], fb[8]); SB();
    mmw<1>(ac[3][0][1], fb[1], fb[7]); SB();
    mmw<0>(ac[3][1][1], fb[0], fb[7]); SB();
    mm(ac[3][1][0], fb[0], fb[8]); SB();
    asm volatile("s_nop 7" : "+v"(ac[3][0][0]), "+v"(ac[3][0][1]), "+v"(ac[3][1][0]), "+v"(ac[3][1][1])::"memory");
    const float om[2] = {rbf(1.0f - gs[0]), rbf(1.0f - gs[1])};
    gs[0] = h ? -gs[0] : gs[0];
    gs[1] = h ? -gs[1] : gs[1];
    float y[16];
#pragma unroll
    for (int p = 0; p < 8; ++p) {
      const unsigned o = dq[p >> 1][p & 1];
      const f32x2 v = f32x2{AV(3, 2 * p), AV(3, 2 * p + 1)} * gs[p >> 2] + f32x2{lo2f(o), hi2f(o)} * om[p >> 2];
      y[2 * p] = v[0];
      y[2 * p + 1] = v[1];
    }
    asm volatile("" : "+v"(oo), "+s"(out), "+v"(gs[0]), "+v"(gs[1]));
    auto strip = [&](u32x4 (&r)[2]) {
      const u32x4 t = {tb[0] ^ 0x40004u, tb[1] ^ 0x40004u, tb[2] ^ 0x40004u, tb[3] ^ 0x40004u};
      r[0] ^= t;
      r[1] ^= t;
      const unsigned o = ((r[0].x | r[0].y | r[0].z | r[1].x | r[1].y | r[1].z) & 0x30003u) | ((r[0].w | r[1].w) & 0x30000u);
      const unsigned a = r[0].x & r[0].y & r[0].z & r[0].w & r[1].x & r[1].y & r[1].z & r[1].w & 0x40004u;
      return (o | a) == 0x40004u;
    };
#define RV_FIN(S, Y)                                                                                                     \
  asm volatile("s_waitcnt vmcnt(%18)" : "+v"(rv[S][0]), "+v"(rv[S][1]), "+v"(y[0]), "+v"(y[1]), "+v"(y[2]), "+v"(y[3]), "+v"(y[4]), "+v"(y[5]), \
               "+v"(y[6]), "+v"(y[7]), "+v"(y[8]), "+v"(y[9]), "+v"(y[10]), "+v"(y[11]), "+v"(y[12]), "+v"(y[13]), "+v"(y[14]), "+v"(y[15])     \
               : "i"(Y) : "memory");                                                                                     \
  if (__builtin_expect(!__all(strip(rv[S])), 0)) {                                                                       \
    _Pragma("clang loop unroll(disable)") for (int it = 0; it < SPIN_MAX; ++it) {                                        \
      RV_LD(S, "\n\ts_waitcnt vmcnt(0)");                                                                                \
      if (__all(strip(rv[S]))) break;                                                                                    \
    }                                                                                                                    \
  }                                                                                                                      \
  {                                                                                                                      \
    const float sc = __uint_as_float((((rv[S][0].w & 3u) | (rv[S][1].w & 3u) << 2) << 27) + ((S) == 2 ? 0xff800000u : 0x7f800000u)); \
    rv[S][0].w &= ~3u;                                                                                                   \
    rv[S][1].w &= ~3u;                                                                                                   \
    _Pragma("unroll") for (int p = 0; p < 8; ++p) {                                                                      \
      const float f = sc * gs[p >> 2];                                                                                   \
      unsigned o = rv[S][p >> 2][p & 3];                                                                                 \
      asm("v_fma_mix_f32 %0, %2, %3, %0 op_sel_hi:[1,0,0]\n\tv_fma_mix_f32 %1, %2, %3, %1 op_sel:[1,0,0] op_sel_hi:[1,0,0]" \
          : "+v"(y[2 * p]), "+v"(y[2 * p + 1]) : "v"(o), "v"(f));                                                        \
    }                                                                                                                    \
  }
    RV_FIN(0, 4);
    RV_FIN(1, 2);
    RV_FIN(2, 0);
    const unsigned idx = (4095u - (__builtin_amdgcn_readfirstlane(f2) & 0xfffu)) & (S_ - 1);
    if (__builtin_expect(idx >> 5 == (unsigned)(ti * 4 + wid), 0)) {
      SPIN(d_rd, 1);
      u32x2 dn_[2];
      asm volatile("ds_read_b64 %0, %2\n\tds_read_b64 %1, %2 offset:32\n\ts_waitcnt lgkmcnt(0)" : "=&v"(dn_[0]), "=&v"(dn_[1]) : "v"(sb + RING + L_UPD + fq * 8) : "memory");
#pragma unroll
      for (int p = 0; p < 8; ++p)
        if ((idx & 31u) == (unsigned)((p >> 2) * 16 + fr)) {
          const unsigned o = dq[p >> 1][p & 1], n = dn_[p >> 1 & 1][p & 1];
          y[2 * p] += om[p >> 2] * (lo2f(n) - lo2f(o));
          y[2 * p + 1] += om[p >> 2] * (hi2f(n) - hi2f(o));
        }
    }
#pragma unroll
    for (int mi = 0; mi < 2; ++mi) {
      const float* z = y + mi * 8;
      const auto s0 = __builtin_amdgcn_permlane16_swap(pk2(z[0], z[1]), pk2(z[4], z[5]), false, false);
      const auto s1 = __builtin_amdgcn_permlane16_swap(pk2(z[2], z[3]), pk2(z[6], z[7]), false, false);
      asm volatile("global_store_dwordx4 %0, %1, %2\n\ts_nop 1" ::"v"(oo + mi * (16u * D_ * 2)), "v"(u32x4{s0[0], s1[0], s0[1], s1[1]}), "s"(out) : "memory");
    }
  }
}

extern "C" void kernel_launch(void* const* d_in, const int* in_sizes, int n_in, void* d_out, int out_size,
                              void* d_ws, size_t ws_size, hipStream_t stream) {
  const u16* x = (const u16*)d_in[0];
  const u16* wsre = (const u16*)d_in[1];
  const u16* wsim = (const u16*)d_in[2];
  const u16* wdre = (const u16*)d_in[3];
  const u16* wdim = (const u16*)d_in[4];
  const u16* w_r = (const u16*)d_in[6];
  const u16* w_gate = (const u16*)d_in[7];
  const u16* w_up = (const u16*)d_in[8];
  const u16* w_down = (const u16*)d_in[9];
  const u16* gate_w = (const u16*)d_in[10];
  u16* out = (u16*)d_out;
  fused_k<<<256, 512, 0, stream>>>(wsre, wsim, x, w_r, gate_w, wdre, wdim, w_gate, w_up, w_down, (char*)d_ws, out);
}
```

```cpp
#include <hip/hip_runtime.h>
#include <hip/hip_bf16.h>
#include <stdint.h>
#include <type_traits>

using bf16 = __hip_bfloat16;
using bf16x8 = __attribute__((ext_vector_type(8))) short;
using f32x4 = __attribute__((ext_vector_type(4))) float;
using f32x2 = __attribute__((ext_vector_type(2))) float;
using u32x4 = __attribute__((ext_vector_type(4))) unsigned;
using u32x2 = __attribute__((ext_vector_type(2))) unsigned;
typedef __attribute__((ext_vector_type(2))) __bf16 bf16x2;
typedef unsigned short u16;
typedef unsigned long long u64;
typedef __attribute__((address_space(3))) unsigned lds_u32;

constexpr int S_ = 2048, D_ = 512, F_ = 1792;

__device__ __forceinline__ float bf2f(u16 v) { return __uint_as_float(((unsigned)v) << 16); }
__device__ __forceinline__ u16 f2bf(float f) {
  bf16 h = __float2bfloat16(f);
  return *reinterpret_cast<u16*>(&h);
}
__device__ __forceinline__ float rbf(float f) { return bf2f(f2bf(f)); }
__device__ __forceinline__ unsigned pk2(float a, float b) {
  return __builtin_bit_cast(unsigned, __builtin_convertvector(f32x2{a, b}, bf16x2));
}
__device__ __forceinline__ float lo2f(unsigned v) { return __uint_as_float(v << 16); }
__device__ __forceinline__ float hi2f(unsigned v) { return __uint_as_float(v & 0xffff0000u); }
__device__ __forceinline__ float sigmoidf_(float v) { return 1.0f / (1.0f + __expf(-v)); }

template <int CTRL>
__device__ __forceinline__ unsigned dpp(unsigned v) { return __builtin_amdgcn_update_dpp(0, v, CTRL, 0xf, 0xf, false); }
template <int CTRL>
__device__ __forceinline__ float dpp_add(float v) { return v + __uint_as_float(dpp<CTRL>(__float_as_uint(v))); }
constexpr int DPP_X1 = 0xb1, DPP_X2 = 0x4e, DPP_HM = 0x141, DPP_M = 0x140;
__device__ __forceinline__ unsigned row_max(unsigned v) {
  v = max(v, dpp<DPP_X1>(v));
  v = max(v, dpp<DPP_X2>(v));
  v = max(v, dpp<DPP_HM>(v));
  return max(v, dpp<DPP_M>(v));
}
__device__ __forceinline__ float sw32(float a, float b) {
  const auto r = __builtin_amdgcn_permlane32_swap(__float_as_uint(a), __float_as_uint(b), false, false);
  return __uint_as_float(r[0]) + __uint_as_float(r[1]);
}
__device__ __forceinline__ float sw16(float a, float b) {
  const auto r = __builtin_amdgcn_permlane16_swap(__float_as_uint(a), __float_as_uint(b), false, false);
  return __uint_as_float(r[0]) + __uint_as_float(r[1]);
}
__device__ __forceinline__ unsigned wave_max(unsigned v) {
  v = row_max(v);
  const auto r = __builtin_amdgcn_permlane16_swap(v, v, false, false);
  v = max(r[0], r[1]);
  const auto q = __builtin_amdgcn_permlane32_swap(v, v, false, false);
  return max(q[0], q[1]);
}

#define WAIT_V(n) asm volatile("s_waitcnt vmcnt(%0)" ::"i"(n) : "memory")
#define WAIT_L(n) asm volatile("s_waitcnt lgkmcnt(%0)" ::"i"(n) : "memory")
#define SB() __builtin_amdgcn_sched_barrier(0)

constexpr int A_B = 128 * 64 * 2;
constexpr int RING = 155648;
constexpr int SCR = 8192;
constexpr int SPIN_MAX = 1 << 16;
constexpr int L_LF = 0;
constexpr int L_CP = 1024;
constexpr int L_RG = 2048;
constexpr int L_G = 4096;
constexpr int L_FLAG = 256;
constexpr int L_H = 4608;
constexpr int L_UPD = 4736;
constexpr int L_RF = 4992;
constexpr int L_X = 7168;

constexpr size_t W_AB = 0;
constexpr size_t W_XB = (size_t)4 << 20;
constexpr size_t W_E = (size_t)16 << 20;
constexpr size_t W_BEST = W_E + 4096;
constexpr size_t W_SEEN = W_E + 8192;
constexpr size_t W_KEY = W_E + 16384;
constexpr size_t W_PA = W_E + 65536;
constexpr size_t W_PB = W_E + 524288;
constexpr unsigned C_SEEN = W_SEEN - W_E, C_BEST = W_BEST - W_E, C_KEY = W_KEY - W_E, C_PA = W_PA - W_E, C_PB = W_PB - W_E;
constexpr int PSLEEP = 4, LSLEEP = 1, GSLEEP = 1, FSTR = 64;
constexpr unsigned C_FL2 = 1u << 20;

template <int OFF>
__device__ __forceinline__ void lds_rd128(bf16x8& d, unsigned a) {
  asm volatile("ds_read_b128 %0, %1 offset:%2" : "=v"(d) : "v"(a), "i"(OFF) : "memory");
}
__device__ __forceinline__ void glds(unsigned lds, unsigned voff, const void* sbase) {
  asm volatile("s_mov_b32 m0, %0\n\ts_nop 0\n\tglobal_load_lds_dwordx4 %1, %2" ::"s"(lds), "v"(voff), "s"(sbase) : "memory");
}
#define GLDS4(POL) asm volatile("s_add_u32 m0, %0, %6\n\ts_nop 0\n\tglobal_load_lds_dwordx4 %1, %5" POL "\n\t"                        \
                                "global_load_lds_dwordx4 %2, %5 offset:1024" POL "\n\tglobal_load_lds_dwordx4 %3, %5 offset:2048" POL "\n\t" \
                                "global_load_lds_dwordx4 %4, %5 offset:3072" POL                                                       \
                                ::"s"(lds), "v"(v0), "v"(v1), "v"(v2), "v"(v3), "s"(sbase), "i"(OFF) : "scc", "memory")
template <int OFF>
__device__ __forceinline__ void glds4(unsigned lds, unsigned v0, unsigned v1, unsigned v2, unsigned v3, const void* sbase) { GLDS4(""); }
template <int OFF>
__device__ __forceinline__ void glds4_sc1(unsigned lds, unsigned v0, unsigned v1, unsigned v2, unsigned v3, const void* sbase) { GLDS4(" sc1"); }
template <int ADD, int MASK, int SH>
__device__ __forceinline__ const char* padd(const char* base, unsigned o) {
  const u64 bb = (u64)base;
  unsigned lo, hi;
  asm volatile("s_add_u32 %0, %4, %5\n\ts_and_b32 %0, %0, %6\n\ts_lshl_b32 %0, %0, %7\n\ts_add_u32 %0, %2, %0\n\ts_addc_u32 %1, %3, 0"
               : "=&s"(lo), "=s"(hi)
               : "s"((unsigned)bb), "s"((unsigned)(bb >> 32)), "s"(o), "i"(ADD), "i"(MASK), "i"(SH)
               : "scc");
  return (const char*)(((u64)hi << 32) | lo);
}
__device__ __forceinline__ void gate(u64& seen, u64 want, unsigned sh, unsigned fo, unsigned tag, const void* wc, unsigned& bud) {
  u64 t, w;
  unsigned v;
  asm volatile("s_lshl_b64 %[w], %[want], %[sh]\n\ts_andn2_b64 %[t], %[w], %[seen]\n\ts_cbranch_scc0 .Lsb%=\n"
               ".Lsa%=:\n\tglobal_load_dword %[v], %[fo], %[wc] sc1\n\ts_waitcnt vmcnt(0)\n\tv_cmp_eq_u32 %[t], %[tag], %[v]\n\t"
               "s_or_b64 %[seen], %[seen], %[t]\n\ts_andn2_b64 %[t], %[w], %[seen]\n\ts_cbranch_scc0 .Lsb%=\n\ts_sleep %[sl]\n\t"
               "s_sub_u32 %[bud], %[bud], 16\n\ts_cbranch_scc0 .Lsa%=\n\ts_mov_b32 %[bud], 0\n.Lsb%=:"
               : [seen] "+s"(seen), [t] "=&s"(t), [w] "=&s"(w), [v] "=&v"(v), [bud] "+s"(bud)
               : [want] "s"(want), [sh] "s"(sh), [fo] "v"(fo), [tag] "s"(tag), [wc] "s"(wc), [sl] "i"(GSLEEP)
               : "scc", "memory");
}
template <int S>
__device__ __forceinline__ const char* k1tile(const char* base, unsigned rot) { return padd<S * 128, 0x380, 0>(base, rot << 7); }
__device__ __forceinline__ void gst128_sc1(unsigned voff, u32x4 v, const void* sbase) {
  asm volatile("global_store_dwordx4 %0, %1, %2 sc1\n\ts_nop 1" ::"v"(voff), "v"(v), "s"(sbase) : "memory");
}

__device__ __forceinline__ void lds_rd32(unsigned& d, unsigned a) { asm volatile("ds_read_b32 %0, %1" : "=v"(d) : "v"(a) : "memory"); }
__device__ __forceinline__ void sigv(unsigned row, const void* tab, unsigned n4) {
  asm volatile("s_mov_b32 m0, %0\n\ts_nop 0\n\tglobal_load_lds_dword %2, %1" ::"s"(row), "s"(tab), "v"(n4) : "memory");
}
template <int N>
__device__ __forceinline__ void sig(unsigned row, const void* tab) {
  unsigned t;
  asm volatile("s_mov_b32 m0, %1\n\tv_mov_b32 %0, %3\n\tglobal_load_lds_dword %0, %2" : "=&v"(t) : "s"(row), "s"(tab), "i"(N * 4) : "memory");
}
template <int N>
__device__ __forceinline__ void spin(unsigned a, unsigned& bud) {
  unsigned t;
  asm volatile(".Lsa%=:\n\tds_read_b32 %0, %2\n\ts_waitcnt lgkmcnt(0)\n\tv_cmp_gt_u32 vcc, %3, %0\n\ts_cbranch_vccz .Lsb%=\n\t"
               "s_sub_u32 %1, %1, 1\n\ts_cbranch_scc0 .Lsa%=\n\ts_mov_b32 %1, 0\n.Lsb%=:"
               : "=&v"(t), "+s"(bud) : "v"(a), "i"(N) : "vcc", "scc", "memory");
}
#define FT0 ""
#define FT1 "\n"
#define FLAG_ASM "s_getreg_b32 %[ib], hwreg(HW_REG_IB_STS)\n\ts_and_b32 %[ib], %[ib], 0xc0000f\n\ts_or_b32 %[ib], %[ib], %[done]\n\t"   \
                 "s_cmp_gt_u32 %[ib], %[y]\n\ts_cbranch_scc1 .Lsk%=\n\ts_waitcnt vmcnt(%[y])\n\ts_mov_b64 exec, 0xffff\n\t"                     \
                 FT0 "global_store_dword %[fo], %[tag], %[wc] sc1\n\ts_mov_b64 exec, -1\n\ts_mov_b32 %[done], 64\n\t" FT1 ".Lsk%=:\n\t"
template <int Y>
__device__ __forceinline__ void tryflag(unsigned& done, unsigned fo, unsigned tag, const void* wc) {
  static_assert(Y < 16, "");
  unsigned ib;
  asm volatile(FLAG_ASM : [ib] "=&s"(ib), [done] "+s"(done) : [fo] "v"(fo), [tag] "v"(tag), [wc] "s"(wc), [y] "i"(Y) : "scc", "memory");
}
template <int N, int Y>
__device__ __forceinline__ void spinf(unsigned a, unsigned& bud, unsigned& done, unsigned fo, unsigned tag, const void* wc) {
  static_assert(Y < 16, "");
  unsigned t, ib;
  asm volatile(".Lsa%=:\n\tds_read_b32 %[t], %[a]\n\t" FLAG_ASM "s_waitcnt lgkmcnt(0)\n\tv_cmp_gt_u32 vcc, %[n], %[t]\n\ts_cbranch_vccz .Lsb%=\n\t"
               "s_sub_u32 %[bud], %[bud], 1\n\ts_cbranch_scc0 .Lsa%=\n\ts_mov_b32 %[bud], 0\n.Lsb%=:"
               : [t] "=&v"(t), [bud] "+s"(bud), [ib] "=&s"(ib), [done] "+s"(done)
               : [a] "v"(a), [n] "i"(N), [fo] "v"(fo), [tag] "v"(tag), [wc] "s"(wc), [y] "i"(Y)
               : "vcc", "scc", "memory");
}
template <int N, int SL>
__device__ __forceinline__ void serve(unsigned a, unsigned& bud) {
  unsigned t;
  asm volatile(".Lsa%=:\n\tds_read_b32 %0, %2\n\ts_waitcnt lgkmcnt(0)\n\tv_cmp_gt_u32 vcc, %3, %0\n\ts_cbranch_vccz .Lsb%=\n\ts_sleep %4\n\t"
               "s_sub_u32 %1, %1, 1\n\ts_cbranch_scc0 .Lsa%=\n\ts_mov_b32 %1, 0\n.Lsb%=:"
               : "=&v"(t), "+s"(bud) : "v"(a), "i"(N), "i"(SL) : "vcc", "scc", "memory");
}
__device__ __forceinline__ void mm(f32x4& d, const bf16x8& a, const bf16x8& b) {
  asm volatile("v_mfma_f32_16x16x32_bf16 %0, %1, %2, %0" : "+v"(d) : "v"(a), "v"(b));
}
template <int N>
__device__ __forceinline__ void mmw(f32x4& d, const bf16x8& a, const bf16x8& b) {
  __builtin_amdgcn_s_waitcnt(0xc07f | (N << 8));
  SB();
  asm volatile("v_mfma_f32_16x16x32_bf16 %0, %1, %2, %0" : "+v"(d) : "v"(a), "v"(b) : "memory");
}
__device__ __forceinline__ void mm0(f32x4& d, const bf16x8& a, const bf16x8& b) {
  asm volatile("v_mfma_f32_16x16x32_bf16 %0, %1, %2, 0" : "=&v"(d) : "v"(a), "v"(b));
}
#define CSTOP STOP
#define STOP "7777:\n\ts_cbranch_execz 7777f\n"
#define TOUR() asm volatile("s_getpc_b64 vcc\n\ts_add_u32 vcc_lo, vcc_lo, 16\n\ts_addc_u32 vcc_hi, vcc_hi, 0\n\ts_mov_b64 exec, 0\n\ts_branch .Lfk_tour\n\ts_mov_b64 exec, -1" ::: "vcc", "scc", "memory")
#define CHK_TAIL "s_cbranch_vccnz .Lsa%=\n.Lsb%=:\n\t.subsection 1\n" CSTOP ".Lsa%=:\n\tds_read_b32 %[fl], %[a]\n\ts_waitcnt lgkmcnt(0)\n\t"        \
                 "v_cmp_gt_u32 vcc, %[n], %[fl]\n\ts_cbranch_vccz .Lsb%=\n\ts_sub_u32 %[bud], %[bud], 1\n\ts_cbranch_scc0 .Lsa%=\n\t"       \
                 "s_mov_b32 %[bud], 0\n\ts_branch .Lsb%=\n\t.subsection 0"
template <int N>
__device__ __forceinline__ void mmchk(f32x4& d, const bf16x8& x, const bf16x8& y, unsigned& fl, unsigned a, unsigned& bud) {
  asm volatile("v_cmp_gt_u32 vcc, %[n], %[fl]\n\tv_mfma_f32_16x16x32_bf16 %[d], %[x], %[y], %[d]\n\t" CHK_TAIL
               : [d] "+v"(d), [fl] "+v"(fl), [bud] "+s"(bud) : [x] "v"(x), [y] "v"(y), [a] "v"(a), [n] "i"(N) : "vcc", "scc", "memory");
}
template <int N, int W>
__device__ __forceinline__ void mmchkw(f32x4& d, const bf16x8& x, const bf16x8& y, unsigned& fl, unsigned a, unsigned& bud) {
  __builtin_amdgcn_s_waitcnt(0xc07f | (W << 8));
  SB();
  asm volatile("v_cmp_gt_u32 vcc, %[n], %[fl]\n\tv_mfma_f32_16x16x32_bf16 %[d], %[x], %[y], %[d]\n\t" CHK_TAIL
               : [d] "+v"(d), [fl] "+v"(fl), [bud] "+s"(bud) : [x] "v"(x), [y] "v"(y), [a] "v"(a), [n] "i"(N) : "vcc", "scc", "memory");
}
template <int N>
__device__ __forceinline__ void mm0w(f32x4& d, const bf16x8& a, const bf16x8& b) {
  asm volatile("s_waitcnt lgkmcnt(%3)\n\tv_mfma_f32_16x16x32_bf16 %0, %1, %2, 0" : "=&v"(d) : "v"(a), "v"(b), "i"(N) : "memory");
}
template <int N>
__device__ __forceinline__ void chk(unsigned& fl, unsigned a, unsigned& bud) {
  asm volatile("v_cmp_gt_u32 vcc, %[n], %[fl]\n\t" CHK_TAIL : [fl] "+v"(fl), [bud] "+s"(bud) : [a] "v"(a), [n] "i"(N) : "vcc", "scc", "memory");
}

__device__ __attribute__((used, noinline, aligned(4096))) void fk_space() { asm volatile(".fill 376, 4, 0xbf800000"); }
__global__ __launch_bounds__(512, 2) void fused_k(const u16* __restrict__ wsre, const u16* __restrict__ wsim,
                                               const u16* __restrict__ x, const u16* __restrict__ w_r,
                                               const u16* __restrict__ gate_w, const u16* __restrict__ wdre,
                                               const u16* __restrict__ wdim, const u16* __restrict__ w_gate,
                                               const u16* __restrict__ w_up, const u16* __restrict__ w_down,
                                               char* ws, u16* __restrict__ out) {
  __shared__ __attribute__((aligned(16))) char smem[RING + SCR];
  static_assert(RING + SCR == 163840, "the whole LDS: one block per CU, the array starts at LDS address 0");
  asm volatile("ds_write2st64_b32 %0, %1, %1 offset1:8\n\ts_waitcnt lgkmcnt(0)\n\ts_barrier" ::"v"((unsigned)(size_t)smem + RING + ((threadIdx.x ^ 256u) << 2)), "v"(0u) : "memory");
  asm volatile("" ::"s"(wsre), "s"(wsim), "s"(x), "s"(w_r), "s"(gate_w), "s"(wdre), "s"(wdim), "s"(w_gate), "s"(w_up), "s"(w_down), "s"(ws));
  const char* wc = ws + W_E;
  const unsigned c_in = (unsigned)__builtin_amdgcn_s_memtime(), r_in = (unsigned)__builtin_amdgcn_s_memrealtime();
#define UNTIL_(now, t0, lim) do { _Pragma("clang loop unroll(disable)") for (int i_ = 0; i_ < 4096; ++i_) { if ((unsigned)(now) - (t0) >= (lim)) break; __builtin_amdgcn_s_sleep(1); } } while (0)
#define UNTIL(ticks, cyc) do { UNTIL_(__builtin_amdgcn_s_memrealtime(), r_in, ticks); UNTIL_(__builtin_amdgcn_s_memtime(), c_in, cyc); } while (0)
  const int tid = threadIdx.x, lane = tid & 63;
  const int wid = __builtin_amdgcn_readfirstlane(tid >> 6);
  const int b = blockIdx.x;
  const int xcd = b & 7, off = b >> 3;
  const int q = xcd >> 1;
  const int h = q >> 1, kh = q & 1;
  const int ej = off & 3, tl = off >> 2;
  const int e0 = (ej >> 1) * 256 + q * 64 + (ej & 1) * 32;
  const bool isq = ej & 1;
  unsigned isp = (unsigned)(~ej & 1);
  asm volatile("" : "+s"(isp));
  const int ti = (xcd & 1) * 8 + tl;
  const int si = (xcd & 3) * 4 + (tl & 3), cp1 = (tl >> 2) * 2 + (ej >> 1);
  const int fr = lane & 15, fq = lane >> 4;
  const int r8 = lane >> 3, c8 = lane & 7;
  const unsigned sb = (unsigned)(size_t)smem;
  volatile lds_u32* lflag = (volatile lds_u32*)(smem + RING + L_FLAG);
  const char* ab = ws + W_AB;
  const unsigned rot = (unsigned)(tl), flip = (unsigned)((ej & 2) * 4);
#define KADDR(P, SH, base) padd<(P) & 7, 7, SH>((base) + ((((P) & 8) ^ flip) << (SH)), rot)
  const unsigned rot1 = (unsigned)((2 * (cp1 + tl) + (xcd & 1)) & 7);
  const unsigned row_s = __builtin_amdgcn_readfirstlane(sb + RING + ((wid ^ 4) << 8));
  u64 pc;
  unsigned tab_o;
  asm volatile("s_getpc_b64 %0\n.Lfk_here:\n\ts_mov_b32 %1, .Lfk_tab-.Lfk_here\n\t.subsection 2\n\t.p2align 8\n.Lfk_tab:\n\t"
               ".set fk_i, 0\n\t.rept 128\n\t.long fk_i\n\t.set fk_i, fk_i+1\n\t.endr\n\t.subsection 0"
               : "=s"(pc), "=s"(tab_o));
  const char* tab = reinterpret_cast<const char*>(pc + tab_o);
#define SIG(n) sig<n>(row_s, tab)
  const unsigned lf_rd = sb + RING + L_LF + (lane & 1) * 516, cp_rd = sb + RING + ((lane & 7) == 4 ? L_LF + 784 : L_CP + (lane & 3) * 260);
  const unsigned la_rd = sb + RING + L_LF, lb_rd = sb + RING + L_LF + 512;
  const unsigned own_rd = sb + RING + L_FLAG + 32 + (lane & 3) * 4;
#define IC(k) std::integral_constant<int, k>{}

  unsigned bud = 1u << 20;
#define SERVE(ADDR, N) serve<N, LSLEEP>(ADDR, bud)
  const unsigned best_rd = sb + RING + L_FLAG + 12, d_rd = sb + RING + L_FLAG + 48;
  const unsigned rf_rd = sb + RING + L_RG + 1024;
  const unsigned tg_rd = sb + RING + L_FLAG + 56, gt_rd = sb + RING + L_FLAG + 4;
#define TAG_WAIT() do { SERVE(tg_rd, 1); tag = __builtin_amdgcn_readfirstlane(lflag[15]); } while (0)
  const unsigned lbase = __builtin_amdgcn_readfirstlane(sb);
  auto row_addr = [&](unsigned (&va)[8]) {
    unsigned l = (unsigned)lane;
    asm volatile("" : "+v"(l));
#pragma unroll
    for (int i = 0; i < 8; ++i) va[i] = (i * 8 + (l >> 3)) * (D_ * 2) + (((l & 7) ^ (l >> 3)) << 4) - (i & 3) * 1024;
  };
  const char* xs = (const char*)x + (size_t)si * 128 * D_ * 2;
  const char* wds = (const char*)(h ? wdim : wdre) + (size_t)cp1 * 128 * D_ * 2;
  if ((wid | 2) == 6) {
    unsigned va[8];
    row_addr(va);
    const bool a = wid == 4;
    const char* base = a ? xs : wds;
    const unsigned n = isq ? (a ? 1 * 2u : 1 * 2u) : 2u;
    const unsigned r0 = rot1 + (isq ? 1u : 0u);
    unsigned l0 = lbase + (a ? 0 : isq ? 131072 : 81920);
    if (!a && !isq) {
      glds(lbase + RING + L_RF, (unsigned)(lane * 16), w_r);
      glds(lbase + RING + L_RF + 1152, (unsigned)(lane * 16), gate_w);
      sig<1>(lbase + RING + L_RG + 1024, tab);
    }
    if (isq) __builtin_amdgcn_s_sleep(20);
#pragma clang loop unroll(disable)
    for (unsigned u = 0; u < n; ++u) {
      const char* p = base + (((((u >> 1) + r0) & 7u) << 7) + ((u & 1u) << 16));
      glds4<0>(l0, va[0], va[1], va[2], va[3], p);
      glds4<4096>(l0, va[4], va[5], va[6], va[7], p);
      sigv(row_s, tab, ((u + 1u) >> 1) << 2);
      l0 += 8192u;
    }
  } else {
    const unsigned len = tab_o + 512u;
    const unsigned po = (unsigned)((wid * 32 + off) * 1024 + lane * 16);
    if (po + 16u <= len) glds(lbase + RING + L_X, po, reinterpret_cast<const char*>(pc & ~63ull));
  }
  unsigned E;
  asm volatile("s_cmp_lg_u32 %3, 7\n\ts_cbranch_scc1 .Le%=\n\tglobal_load_dword %0, %1, %2 sc1\n.Le%=:" : "=&v"(E) : "v"(0u), "s"(wc), "s"(wid) : "scc", "memory");
  if (wid >= 4) {
  unsigned vw[16], vx[16];
  auto X_ADDR = [&]() {
    unsigned va[8];
    row_addr(va);
#pragma unroll
    for (int i = 0; i < 16; ++i) vx[i] = va[i & 7] + (unsigned)((i >> 3) * 64 * D_ * 2);
  };
  const char* Ws = (const char*)(h ? wsim : wsre) + (size_t)ti * 128 * S_ * 2 + kh * S_;
  auto tile16 = [&](auto O, const char* p, const unsigned (&v)[16]) {
    glds4<decltype(O)::value>(lbase, v[0], v[1], v[2], v[3], p);
    glds4<decltype(O)::value + 4096>(lbase, v[4], v[5], v[6], v[7], p);
    glds4<decltype(O)::value + 8192>(lbase, v[8], v[9], v[10], v[11], p);
    glds4<decltype(O)::value + 12288>(lbase, v[12], v[13], v[14], v[15], p);
  };
  auto W_ADDR = [&]() {
    unsigned r8_ = (unsigned)r8;
    asm volatile("" : "+v"(r8_));
#pragma unroll
    for (int i = 0; i < 16; ++i) vw[i] = (i * 8 + r8_) * (S_ * 2) + (((unsigned)c8 ^ r8_) << 4) - (i & 3) * 1024;
  };
  auto stageW = [&](auto P, auto O) { tile16(O, KADDR(decltype(P)::value, 7, Ws), vw); };
  auto stageW2 = [&](auto P, auto O, auto O2) {
    const char *p = KADDR(decltype(P)::value, 7, Ws), *p2 = KADDR(decltype(P)::value + 1, 7, Ws);
    glds4<decltype(O)::value>(lbase, vw[0], vw[1], vw[2], vw[3], p);
    glds4<decltype(O2)::value>(lbase, vw[0], vw[1], vw[2], vw[3], p2);
    glds4<decltype(O)::value + 4096>(lbase, vw[4], vw[5], vw[6], vw[7], p);
    glds4<decltype(O2)::value + 4096>(lbase, vw[4], vw[5], vw[6], vw[7], p2);
    glds4<decltype(O)::value + 8192>(lbase, vw[8], vw[9], vw[10], vw[11], p);
    glds4<decltype(O2)::value + 8192>(lbase, vw[8], vw[9], vw[10], vw[11], p2);
    glds4<decltype(O)::value + 12288>(lbase, vw[12], vw[13], vw[14], vw[15], p);
    glds4<decltype(O2)::value + 12288>(lbase, vw[12], vw[13], vw[14], vw[15], p2);
  };
  const int qi = (off >> 1) * 8 + xcd;
  const unsigned g_o = (unsigned)(C_KEY + ti * 1024 + (lane >> 2 & 7) * 128 + (lane & 3) * 16);
#define G_PUT(gv, best) asm volatile("ds_write_b128 %0, %1\n\tds_write_b32 %2, %3" ::"v"(sb + RING + L_G + (lane & 31) * 16),                                    \
                                     "v"(u32x4{gv.x << 16, gv.x & 0xffff0000u, gv.y << 16, gv.y & 0xffff0000u}), "v"(best_rd), "v"(best) : "memory")
  if (wid == 4) {
    X_ADDR();
    auto stageX = [&](auto K, auto O) { tile16(O, k1tile<decltype(K)::value>(xs, rot1), vx); };
    W_ADDR();
    if (isp) {
    stageX(IC(1), IC(16384));
    SIG(2);
    stageX(IC(2), IC(32768));
    SIG(3);
    stageX(IC(3), IC(49152));
    SIG(4);
    stageX(IC(4), IC(65536));
    SIG(5);
    SERVE(cp_rd, 1);
    stageX(IC(5), IC(0));
    SIG(6);
    SERVE(cp_rd, 2);
    stageX(IC(6), IC(16384));
    SIG(7);
    SERVE(cp_rd, 3);
    stageX(IC(7), IC(32768));
    SIG(8);
    }
    if (isq) {
    }
    if (isp) SERVE(cp_rd, 4);
    stageW(IC(0), IC(49152));
    if (isp) SERVE(cp_rd, 5);
    stageW(IC(1), IC(65536));
    SIG(9);
    stageW(IC(2), IC(81920));
    if (isp) SERVE(cp_rd, 6);
    stageW(IC(3), IC(0));
    SIG(10);
    SERVE(own_rd, 1);
    stageW(IC(4), IC(98304));
    if (isp) SERVE(cp_rd, 7);
    stageW(IC(5), IC(16384));
    SIG(11);
    stageW(IC(6), IC(114688));
    if (isp) SERVE(cp_rd, 8);
    stageW(IC(7), IC(32768));
    SIG(15);
    if (isq) { SERVE(lb_rd, 12); }
    stageW(IC(8), IC(131072));
    SIG(16);
    SERVE(own_rd, 2);
    stageW(IC(9), IC(49152));
    SIG(17);
    stageW(IC(10), IC(65536));
    SIG(18);
    SERVE(own_rd, 3);
    stageW(IC(11), IC(81920));
    SIG(64);
    bud = 1u << 18;
    __builtin_amdgcn_s_setprio(3);
    unsigned tag;
    TAG_WAIT();
    if (isp) {
      UNTIL(855u, 17991u);
      u32x2 kb;
      u32x4 gv;
#pragma clang loop unroll(disable)
      for (int it = 0; it < SPIN_MAX; ++it) {
        asm volatile("global_load_dwordx2 %0, %2, %4 sc1\n\tglobal_load_dwordx4 %1, %3, %4 sc1\n\ts_waitcnt vmcnt(0)" : "=&v"(kb), "=&v"(gv) : "v"((unsigned)C_BEST), "v"(g_o), "s"(wc) : "memory");
        if (__all(kb.y == tag && gv.w == tag)) break;
        __builtin_amdgcn_s_sleep(PSLEEP);
      }
      G_PUT(gv, kb.x);
    }
    SERVE(best_rd, 1);
    const unsigned best = __builtin_amdgcn_readfirstlane(lflag[3]);
    const int idx = (4095 - (int)(best & 0xfffu)) & (S_ - 1);
    if ((idx >> 7) == ti) {
      const unsigned uo = best & 0xffff0000u;
      const float rsel = rbf(sigmoidf_(__uint_as_float((uo & 0x80000000u) ? (uo & 0x7fffffffu) : (~uo & 0xffff0000u))));
      const int col = (lane & 7) * 2 + (lane >> 5);
      unsigned xv[2];
      asm volatile("global_load_ushort %0, %2, %3\n\tglobal_load_ushort %1, %2, %3 offset:32\n\ts_waitcnt vmcnt(0)"
                   : "=&v"(xv[0]), "=&v"(xv[1]) : "v"((unsigned)((idx * D_ + e0 + col) * 2)), "s"(x) : "memory");
      float rs_ = rsel;
      unsigned pa = sb + RING + L_UPD + col * 2;
      u32x4 ov[8];
      unsigned go = (unsigned)(C_PB + r8 * 4096 + e0 * 8 + c8 * 16);
      unsigned g3 = lane < 32 ? go + 98304u : (unsigned)(C_SEEN + (lane & 7) * 256);
      asm volatile("" : "+v"(rs_), "+v"(pa), "+v"(go), "+v"(g3));
      SERVE(cp_rd, 18); UNTIL(1045u, 21373u);
#pragma clang loop unroll(disable)
      for (int it = 0; it < SPIN_MAX; ++it) {
        asm volatile("global_load_dwordx4 %0, %8, %12 sc1\n\tglobal_load_dwordx4 %1, %9, %12 sc1\n\t"
                     "global_load_dwordx4 %2, %10, %12 sc1\n\tglobal_load_dwordx4 %3, %11, %12 sc1\n\t"
                     "global_load_dwordx4 %4, %8, %12 offset:128 sc1\n\tglobal_load_dwordx4 %5, %9, %12 offset:128 sc1\n\t"
                     "global_load_dwordx4 %6, %10, %12 offset:128 sc1\n\tglobal_load_dwordx4 %7, %11, %12 offset:128 sc1\n\ts_waitcnt vmcnt(0)"
                     : "=&v"(ov[0]), "=&v"(ov[1]), "=&v"(ov[2]), "=&v"(ov[3]), "=&v"(ov[4]), "=&v"(ov[5]), "=&v"(ov[6]), "=&v"(ov[7])
                     : "v"(go), "v"(go + 32768u), "v"(go + 65536u), "v"(g3), "s"(wc)
                     : "memory");
        bool ok = true;
#pragma unroll
        for (int k = 0; k < 8; ++k) ok = ok && ov[k].y == tag && ov[k].w == tag;
        if (__all(ok)) break;
        __builtin_amdgcn_s_sleep(PSLEEP);
      }
      if (ej == 0 && q == 0 && lane == 0)
        asm volatile("global_store_dword %0, %1, %2 sc1\n\ts_nop 1" ::"v"(0u), "v"(tag), "s"(wc) : "memory");
      float u[2];
#pragma unroll
      for (int c = 0; c < 2; ++c) {
        if (lane >= 32) ov[c * 4 + 3].x = ov[c * 4 + 3].z = 0u;
        float s0 = (__uint_as_float(ov[c * 4].x) + __uint_as_float(ov[c * 4 + 1].x)) + (__uint_as_float(ov[c * 4 + 2].x) + __uint_as_float(ov[c * 4 + 3].x));
        float s1 = (__uint_as_float(ov[c * 4].z) + __uint_as_float(ov[c * 4 + 1].z)) + (__uint_as_float(ov[c * 4 + 2].z) + __uint_as_float(ov[c * 4 + 3].z));
        s0 = dpp_add<0x128>(s0); s1 = dpp_add<0x128>(s1);
        const float t2 = sw16(sw32(s0, s1), sw32(s0, s1));
        u[c] = rbf(rbf(t2) * rs_);
      }
      const unsigned p0 = f2bf(lo2f(xv[0]) + u[0]), p1 = f2bf(lo2f(xv[1]) + u[1]);
      if ((lane & 31) < 8)
        asm volatile("ds_write_b16 %0, %1\n\tds_write_b16 %0, %2 offset:32"
                     ::"v"(pa), "v"(p0), "v"(p1) : "memory");
    }
    asm volatile("ds_write_b32 %0, %1\n\ts_waitcnt vmcnt(0) lgkmcnt(0)" ::"v"(d_rd), "v"(1u) : "memory");
  } else if (wid == 6) {
    X_ADDR();
    const unsigned vb0 = (unsigned)(lane * 16), vb2 = vb0 + 2048u;
    const char* abg = ab + (size_t)((h * 4 + ej) * 32 + kh * 8) * A_B;
    const char* abA = abg + (h ^ 1) * 16 * A_B;
    const char* abH = abg + h * 16 * A_B;
    auto stageD = [&](auto K, auto O) { tile16(O, k1tile<decltype(K)::value>(wds, rot1), vx); };
    auto stageQ = [&](auto O) {
      const char* p = (const char*)x + (size_t)ti * 128 * D_ * 2 + e0 * 2;
      const unsigned v = (unsigned)((lane >> 2) * (D_ * 2) + ((lane & 3) ^ (lane >> 4)) * 16);
      glds4<decltype(O)::value>(lbase, v, v + 15360u, v + 30720u, v + 46080u, p);
      glds4<decltype(O)::value + 4096>(lbase, v, v + 15360u, v + 30720u, v + 46080u, p + 64 * (D_ * 2));
    };
    unsigned tag;
    u64 seen = 0ull;
    const unsigned flag_o = (unsigned)(C_FL2 + (((h * 4 + ej) * 2 + kh) * 32 + (lane & 31)) * FSTR);
    auto GATE_ALL = [&]() {
      gate(seen, ~0ull, 0u, flag_o, tag, wc, bud);
      if (isq) asm volatile("ds_write_b32 %0, %1" ::"v"(gt_rd), "v"(1u) : "memory");
    };
    const unsigned dl = (unsigned)(kh ^ 1) * 2048u;
    const unsigned vc0 = vb2 + dl, vc1 = vb2 - dl, vc2 = vc0 + 4096u, vc3 = vc1 + 4096u;
    auto stageA = [&](auto P, auto H, auto O, auto G) {
      constexpr int hf = decltype(H)::value;
      const char* p = KADDR(decltype(P)::value, 13, (hf ? abH : abA) - 2048);
      glds4_sc1<decltype(O)::value>(lbase, vc0, vc0, vc1, vc1, p);
      glds4_sc1<decltype(O)::value + 4096>(lbase, vc2, vc2, vc3, vc3, p);
    };
    auto stageN = [&](auto P, auto O, const char* base) {
      glds4_sc1<decltype(O)::value>(lbase, vb0, vb0, vb2, vb2, KADDR(decltype(P)::value, 13, base));
      glds4_sc1<decltype(O)::value + 4096>(lbase, vb0, vb0, vb2, vb2, KADDR(decltype(P)::value + 1, 13, base));
    };
    auto touch = [&]() {
      asm volatile("s_mov_b32 m0, %0\n\ts_nop 0\n\tglobal_load_lds_dword %1, %2 sc1\n\tglobal_load_lds_dword %1, %3 sc1"
                   ::"s"(lbase + RING + L_X), "v"((unsigned)(lane * 128)), "s"(KADDR(8, 13, abA)), "s"(KADDR(8, 13, abH)) : "memory");
    };
    if (isp) {
    stageD(IC(1), IC(98304));
    SIG(2);
    WAIT_V(18);
    stageD(IC(2), IC(114688));
    SIG(3);
    WAIT_V(18);
    stageD(IC(3), IC(131072));
    SIG(4);
    SERVE(cp_rd, 1);
    WAIT_V(18);
    stageD(IC(4), IC(81920));
    SIG(5);
    SERVE(cp_rd, 2);
    WAIT_V(18);
    stageD(IC(5), IC(98304));
    SIG(6);
    SERVE(cp_rd, 3);
    WAIT_V(18);
    stageD(IC(6), IC(114688));
    SIG(7);
    SERVE(cp_rd, 4);
    WAIT_V(18);
    stageD(IC(7), IC(131072));
    SIG(8);
    }
    if (isq) UNTIL(144u, 3075u);
    stageQ(IC(147456));
    sig<12>(row_s, tab);
    W_ADDR();
    TAG_WAIT();
    SERVE(own_rd, 1);
    SERVE(cp_rd, 8);
    SERVE(la_rd, 9);
    SERVE(own_rd, 3); if (isq) SERVE(own_rd, 5);
    UNTIL(569u, 11787u);
    GATE_ALL();
    SERVE(own_rd, 3);
    stageA(IC(0), IC(0), IC(0), IC(0));
    SIG(13);
    stageA(IC(0), IC(1), IC(8192), IC(0));
    SIG(14);
    SERVE(own_rd, 4);
    stageA(IC(1), IC(0), IC(98304), IC(0));
    SIG(15);
    stageA(IC(1), IC(1), IC(106496), IC(0));
    SIG(16);
    stageA(IC(2), IC(0), IC(16384), IC(0));
    stageA(IC(2), IC(1), IC(24576), IC(0));
    SIG(17);
    SERVE(own_rd, 5);
    stageA(IC(3), IC(0), IC(114688), IC(0));
    stageA(IC(3), IC(1), IC(122880), IC(0));
    SIG(18);
    stageA(IC(4), IC(0), IC(32768), IC(0));
    stageA(IC(4), IC(1), IC(40960), IC(0));
    SIG(19);
    SERVE(cp_rd, 9);
    stageA(IC(5), IC(0), IC(0), IC(0));
    SERVE(cp_rd, 10);
    stageA(IC(5), IC(1), IC(8192), IC(0));
    SIG(20);
    SERVE(cp_rd, 11);
    stageW(IC(12), IC(98304));
    stageA(IC(6), IC(0), IC(131072), IC(0));
    stageA(IC(7), IC(0), IC(139264), IC(0));
    SIG(21);
    SERVE(cp_rd, 12);
    stageW(IC(13), IC(16384));
    stageN(IC(6), IC(49152), abH + dl);
    stageN(IC(8), IC(57344), abA + dl);
    SIG(22);
    SERVE(cp_rd, 13);
    stageN(IC(10), IC(114688), abA + dl);
    stageN(IC(12), IC(122880), abA + dl);
    stageW(IC(14), IC(65536));
    SIG(23);
    SERVE(cp_rd, 14);
    stageW(IC(15), IC(32768));
    stageN(IC(14), IC(81920), abA + dl);
    stageN(IC(8), IC(90112), abA + (dl ^ 2048u));
    SIG(24);
    SERVE(cp_rd, 15);
    stageN(IC(10), IC(0), abA + (dl ^ 2048u));
    stageN(IC(12), IC(8192), abA + (dl ^ 2048u));
    SIG(25);
    SERVE(cp_rd, 16);
    stageN(IC(14), IC(131072), abA + (dl ^ 2048u));
    stageN(IC(8), IC(139264), abH + dl);
    SIG(26);
    SERVE(cp_rd, 17);
    stageN(IC(10), IC(49152), abH + dl);
    stageN(IC(12), IC(57344), abH + dl);
    SIG(27);
    stageN(IC(14), IC(98304), abH + dl);
    stageN(IC(6), IC(106496), abH + (dl ^ 2048u));
    SIG(28);
    SERVE(cp_rd, 18);
    stageN(IC(8), IC(16384), abH + (dl ^ 2048u));
    stageN(IC(10), IC(24576), abH + (dl ^ 2048u));
    SIG(29);
    stageN(IC(12), IC(114688), abH + (dl ^ 2048u));
    stageN(IC(14), IC(122880), abH + (dl ^ 2048u));
    SIG(64);
    WAIT_V(0);
  } else {
    const int sw = wid >> 1 & 1;
    __builtin_amdgcn_s_setprio(2);
    unsigned tag;
    int idx = 0;
    auto TAG_PUT = [&]() {
      unsigned es;
      asm volatile("s_waitcnt vmcnt(0)\n\tv_readfirstlane_b32 %0, %1" : "=s"(es) : "v"(E) : "memory");
      asm volatile("ds_write2_b32 %0, %1, %2 offset1:1" ::"v"(tg_rd), "v"(1u), "v"(es * 0x9E3779B1u + 0x7F4A7C15u) : "memory");
    };
    auto ROUTER = [&]() {
      const unsigned ra = sb + ((h * 4 + cp1) * 16 + fr) * 128 + ((fq ^ (fr & 7)) << 4), rb = sb + RING + L_RF + (fr & 1) * 1152 + fq * 16;
      f32x4 d;
      SERVE(rf_rd, 1);
#define RSTEP(s, o) do { bf16x8 a0, a1, b0, b1; SERVE(la_rd, (s) + 1);                                                                       \
      asm volatile("ds_read_b128 %0, %4\n\tds_read_b128 %1, %5\n\tds_read_b128 %2, %6\n\tds_read_b128 %3, %6 offset:64\n\ts_waitcnt lgkmcnt(0)"  \
                   : "=&v"(a0), "=&v"(a1), "=&v"(b0), "=&v"(b1) : "v"(ra + (o)), "v"((ra ^ 64u) + (o)), "v"(rb + (((s) + rot1) & 7u) * 128u) : "memory"); \
      sig<(s) == 7 ? 64 : (s) + 1>(lbase + RING + L_LF + 768, tab); if ((s) == 0) mm0(d, a0, b0); else mm(d, a0, b0); mm(d, a1, b1); } while (0)
      RSTEP(0, 0u);
      RSTEP(1, 16384u);
      RSTEP(2, 32768u);
      RSTEP(3, 49152u);
      RSTEP(4, 65536u);
      RSTEP(5, 0u);
      RSTEP(6, 16384u);
      RSTEP(7, 32768u);
      asm volatile("s_nop 7" : "+v"(d));
      const int rid = si * 8 + h * 4 + cp1;
      unsigned key = 0u;
      float g[4];
#pragma unroll
      for (int i = 0; i < 4; ++i) {
        const float r = rbf(d[i]);
        g[i] = sigmoidf_(r);
        const unsigned u = __float_as_uint(r);
        const unsigned uo = (u & 0x80000000u) ? ~u : (u | 0x80000000u);
        key = max(key, (uo & 0xffff0000u) | (unsigned)(4095 - (rid * 16 + fq * 4 + i)));
      }
      key = dpp<0xa0>(key);
      const auto k1 = __builtin_amdgcn_permlane16_swap(key, key, false, false);
      key = max(k1[0], k1[1]);
      const auto k2 = __builtin_amdgcn_permlane32_swap(key, key, false, false);
      key = max(k2[0], k2[1]);
      if ((fr | 2) == 3) gst128_sc1((unsigned)(C_KEY + rid * 128 + (fr >> 1) * 64 + fq * 16), u32x4{pk2(g[0], g[1]), pk2(g[2], g[3]), key, tag}, wc);
    };
    auto POLL_KEYS = [&]() {
      u32x2 k0, k1;
      u32x4 gv;
#pragma clang loop unroll(disable)
      for (int it = 0; it < SPIN_MAX; ++it) {
        asm volatile("global_load_dwordx2 %0, %3, %6 sc1\n\tglobal_load_dwordx2 %1, %4, %6 sc1\n\tglobal_load_dwordx4 %2, %5, %6 sc1\n\ts_waitcnt vmcnt(0)"
                     : "=&v"(k0), "=&v"(k1), "=&v"(gv) : "v"((unsigned)(C_KEY + lane * 128 + 8)), "v"((unsigned)(C_KEY + lane * 128 + 8200)), "v"(g_o), "s"(wc) : "memory");
        if (__all(k0.y == tag && k1.y == tag && gv.w == tag)) break;
        __builtin_amdgcn_s_sleep(PSLEEP);
      }
      const unsigned best = wave_max(max(k0.x, k1.x));
      G_PUT(gv, best);
      if (b == 8 && lane < 8) gst128_sc1((unsigned)(C_BEST + lane * 16), u32x4{best, tag, best, tag}, wc);
    };
    auto IDX_READ = [&]() { idx = (4095 - (int)(__builtin_amdgcn_readfirstlane(lflag[3]) & 0xfffu)) & (S_ - 1); };
    const int on = 111 - qi;
    const bool ffn = on >= 0;
    const int fo = ffn ? qi >> 2 : 0, fp = qi & 3;
    u32x4 fg[16];
    const u16* wgu = sw ? w_up : w_gate;
    const unsigned vg = (unsigned)((fp * 128 + r8 * 8) * (F_ * 2) + fo * 128 + c8 * 16);
#define LOAD4(d, v, step, base, on)                                                                                        \
  asm volatile("s_cmp_lt_i32 %9, 0\n\ts_cbranch_scc1 .Lq%=\n\tglobal_load_dwordx4 %0, %4, %8\n\tglobal_load_dwordx4 %1, %5, %8\n\t" \
               "global_load_dwordx4 %2, %6, %8\n\tglobal_load_dwordx4 %3, %7, %8\n.Lq%=:"                                   \
               : "+v"((d)[0]), "+v"((d)[1]), "+v"((d)[2]), "+v"((d)[3])                                                    \
               : "v"(v), "v"((v) + (step)), "v"((v) + 2u * (step)), "v"((v) + 3u * (step)), "s"(base), "s"(on) : "scc", "memory")
#define ANY8(w) asm volatile("" : "=v"((w)[0]), "=v"((w)[1]), "=v"((w)[2]), "=v"((w)[3]), "=v"((w)[4]), "=v"((w)[5]), "=v"((w)[6]), "=v"((w)[7]))
    ANY8(fg);
    ANY8(fg + 8);
#define FFN_LOAD(p) LOAD4(fg + (p) * 4, vg + (((p) & 1) * 4u + ((p) >> 1) * 64u) * (F_ * 2), (unsigned)(F_ * 2), wgu, on)
    auto turn = [&](u32x4* w) {
      u32x4 t[8];
#pragma unroll
      for (int d = 0; d < 4; ++d)
#pragma unroll
        for (int j = 0; j < 4; ++j) {
          t[2 * d][j] = __builtin_amdgcn_perm(w[2 * j + 1][d], w[2 * j][d], 0x05040100u);
          t[2 * d + 1][j] = __builtin_amdgcn_perm(w[2 * j + 1][d], w[2 * j][d], 0x07060302u);
        }
#pragma unroll
      for (int i = 0; i < 8; ++i) w[i] = t[i];
      asm volatile("" : "+v"(w[0]), "+v"(w[1]), "+v"(w[2]), "+v"(w[3]), "+v"(w[4]), "+v"(w[5]), "+v"(w[6]), "+v"(w[7]));
    };
#define PIN8(w) asm volatile("s_waitcnt vmcnt(0)" : "+v"((w)[0]), "+v"((w)[1]), "+v"((w)[2]), "+v"((w)[3]), "+v"((w)[4]), "+v"((w)[5]), "+v"((w)[6]), "+v"((w)[7])::"memory")
    unsigned pa_o = (unsigned)(C_PA + qi * 1024 + (sw * 64 + c8 * 8 + fq * 2) * 8);
    auto FFN_RUN = [&]() {
      bf16x8 xa_[2];
      asm volatile("global_load_dwordx4 %0, %2, %3\n\tglobal_load_dwordx4 %1, %2, %3 offset:128\n\ts_waitcnt vmcnt(0)"
                   : "=&v"(xa_[0]), "=&v"(xa_[1]) : "v"((unsigned)(idx * (D_ * 2) + (fp * 128 + (fq * 2 + (fr & 1)) * 8) * 2)), "s"(x) : "memory");
      f32x4 d[8];
#pragma unroll
      for (int j = 0; j < 8; ++j) mm0(d[j], xa_[0], __builtin_bit_cast(bf16x8, fg[j]));
#pragma unroll
      for (int j = 0; j < 8; ++j) mm(d[j], xa_[1], __builtin_bit_cast(bf16x8, fg[8 + j]));
      asm volatile("s_nop 7" : "+v"(d[0]), "+v"(d[1]), "+v"(d[2]), "+v"(d[3]), "+v"(d[4]), "+v"(d[5]), "+v"(d[6]), "+v"(d[7]));
      float dp[8];
#pragma unroll
      for (int j = 0; j < 8; ++j) dp[j] = fr < 8 ? d[j][0] : d[j][1];
      const float s0 = dpp_add<0x128>(lane < 32 ? (fq & 1 ? dp[2] : dp[0]) : (fq & 1 ? dp[6] : dp[4]));
      const float s1 = dpp_add<0x128>(lane < 32 ? (fq & 1 ? dp[3] : dp[1]) : (fq & 1 ? dp[7] : dp[5]));
      const u32x4 o = {__float_as_uint(s0), tag, __float_as_uint(s1), tag};
      SERVE(gt_rd, 1);
      if ((lane & 8) == 0)
        gst128_sc1(pa_o, o, wc);
    };
    u32x4 fd[16];
    const unsigned vd = (unsigned)((fo * 64 + fq * 16) * (D_ * 2) + fp * 256 + fr * 16);
    ANY8(fd);
    ANY8(fd + 8);
#define DOWN_LOAD(p) LOAD4(fd + (p) * 4, vd + (p) * 4096u, 1024u, w_down, on | (sw - 1))
    unsigned pb_o = (unsigned)(C_PB + fo * 4096 + (fp * 128 + fr * 8 + fq * 2) * 8), pvo = (unsigned)(C_PA + fo * 4096 + lane * 16);
    auto PREP_F = [&]() { PIN8(fg); PIN8(fg + 8); turn(fg); turn(fg + 8); asm volatile("" : "+v"(pa_o)); };
    auto PREP_D = [&]() { PIN8(fd); PIN8(fd + 8); turn(fd); turn(fd + 8); asm volatile("" : "+v"(pb_o), "+v"(pvo)); };
    auto SEEN = [&]() { if (lane < 8) gst128_sc1((unsigned)(C_SEEN + (qi - 112) * 128 + lane * 16), u32x4{tag, tag, tag, tag}, wc); };
    auto RED_AB = [&]() {
      u32x4 pv[4];
#pragma clang loop unroll(disable)
      for (int it = 0; it < SPIN_MAX; ++it) {
        asm volatile("global_load_dwordx4 %0, %4, %5 sc1\n\tglobal_load_dwordx4 %1, %4, %5 offset:1024 sc1\n\t"
                     "global_load_dwordx4 %2, %4, %5 offset:2048 sc1\n\tglobal_load_dwordx4 %3, %4, %5 offset:3072 sc1\n\ts_waitcnt vmcnt(0)"
                     : "=&v"(pv[0]), "=&v"(pv[1]), "=&v"(pv[2]), "=&v"(pv[3]) : "v"(pvo), "s"(wc) : "memory");
        bool ok = true;
#pragma unroll
        for (int k = 0; k < 4; ++k) ok = ok && pv[k].y == tag && pv[k].w == tag;
        if (__all(ok)) break;
        __builtin_amdgcn_s_sleep(PSLEEP);
      }
      float s0 = 0.f, s1 = 0.f;
#pragma unroll
      for (int k = 0; k < 4; ++k) { s0 += __uint_as_float(pv[k].x); s1 += __uint_as_float(pv[k].z); }
      const auto r = __builtin_amdgcn_permlane32_swap(__float_as_uint(s0), __float_as_uint(s1), false, false);
      const float gt = rbf(__uint_as_float(r[0]));
      const unsigned hb = f2bf(rbf(gt * sigmoidf_(gt)) * rbf(__uint_as_float(r[1])));
      asm volatile("ds_write_b16 %0, %1" ::"v"(sb + RING + L_H + (lane & 31) * 4 + (lane >> 5) * 2), "v"(hb) : "memory");
    };
    auto RED_C = [&]() {
      bf16x8 hp[2];
      asm volatile("ds_read_b128 %0, %2\n\tds_read_b128 %1, %2 offset:16\n\ts_waitcnt lgkmcnt(0)" : "=&v"(hp[0]), "=&v"(hp[1]) : "v"(sb + RING + L_H + fq * 32) : "memory");
      f32x4 d[8];
#pragma unroll
      for (int j = 0; j < 8; ++j) mm0(d[j], hp[0], __builtin_bit_cast(bf16x8, fd[j]));
#pragma unroll
      for (int j = 0; j < 8; ++j) mm(d[j], hp[1], __builtin_bit_cast(bf16x8, fd[8 + j]));
      asm volatile("s_nop 7" : "+v"(d[0]), "+v"(d[1]), "+v"(d[2]), "+v"(d[3]), "+v"(d[4]), "+v"(d[5]), "+v"(d[6]), "+v"(d[7]));
      const float dp[8] = {d[0][0], d[1][0], d[2][0], d[3][0], d[4][0], d[5][0], d[6][0], d[7][0]};
      const float v0 = lane < 32 ? (fq & 1 ? dp[2] : dp[0]) : (fq & 1 ? dp[6] : dp[4]);
      const float v1 = lane < 32 ? (fq & 1 ? dp[3] : dp[1]) : (fq & 1 ? dp[7] : dp[5]);
      gst128_sc1(pb_o, u32x4{__float_as_uint(v0), tag, __float_as_uint(v1), tag}, wc);
    };
    if (sw) TAG_PUT();
    if (isp) {
      if (sw) TOUR();
      TAG_WAIT();
      if (sw == 0) ROUTER();
    SERVE(lb_rd, 16);
      DOWN_LOAD(0);
      DOWN_LOAD(1);
      DOWN_LOAD(2);
      DOWN_LOAD(3);
      if (sw) PREP_D();
    SERVE(cp_rd, 14);
    UNTIL(855u, 17991u);
      if (ffn && sw) RED_AB();
      if (ffn && sw) RED_C();
    }
    if (isq) {
      TAG_WAIT();
      if (!ffn && sw) SEEN();
      if (sw == 0) sig<64>(lbase + RING + L_LF + 768, tab);
    SERVE(la_rd, 11);
      FFN_LOAD(0);
      FFN_LOAD(1);
      FFN_LOAD(2);
      FFN_LOAD(3);
    SERVE(own_rd, 5);
    UNTIL(574u, 11890u);
      if (sw == 0) POLL_KEYS();
      PREP_F();
      SERVE(best_rd, 1);
      IDX_READ();
      if (ffn) FFN_RUN();
    }
  }
  } else {
    const int wm = wid >> 1, wn = wid & 1;
    unsigned fl;
    unsigned flagged = (unsigned)(ej & 1) * 64u;
    const unsigned flag_o = (unsigned)(C_FL2 + (((h * 4 + (cp1 & 2) + wn) * 2 + kh) * 32 + ((si & 7) * 2 + (cp1 & 1)) * 2 + wm) * FSTR + lane * 4);
    unsigned tagv;
#define TRYFLAG(Y) tryflag<Y>(flagged, flag_o, tagv, wc)
#define FLAG(Y) do { WAIT_V(Y); TRYFLAG(Y); } while (0)
    const unsigned own_wr = sb + RING + L_FLAG + 32 + wid * 4;
#define OWN_SIG(n) do { unsigned t_; asm volatile("v_mov_b32 %0, %2\n\tds_write_b32 %1, %0" : "=&v"(t_) : "v"(own_wr), "i"(n) : "memory"); } while (0)
#define SPIN(ADDR, N) spin<N>(ADDR, bud)
#define SPINF(ADDR, N, Y) spinf<N, Y>(ADDR, bud, flagged, flag_o, tagv, wc)
    const unsigned wa0 = sb + (wid * 32 + fr) * 128 + ((fq ^ (fr & 7)) << 4), wa1 = wa0 ^ 64u;
    const unsigned xa = sb + lane * 16;
    const unsigned wa0_0 = wa0 + 0u;
    const unsigned wa0_1 = wa0 + 65536u;
    const unsigned wa0_2 = wa0 + 131072u;
    const unsigned wa1_0 = wa1 + 0u;
    const unsigned wa1_1 = wa1 + 65536u;
    const unsigned wa1_2 = wa1 + 131072u;
    const unsigned xa_0 = xa + 0u;
    const unsigned xa_1 = xa + 65536u;
    const unsigned xa_2 = xa + 131072u;
    bf16x8 wr[16][4], fb[9];
    asm volatile(".subsection 1\n.Lfk_tour:\n\ts_branch 7777f\n\t.subsection 0");
    if (isp) {
      const int row = wm * 64 + fr, xr = wn * 32 + fr;
      const unsigned pa0 = sb + row * 128 + ((fq ^ (row & 7)) << 4) + wn * 4096, pa1 = pa0 ^ 64u;
      const unsigned pb0 = sb + row * 128 + ((fq ^ (row & 7)) << 4) + (1 - wn) * 4096, pb1 = pb0 ^ 64u;
      const unsigned px0 = sb + xr * 128 + ((fq ^ (xr & 7)) << 4), px1 = px0 ^ 64u;
      f32x4 ac1[4][4];
      bf16x8 hw[2][4], hx[2][4];
      unsigned go[2];
      go[0] = (unsigned)((((h * 4 + (cp1 & 2) + wn) * 2 + (cp1 & 1)) * 16 + si) * A_B + wm * 8192 + wn * 4096 + (fq & 1) * 1024 + (fq >> 1) * 256 + fr * 16);
      go[1] = go[0] ^ 4096u;
#define P1_SETUP() asm volatile("" : "+v"(go[0]), "+v"(go[1]))
      unsigned pv[4];
#define PUB0(n, mi) asm volatile("v_cvt_pk_bf16_f32 %0, %2, %3\n\tv_cvt_pk_bf16_f32 %1, %4, %5" : "=&v"(pv[0]), "=&v"(pv[2])             \
                                 : "v"(ac1[n][mi][0]), "v"(ac1[n][mi][1]), "v"(ac1[(n) + 1][mi][0]), "v"(ac1[(n) + 1][mi][1]))
#define PUB1(n, mi) asm volatile("v_cvt_pk_bf16_f32 %0, %2, %3\n\tv_cvt_pk_bf16_f32 %1, %4, %5" : "=&v"(pv[1]), "=&v"(pv[3])             \
                                 : "v"(ac1[n][mi][2]), "v"(ac1[n][mi][3]), "v"(ac1[(n) + 1][mi][2]), "v"(ac1[(n) + 1][mi][3]))
#define PUB2() asm volatile("s_nop 1\n\tv_permlane16_swap_b32 %0, %2\n\tv_permlane16_swap_b32 %1, %3" : "+v"(pv[0]), "+v"(pv[1]), "+v"(pv[2]), "+v"(pv[3]))
#define PUB3(n, mi) asm volatile("global_store_dwordx4 %0, %1, %2 offset:%3 sc1\n\ts_nop 1" ::"v"(go[(mi) >> 1]), "v"(u32x4{pv[0], pv[1], pv[2], pv[3]}), "s"(ab), "i"(((mi) & 1) * 512 + (n) * 1024) : "memory")
      const unsigned pa0_0 = pa0 + 0u;
      const unsigned pa0_1 = pa0 + 65536u;
      const unsigned pa1_0 = pa1 + 0u;
      const unsigned pa1_1 = pa1 + 65536u;
      const unsigned pb0_0 = pb0 + 0u;
      const unsigned pb0_1 = pb0 + 65536u;
      const unsigned pb1_0 = pb1 + 0u;
      const unsigned pb1_1 = pb1 + 65536u;
      const unsigned px0_1 = px0 + 65536u;
      const unsigned px0_2 = px0 + 131072u;
      const unsigned px1_1 = px1 + 65536u;
      const unsigned px1_2 = px1 + 131072u;
      SPIN(lf_rd, 1); SB();
      lds_rd128<16384>(hx[0][0], px0_1); SB();
      lds_rd128<0>(hw[0][0], pa0_0); SB();
      lds_rd128<2048>(hw[0][1], pa0_0); SB();
      lds_rd128<0>(hw[0][2], pb0_0); SB();
      lds_rd128<2048>(hw[0][3], pb0_0); SB();
      lds_rd128<18432>(hx[0][1], px0_1); SB();
      lds_rd128<24576>(hx[0][2], px0_1); SB();
      lds_rd128<26624>(hx[0][3], px0_1); SB();
      mm0w<6>(ac1[0][0], hw[0][0], hx[0][0]); SB();
      lds_rd128<16384>(hx[1][0], px1_1); SB();
      mm0w<6>(ac1[0][1], hw[0][1], hx[0][0]); SB();
      lds_rd128<0>(hw[1][0], pa1_0); SB();
      mm0w<6>(ac1[0][2], hw[0][2], hx[0][0]); SB();
      lds_rd128<2048>(hw[1][1], pa1_0); SB();
      mm0w<6>(ac1[0][3], hw[0][3], hx[0][0]); SB();
      lds_rd128<0>(hw[1][2], pb1_0); SB();
      mm0w<6>(ac1[1][3], hw[0][3], hx[0][1]); SB();
      lds_rd128<2048>(hw[1][3], pb1_0); SB();
      mm0(ac1[1][2], hw[0][2], hx[0][1]); SB();
      lds_rd128<18432>(hx[1][1], px1_1); SB();
      mm0(ac1[1][1], hw[0][1], hx[0][1]); SB();
      lds_rd128<24576>(hx[1][2], px1_1); SB();
      mm0(ac1[1][0], hw[0][0], hx[0][1]); SB();
      lds_rd128<26624>(hx[1][3], px1_1); SB();
      mm0w<8>(ac1[2][0], hw[0][0], hx[0][2]); SB();
      mm0(ac1[2][1], hw[0][1], hx[0][2]); SB();
      mm0(ac1[2][2], hw[0][2], hx[0][2]); SB();
      mm0(ac1[2][3], hw[0][3], hx[0][2]); SB();
      mm0(ac1[3][3], hw[0][3], hx[0][3]); SB();
      mm0(ac1[3][2], hw[0][2], hx[0][3]); SB();
      asm volatile("s_waitcnt lgkmcnt(0)" : "+v"(hx[1][0]), "+v"(hw[1][0]), "+v"(hw[1][1]), "+v"(hw[1][2]), "+v"(hw[1][3]), "+v"(hx[1][1]), "+v"(hx[1][2]), "+v"(hx[1][3])::"memory"); SB();
      SIG(1); SB();
      mm0(ac1[3][1], hw[0][1], hx[0][3]); SB();
      mm0(ac1[3][0], hw[0][0], hx[0][3]); SB();
      mm(ac1[0][0], hw[1][0], hx[1][0]); SB();
      lds_rd32(fl, lf_rd); SB();
      mm(ac1[0][1], hw[1][1], hx[1][0]); SB();
      mm(ac1[0][2], hw[1][2], hx[1][0]); SB();
      mm(ac1[0][3], hw[1][3], hx[1][0]); SB();
      mm(ac1[1][3], hw[1][3], hx[1][1]); SB();
      mm(ac1[1][2], hw[1][2], hx[1][1]); SB();
      mmchkw<2, 0>(ac1[1][1], hw[1][1], hx[1][1], fl, lf_rd, bud); SB();
      lds_rd128<32768>(hx[0][0], px0_1); SB();
      mm(ac1[1][0], hw[1][0], hx[1][1]); SB();
      lds_rd128<16384>(hw[0][0], pa0_0); SB();
      mm(ac1[2][0], hw[1][0], hx[1][2]); SB();
      lds_rd128<18432>(hw[0][1], pa0_0); SB();
      mm(ac1[2][1], hw[1][1], hx[1][2]); SB();
      lds_rd128<16384>(hw[0][2], pb0_0); SB();
      mm(ac1[2][2], hw[1][2], hx[1][2]); SB();
      lds_rd128<18432>(hw[0][3], pb0_0); SB();
      mm(ac1[2][3], hw[1][3], hx[1][2]); SB();
      lds_rd128<34816>(hx[0][1], px0_1); SB();
      mm(ac1[3][3], hw[1][3], hx[1][3]); SB();
      lds_rd128<40960>(hx[0][2], px0_1); SB();
      mm(ac1[3][2], hw[1][2], hx[1][3]); SB();
      lds_rd128<43008>(hx[0][3], px0_1); SB();
      mm(ac1[3][1], hw[1][1], hx[1][3]); SB();
      mm(ac1[3][0], hw[1][0], hx[1][3]); SB();
      mmw<5>(ac1[0][0], hw[0][0], hx[0][0]); SB();
      lds_rd128<32768>(hx[1][0], px1_1); SB();
      mm(ac1[0][1], hw[0][1], hx[0][0]); SB();
      lds_rd128<16384>(hw[1][0], pa1_0); SB();
      mmw<5>(ac1[0][2], hw[0][2], hx[0][0]); SB();
      lds_rd128<18432>(hw[1][1], pa1_0); SB();
      mm(ac1[0][3], hw[0][3], hx[0][0]); SB();
      lds_rd128<16384>(hw[1][2], pb1_0); SB();
      mmw<5>(ac1[1][3], hw[0][3], hx[0][1]); SB();
      lds_rd128<18432>(hw[1][3], pb1_0); SB();
      mm(ac1[1][2], hw[0][2], hx[0][1]); SB();
      lds_rd128<34816>(hx[1][1], px1_1); SB();
      mm(ac1[1][1], hw[0][1], hx[0][1]); SB();
      lds_rd128<40960>(hx[1][2], px1_1); SB();
      mm(ac1[1][0], hw[0][0], hx[0][1]); SB();
      lds_rd128<43008>(hx[1][3], px1_1); SB();
      mm(ac1[2][0], hw[0][0], hx[0][2]); SB();
      mm(ac1[2][1], hw[0][1], hx[0][2]); SB();
      mm(ac1[2][2], hw[0][2], hx[0][2]); SB();
      mm(ac1[2][3], hw[0][3], hx[0][2]); SB();
      mmw<7>(ac1[3][3], hw[0][3], hx[0][3]); SB();
      mm(ac1[3][2], hw[0][2], hx[0][3]); SB();
      asm volatile("s_waitcnt lgkmcnt(0)" : "+v"(hw[1][0]), "+v"(hw[1][1]), "+v"(hw[1][2]), "+v"(hw[1][3]), "+v"(hx[1][1]), "+v"(hx[1][2]), "+v"(hx[1][3])::"memory"); SB();
      SIG(2); SB();
      mm(ac1[3][1], hw[0][1], hx[0][3]); SB();
      mm(ac1[3][0], hw[0][0], hx[0][3]); SB();
      mm(ac1[0][0], hw[1][0], hx[1][0]); SB();
      lds_rd32(fl, lf_rd); SB();
      mm(ac1[0][1], hw[1][1], hx[1][0]); SB();
      mm(ac1[0][2], hw[1][2], hx[1][0]); SB();
      mm(ac1[0][3], hw[1][3], hx[1][0]); SB();
      mm(ac1[1][3], hw[1][3], hx[1][1]); SB();
      mm(ac1[1][2], hw[1][2], hx[1][1]); SB();
      mmchkw<3, 0>(ac1[1][1], hw[1][1], hx[1][1], fl, lf_rd, bud); SB();
      lds_rd128<49152>(hx[0][0], px0_1); SB();
      mm(ac1[1][0], hw[1][0], hx[1][1]); SB();
      lds_rd128<32768>(hw[0][0], pa0_0); SB();
      mm(ac1[2][0], hw[1][0], hx[1][2]); SB();
      lds_rd128<34816>(hw[0][1], pa0_0); SB();
      mm(ac1[2][1], hw[1][1], hx[1][2]); SB();
      lds_rd128<32768>(hw[0][2], pb0_0); SB();
      mm(ac1[2][2], hw[1][2], hx[1][2]); SB();
      lds_rd128<34816>(hw[0][3], pb0_0); SB();
      mm(ac1[2][3], hw[1][3], hx[1][2]); SB();
      lds_rd128<51200>(hx[0][1], px0_1); SB();
      mm(ac1[3][3], hw[1][3], hx[1][3]); SB();
      lds_rd128<57344>(hx[0][2], px0_1); SB();
      mm(ac1[3][2], hw[1][2], hx[1][3]); SB();
      lds_rd128<59392>(hx[0][3], px0_1); SB();
      mm(ac1[3][1], hw[1][1], hx[1][3]); SB();
      mm(ac1[3][0], hw[1][0], hx[1][3]); SB();
      mmw<5>(ac1[0][0], hw[0][0], hx[0][0]); SB();
      lds_rd128<49152>(hx[1][0], px1_1); SB();
      mm(ac1[0][1], hw[0][1], hx[0][0]); SB();
      lds_rd128<32768>(hw[1][0], pa1_0); SB();
      mmw<5>(ac1[0][2], hw[0][2], hx[0][0]); SB();
      lds_rd128<34816>(hw[1][1], pa1_0); SB();
      mm(ac1[0][3], hw[0][3], hx[0][0]); SB();
      lds_rd128<32768>(hw[1][2], pb1_0); SB();
      mmw<5>(ac1[1][3], hw[0][3], hx[0][1]); SB();
      lds_rd128<34816>(hw[1][3], pb1_0); SB();
      mm(ac1[1][2], hw[0][2], hx[0][1]); SB();
      lds_rd128<51200>(hx[1][1], px1_1); SB();
      mm(ac1[1][1], hw[0][1], hx[0][1]); SB();
      lds_rd128<57344>(hx[1][2], px1_1); SB();
      mm(ac1[1][0], hw[0][0], hx[0][1]); SB();
      lds_rd128<59392>(hx[1][3], px1_1); SB();
      mm(ac1[2][0], hw[0][0], hx[0][2]); SB();
      mm(ac1[2][1], hw[0][1], hx[0][2]); SB();
      mm(ac1[2][2], hw[0][2], hx[0][2]); SB();
      mm(ac1[2][3], hw[0][3], hx[0][2]); SB();
      mmw<7>(ac1[3][3], hw[0][3], hx[0][3]); SB();
      mm(ac1[3][2], hw[0][2], hx[0][3]); SB();
      asm volatile("s_waitcnt lgkmcnt(0)" : "+v"(hw[1][0]), "+v"(hw[1][1]), "+v"(hw[1][2]), "+v"(hw[1][3]), "+v"(hx[1][1]), "+v"(hx[1][2]), "+v"(hx[1][3])::"memory"); SB();
      SIG(3); SB();
      mm(ac1[3][1], hw[0][1], hx[0][3]); SB();
      mm(ac1[3][0], hw[0][0], hx[0][3]); SB();
      mm(ac1[0][0], hw[1][0], hx[1][0]); SB();
      lds_rd32(fl, lf_rd); SB();
      mm(ac1[0][1], hw[1][1], hx[1][0]); SB();
      mm(ac1[0][2], hw[1][2], hx[1][0]); SB();
      mm(ac1[0][3], hw[1][3], hx[1][0]); SB();
      mm(ac1[1][3], hw[1][3], hx[1][1]); SB();
      mm(ac1[1][2], hw[1][2], hx[1][1]); SB();
      mmchkw<4, 0>(ac1[1][1], hw[1][1], hx[1][1], fl, lf_rd, bud); SB();
      lds_rd128<0>(hx[0][0], px0_2); SB();
      mm(ac1[1][0], hw[1][0], hx[1][1]); SB();
      lds_rd128<49152>(hw[0][0], pa0_0); SB();
      mm(ac1[2][0], hw[1][0], hx[1][2]); SB();
      asm volatile(STOP ::: "memory"); SB();
      lds_rd128<51200>(hw[0][1], pa0_0); SB();
      mm(ac1[2][1], hw[1][1], hx[1][2]); SB();
      lds_rd128<49152>(hw[0][2], pb0_0); SB();
      mm(ac1[2][2], hw[1][2], hx[1][2]); SB();
      lds_rd128<51200>(hw[0][3], pb0_0); SB();
      mm(ac1[2][3], hw[1][3], hx[1][2]); SB();
      lds_rd128<2048>(hx[0][1], px0_2); SB();
      mm(ac1[3][3], hw[1][3], hx[1][3]); SB();
      lds_rd128<8192>(hx[0][2], px0_2); SB();
      mm(ac1[3][2], hw[1][2], hx[1][3]); SB();
      lds_rd128<10240>(hx[0][3], px0_2); SB();
      mm(ac1[3][1], hw[1][1], hx[1][3]); SB();
      mm(ac1[3][0], hw[1][0], hx[1][3]); SB();
      mmw<5>(ac1[0][0], hw[0][0], hx[0][0]); SB();
      lds_rd128<0>(hx[1][0], px1_2); SB();
      mm(ac1[0][1], hw[0][1], hx[0][0]); SB();
      lds_rd128<49152>(hw[1][0], pa1_0); SB();
      mmw<5>(ac1[0][2], hw[0][2], hx[0][0]); SB();
      lds_rd128<51200>(hw[1][1], pa1_0); SB();
      mm(ac1[0][3], hw[0][3], hx[0][0]); SB();
      lds_rd128<49152>(hw[1][2], pb1_0); SB();
      mmw<5>(ac1[1][3], hw[0][3], hx[0][1]); SB();
      lds_rd128<51200>(hw[1][3], pb1_0); SB();
      mm(ac1[1][2], hw[0][2], hx[0][1]); SB();
      lds_rd128<2048>(hx[1][1], px1_2); SB();
      mm(ac1[1][1], hw[0][1], hx[0][1]); SB();
      lds_rd128<8192>(hx[1][2], px1_2); SB();
      mm(ac1[1][0], hw[0][0], hx[0][1]); SB();
      lds_rd128<10240>(hx[1][3], px1_2); SB();
      mm(ac1[2][0], hw[0][0], hx[0][2]); SB();
      mm(ac1[2][1], hw[0][1], hx[0][2]); SB();
      mm(ac1[2][2], hw[0][2], hx[0][2]); SB();
      mm(ac1[2][3], hw[0][3], hx[0][2]); SB();
      mmw<7>(ac1[3][3], hw[0][3], hx[0][3]); SB();
      mm(ac1[3][2], hw[0][2], hx[0][3]); SB();
      asm volatile("s_waitcnt lgkmcnt(0)" : "+v"(hw[1][0]), "+v"(hw[1][1]), "+v"(hw[1][2]), "+v"(hw[1][3]), "+v"(hx[1][1]), "+v"(hx[1][2]), "+v"(hx[1][3])::"memory"); SB();
      SIG(4); SB();
      mm(ac1[3][1], hw[0][1], hx[0][3]); SB();
      mm(ac1[3][0], hw[0][0], hx[0][3]); SB();
      mm(ac1[0][0], hw[1][0], hx[1][0]); SB();
      lds_rd32(fl, lf_rd); SB();
      mm(ac1[0][1], hw[1][1], hx[1][0]); SB();
      mm(ac1[0][2], hw[1][2], hx[1][0]); SB();
      mm(ac1[0][3], hw[1][3], hx[1][0]); SB();
      mm(ac1[1][3], hw[1][3], hx[1][1]); SB();
      mm(ac1[1][2], hw[1][2], hx[1][1]); SB();
      mmchkw<5, 0>(ac1[1][1], hw[1][1], hx[1][1], fl, lf_rd, bud); SB();
      lds_rd128<16384>(hx[0][0], px0_1); SB();
      mm(ac1[1][0], hw[1][0], hx[1][1]); SB();
      lds_rd128<0>(hw[0][0], pa0_1); SB();
      mm(ac1[2][0], hw[1][0], hx[1][2]); SB();
      lds_rd128<2048>(hw[0][1], pa0_1); SB();
      mm(ac1[2][1], hw[1][1], hx[1][2]); SB();
      lds_rd128<0>(hw[0][2], pb0_1); SB();
      mm(ac1[2][2], hw[1][2], hx[1][2]); SB();
      lds_rd128<2048>(hw[0][3], pb0_1); SB();
      mm(ac1[2][3], hw[1][3], hx[1][2]); SB();
      lds_rd128<18432>(hx[0][1], px0_1); SB();
      mm(ac1[3][3], hw[1][3], hx[1][3]); SB();
      lds_rd128<24576>(hx[0][2], px0_1); SB();
      mm(ac1[3][2], hw[1][2], hx[1][3]); SB();
      lds_rd128<26624>(hx[0][3], px0_1); SB();
      mm(ac1[3][1], hw[1][1], hx[1][3]); SB();
      mm(ac1[3][0], hw[1][0], hx[1][3]); SB();
      mmw<5>(ac1[0][0], hw[0][0], hx[0][0]); SB();
      lds_rd128<16384>(hx[1][0], px1_1); SB();
      mm(ac1[0][1], hw[0][1], hx[0][0]); SB();
      lds_rd128<0>(hw[1][0], pa1_1); SB();
      mmw<5>(ac1[0][2], hw[0][2], hx[0][0]); SB();
      lds_rd128<2048>(hw[1][1], pa1_1); SB();
      mm(ac1[0][3], hw[0][3], hx[0][0]); SB();
      lds_rd128<0>(hw[1][2], pb1_1); SB();
      mmw<5>(ac1[1][3], hw[0][3], hx[0][1]); SB();
      lds_rd128<2048>(hw[1][3], pb1_1); SB();
      mm(ac1[1][2], hw[0][2], hx[0][1]); SB();
      lds_rd128<18432>(hx[1][1], px1_1); SB();
      mm(ac1[1][1], hw[0][1], hx[0][1]); SB();
      lds_rd128<24576>(hx[1][2], px1_1); SB();
      mm(ac1[1][0], hw[0][0], hx[0][1]); SB();
      lds_rd128<26624>(hx[1][3], px1_1); SB();
      mm(ac1[2][0], hw[0][0], hx[0][2]); SB();
      mm(ac1[2][1], hw[0][1], hx[0][2]); SB();
      mm(ac1[2][2], hw[0][2], hx[0][2]); SB();
      mm(ac1[2][3], hw[0][3], hx[0][2]); SB();
      mmw<7>(ac1[3][3], hw[0][3], hx[0][3]); SB();
      mm(ac1[3][2], hw[0][2], hx[0][3]); SB();
      asm volatile("s_waitcnt lgkmcnt(0)" : "+v"(hw[1][0]), "+v"(hw[1][1]), "+v"(hw[1][2]), "+v"(hw[1][3]), "+v"(hx[1][1]), "+v"(hx[1][2]), "+v"(hx[1][3])::"memory"); SB();
      SIG(5); SB();
      mm(ac1[3][1], hw[0][1], hx[0][3]); SB();
      mm(ac1[3][0], hw[0][0], hx[0][3]); SB();
      P1_SETUP(); SB();
      mm(ac1[0][0], hw[1][0], hx[1][0]); SB();
      lds_rd32(fl, lf_rd); SB();
      mm(ac1[0][1], hw[1][1], hx[1][0]); SB();
      mm(ac1[0][2], hw[1][2], hx[1][0]); SB();
      mm(ac1[0][3], hw[1][3], hx[1][0]); SB();
      mm(ac1[1][3], hw[1][3], hx[1][1]); SB();
      mm(ac1[1][2], hw[1][2], hx[1][1]); SB();
      mmchkw<6, 0>(ac1[1][1], hw[1][1], hx[1][1], fl, lf_rd, bud); SB();
      lds_rd128<32768>(hx[0][0], px0_1); SB();
      mm(ac1[1][0], hw[1][0], hx[1][1]); SB();
      lds_rd128<0>(hw[0][0], pa0_0); SB();
      mm(ac1[2][0], hw[1][0], hx[1][2]); SB();
      lds_rd128<2048>(hw[0][1], pa0_0); SB();
      mm(ac1[2][1], hw[1][1], hx[1][2]); SB();
      lds_rd128<0>(hw[0][2], pb0_0); SB();
      mm(ac1[2][2], hw[1][2], hx[1][2]); SB();
      lds_rd128<2048>(hw[0][3], pb0_0); SB();
      mm(ac1[2][3], hw[1][3], hx[1][2]); SB();
      lds_rd128<34816>(hx[0][1], px0_1); SB();
      mm(ac1[3][3], hw[1][3], hx[1][3]); SB();
      lds_rd128<40960>(hx[0][2], px0_1); SB();
      mm(ac1[3][2], hw[1][2], hx[1][3]); SB();
      lds_rd128<43008>(hx[0][3], px0_1); SB();
      mm(ac1[3][1], hw[1][1], hx[1][3]); SB();
      mm(ac1[3][0], hw[1][0], hx[1][3]); SB();
      mmw<5>(ac1[0][0], hw[0][0], hx[0][0]); SB();
      lds_rd128<32768>(hx[1][0], px1_1); SB();
      mm(ac1[0][1], hw[0][1], hx[0][0]); SB();
      lds_rd128<0>(hw[1][0], pa1_0); SB();
      mmw<5>(ac1[0][2], hw[0][2], hx[0][0]); SB();
      lds_rd128<2048>(hw[1][1], pa1_0); SB();
      mm(ac1[0][3], hw[0][3], hx[0][0]); SB();
      lds_rd128<0>(hw[1][2], pb1_0); SB();
      mmw<5>(ac1[1][3], hw[0][3], hx[0][1]); SB();
      lds_rd128<2048>(hw[1][3], pb1_0); SB();
      mm(ac1[1][2], hw[0][2], hx[0][1]); SB();
      lds_rd128<34816>(hx[1][1], px1_1); SB();
      mm(ac1[1][1], hw[0][1], hx[0][1]); SB();
      lds_rd128<40960>(hx[1][2], px1_1); SB();
      mm(ac1[1][0], hw[0][0], hx[0][1]); SB();
      lds_rd128<43008>(hx[1][3], px1_1); SB();
      mm(ac1[2][0], hw[0][0], hx[0][2]); SB();
      mm(ac1[2][1], hw[0][1], hx[0][2]); SB();
      mm(ac1[2][2], hw[0][2], hx[0][2]); SB();
      mm(ac1[2][3], hw[0][3], hx[0][2]); SB();
      mmw<7>(ac1[3][3], hw[0][3], hx[0][3]); SB();
      mm(ac1[3][2], hw[0][2], hx[0][3]); SB();
      asm volatile("s_waitcnt lgkmcnt(0)" : "+v"(hw[1][0]), "+v"(hw[1][1]), "+v"(hw[1][2]), "+v"(hw[1][3]), "+v"(hx[1][1]), "+v"(hx[1][2]), "+v"(hx[1][3])::"memory"); SB();
      SIG(6); SB();
      mm(ac1[3][1], hw[0][1], hx[0][3]); SB();
      mm(ac1[3][0], hw[0][0], hx[0][3]); SB();
      mm(ac1[0][0], hw[1][0], hx[1][0]); SB();
      lds_rd32(fl, lf_rd); SB();
      mm(ac1[0][1], hw[1][1], hx[1][0]); SB();
      mm(ac1[0][2], hw[1][2], hx[1][0]); SB();
      mm(ac1[0][3], hw[1][3], hx[1][0]); SB();
      mm(ac1[1][3], hw[1][3], hx[1][1]); SB();
      mm(ac1[1][2], hw[1][2], hx[1][1]); SB();
      mmchkw<7, 0>(ac1[1][1], hw[1][1], hx[1][1], fl, lf_rd, bud); SB();
      lds_rd128<49152>(hx[0][0], px0_1); SB();
      mm(ac1[1][0], hw[1][0], hx[1][1]); SB();
      lds_rd128<16384>(hw[0][0], pa0_0); SB();
      mm(ac1[2][0], hw[1][0], hx[1][2]); SB();
      lds_rd128<18432>(hw[0][1], pa0_0); SB();
      mm(ac1[2][1], hw[1][1], hx[1][2]); SB();
      lds_rd128<16384>(hw[0][2], pb0_0); SB();
      mm(ac1[2][2], hw[1][2], hx[1][2]); SB();
      lds_rd128<18432>(hw[0][3], pb0_0); SB();
      mm(ac1[2][3], hw[1][3], hx[1][2]); SB();
      lds_rd128<51200>(hx[0][1], px0_1); SB();
      mm(ac1[3][3], hw[1][3], hx[1][3]); SB();
      lds_rd128<57344>(hx[0][2], px0_1); SB();
      mm(ac1[3][2], hw[1][2], hx[1][3]); SB();
      lds_rd128<59392>(hx[0][3], px0_1); SB();
      mm(ac1[3][1], hw[1][1], hx[1][3]); SB();
      mm(ac1[3][0], hw[1][0], hx[1][3]); SB();
      mmw<5>(ac1[0][0], hw[0][0], hx[0][0]); SB();
      lds_rd128<49152>(hx[1][0], px1_1); SB();
      mm(ac1[0][1], hw[0][1], hx[0][0]); SB();
      lds_rd128<16384>(hw[1][0], pa1_0); SB();
      mmw<5>(ac1[0][2], hw[0][2], hx[0][0]); SB();
      lds_rd128<18432>(hw[1][1], pa1_0); SB();
      mm(ac1[0][3], hw[0][3], hx[0][0]); SB();
      lds_rd128<16384>(hw[1][2], pb1_0); SB();
      mmw<5>(ac1[1][3], hw[0][3], hx[0][1]); SB();
      lds_rd128<18432>(hw[1][3], pb1_0); SB();
      mm(ac1[1][2], hw[0][2], hx[0][1]); SB();
      lds_rd128<51200>(hx[1][1], px1_1); SB();
      mm(ac1[1][1], hw[0][1], hx[0][1]); SB();
      lds_rd128<57344>(hx[1][2], px1_1); SB();
      mm(ac1[1][0], hw[0][0], hx[0][1]); SB();
      lds_rd128<59392>(hx[1][3], px1_1); SB();
      mm(ac1[2][0], hw[0][0], hx[0][2]); SB();
      mm(ac1[2][1], hw[0][1], hx[0][2]); SB();
      mm(ac1[2][2], hw[0][2], hx[0][2]); SB();
      mm(ac1[2][3], hw[0][3], hx[0][2]); SB();
      mmw<7>(ac1[3][3], hw[0][3], hx[0][3]); SB();
      mm(ac1[3][2], hw[0][2], hx[0][3]); SB();
      asm volatile("s_waitcnt lgkmcnt(0)" : "+v"(hw[1][0]), "+v"(hw[1][1]), "+v"(hw[1][2]), "+v"(hw[1][3]), "+v"(hx[1][1]), "+v"(hx[1][2]), "+v"(hx[1][3])::"memory"); SB();
      SIG(7); SB();
      mm(ac1[3][1], hw[0][1], hx[0][3]); SB();
      mm(ac1[3][0], hw[0][0], hx[0][3]); SB();
      mm(ac1[0][0], hw[1][0], hx[1][0]); SB();
      lds_rd32(fl, lf_rd); SB();
      mm(ac1[0][1], hw[1][1], hx[1][0]); SB();
      mm(ac1[0][2], hw[1][2], hx[1][0]); SB();
      mm(ac1[0][3], hw[1][3], hx[1][0]); SB();
      mm(ac1[1][3], hw[1][3], hx[1][1]); SB();
      mm(ac1[1][2], hw[1][2], hx[1][1]); SB();
      mmchkw<8, 0>(ac1[1][1], hw[1][1], hx[1][1], fl, lf_rd, bud); SB();
      lds_rd128<0>(hx[0][0], px0_2); SB();
      mm(ac1[1][0], hw[1][0], hx[1][1]); SB();
      lds_rd128<32768>(hw[0][0], pa0_0); SB();
      mm(ac1[2][0], hw[1][0], hx[1][2]); SB();
      lds_rd128<34816>(hw[0][1], pa0_0); SB();
      mm(ac1[2][1], hw[1][1], hx[1][2]); SB();
      lds_rd128<32768>(hw[0][2], pb0_0); SB();
      mm(ac1[2][2], hw[1][2], hx[1][2]); SB();
      lds_rd128<34816>(hw[0][3], pb0_0); SB();
      mm(ac1[2][3], hw[1][3], hx[1][2]); SB();
      lds_rd128<2048>(hx[0][1], px0_2); SB();
      mm(ac1[3][3], hw[1][3], hx[1][3]); SB();
      lds_rd128<8192>(hx[0][2], px0_2); SB();
      mm(ac1[3][2], hw[1][2], hx[1][3]); SB();
      lds_rd128<10240>(hx[0][3], px0_2); SB();
      mm(ac1[3][1], hw[1][1], hx[1][3]); SB();
      mm(ac1[3][0], hw[1][0], hx[1][3]); SB();
      mmw<5>(ac1[0][0], hw[0][0], hx[0][0]); SB();
      lds_rd128<0>(hx[1][0], px1_2); SB();
      mmw<3>(ac1[1][0], hw[0][0], hx[0][1]); SB();
      lds_rd128<32768>(hw[1][0], pa1_0); SB();
      mm(ac1[0][1], hw[0][1], hx[0][0]); SB();
      lds_rd128<34816>(hw[1][1], pa1_0); SB();
      mm(ac1[1][1], hw[0][1], hx[0][1]); SB();
      lds_rd128<32768>(hw[1][2], pb1_0); SB();
      mm(ac1[0][2], hw[0][2], hx[0][0]); SB();
      lds_rd128<34816>(hw[1][3], pb1_0); SB();
      mm(ac1[1][2], hw[0][2], hx[0][1]); SB();
      lds_rd128<2048>(hx[1][1], px1_2); SB();
      mm(ac1[0][3], hw[0][3], hx[0][0]); SB();
      lds_rd128<8192>(hx[1][2], px1_2); SB();
      mm(ac1[1][3], hw[0][3], hx[0][1]); SB();
      lds_rd128<10240>(hx[1][3], px1_2); SB();
      mmw<6>(ac1[0][0], hw[1][0], hx[1][0]); SB();
      mmw<2>(ac1[1][0], hw[1][0], hx[1][1]); SB();
      mm(ac1[0][1], hw[1][1], hx[1][0]); SB();
      mm(ac1[1][1], hw[1][1], hx[1][1]); SB();
      mm(ac1[0][2], hw[1][2], hx[1][0]); SB();
      asm volatile("s_waitcnt lgkmcnt(0)" : "+v"(hx[1][2]), "+v"(hx[1][3])::"memory"); SB();
      SIG(8); SB();
      mm(ac1[1][2], hw[1][2], hx[1][1]); SB();
      PUB0(0, 0); SB();
      mm(ac1[0][3], hw[1][3], hx[1][0]); SB();
      PUB1(0, 0); SB();
      mm(ac1[1][3], hw[1][3], hx[1][1]); SB();
      PUB2(); SB();
      mm(ac1[2][0], hw[0][0], hx[0][2]); SB();
      PUB3(0, 0); SB();
      mm(ac1[3][0], hw[0][0], hx[0][3]); SB();
      PUB0(0, 1); SB();
      mm(ac1[2][0], hw[1][0], hx[1][2]); SB();
      PUB1(0, 1); SB();
      mm(ac1[3][0], hw[1][0], hx[1][3]); SB();
      PUB2(); SB();
      mm(ac1[2][1], hw[0][1], hx[0][2]); SB();
      PUB3(0, 1); SB();
      mm(ac1[3][1], hw[0][1], hx[0][3]); SB();
      PUB0(0, 2); SB();
      mm(ac1[2][1], hw[1][1], hx[1][2]); SB();
      PUB1(0, 2); SB();
      mm(ac1[3][1], hw[1][1], hx[1][3]); SB();
      PUB2(); SB();
      mm(ac1[2][2], hw[0][2], hx[0][2]); SB();
      PUB3(0, 2); SB();
      mm(ac1[3][2], hw[0][2], hx[0][3]); SB();
      PUB0(0, 3); SB();
      mm(ac1[2][2], hw[1][2], hx[1][2]); SB();
      PUB1(0, 3); SB();
      mm(ac1[3][2], hw[1][2], hx[1][3]); SB();
      PUB2(); SB();
      mm(ac1[2][3], hw[0][3], hx[0][2]); SB();
      PUB3(0, 3); SB();
      mm(ac1[3][3], hw[0][3], hx[0][3]); SB();
      PUB0(2, 0); SB();
      mm(ac1[2][3], hw[1][3], hx[1][2]); SB();
      PUB1(2, 0); SB();
      mm(ac1[3][3], hw[1][3], hx[1][3]); SB();
      PUB2(); SB();
      asm volatile("s_nop 7" : "+v"(ac1[2][3]), "+v"(ac1[3][3]), "+v"(ac1[2][2]), "+v"(ac1[3][2])); SB();
      PUB3(2, 0); SB();
      PUB0(2, 1); SB();
      PUB1(2, 1); SB();
      PUB2(); SB();
      PUB3(2, 1); SB();
      PUB0(2, 2); SB();
      PUB1(2, 2); SB();
      PUB2(); SB();
      PUB3(2, 2); SB();
      PUB0(2, 3); SB();
      PUB1(2, 3); SB();
      PUB2(); SB();
      PUB3(2, 3); SB();
      OWN_SIG(1);
    }
    if (isq) {
      SIG(8);
      OWN_SIG(1);
      if (wid == 0) { UNTIL(144u, 3075u); TOUR(); }
    }
    SPIN(tg_rd, 1);
    const unsigned tag = __builtin_amdgcn_readfirstlane(lflag[15]);
    tagv = tag;
    asm volatile("" : "+v"(tagv));
    f32x4 ac[4][2][2];
    const unsigned xg = (unsigned)((ti * 4 + ej) * 98304 + q * 24576 + wid * 2048 + lane * 16);
    const char* xb = ws + W_XB;
    const unsigned hw_ = sb + 147456 + (wid * 32 + fr) * 64 + (((fq >> 1) ^ (fr >> 2)) << 4) + (fq & 1) * 8;
    const unsigned tb[4] = {tag & 0x70007u, tag >> 3 & 0x70007u, tag >> 6 & 0x70007u, tag >> 9 & 0x70004u};
    unsigned pw[8], em[2], ek[2], msk = 0x70007u;
    u64 nz;
#define AV(G, i) ac[G][(i) >> 2 & 1][(i) >> 3][(i) & 3]
#define EM0(G, c) asm volatile("v_max3_f32 %0, |%1|, |%2|, |%3|" : "=v"(em[c]) : "v"(AV(G, 8 * (c))), "v"(AV(G, 8 * (c) + 1)), "v"(AV(G, 8 * (c) + 2)))
#define EM1(G, c, i, j) asm volatile("v_max3_f32 %0, %0, |%1|, |%2|" : "+v"(em[c]) : "v"(AV(G, i)), "v"(AV(G, j)))
#define EM2() asm volatile("v_max_f32 %0, %0, %1" : "+v"(em[0]) : "v"(em[1]))
#define ES0() asm volatile("v_subrev_u32 %0, 0x39800000, %1" : "=v"(em[1]) : "v"(em[0]))
#define ES1() asm volatile("v_cmp_ne_u32 %0, 0, %1" : "=s"(nz) : "v"(em[0]))
#define EK(c) asm volatile("v_or_b32 %0, %1, %2" : "=v"(ek[c]) : "i"(2 * (c)), "s"(tb[3]))
#define EH(G, P) asm volatile("v_cvt_pk_f16_f32 %0, %1, %2" : "=v"(pw[P]) : "v"(AV(G, 2 * (P))), "v"(AV(G, 2 * (P) + 1)))
#define ET(P) asm volatile("v_bfi_b32 %0, %1, %2, %0" : "+v"(pw[P]) : "v"(msk), "s"(tb[(P) & 3]))
#define EU(P) asm volatile("v_bfi_b32 %0, %1, %2, %0" : "+v"(pw[P]) : "v"(msk), "v"(ek[(P) >> 2]))
#define EX(G) asm volatile("v_cmp_le_u32 vcc, 0x0d800000, %17\n\ts_and_b64 vcc, vcc, %20\n\ts_cbranch_scc1 .Lxa%=\n.Lxb%=:\n\t.subsection 1\n.Lxa%=:\n\t"            \
      "v_lshrrev_b32 %17, 23, %16\n\tv_add_u32 %17, 6, %17\n\tv_lshrrev_b32 %17, 4, %17\n\tv_med3_u32 %17, %17, 1, 15\n\tv_lshlrev_b32 %16, 27, %17\n\t"              \
      "v_sub_u32 %16, 0x7f800000, %16\n\tv_and_or_b32 %18, %17, 3, %21\n\tv_lshrrev_b32 %17, 2, %17\n\tv_or_b32 %19, %21, %17\n\t"                                  \
      "v_mul_f32 %0, %0, %16\n\tv_mul_f32 %1, %1, %16\n\tv_mul_f32 %2, %2, %16\n\tv_mul_f32 %3, %3, %16\n\tv_mul_f32 %4, %4, %16\n\tv_mul_f32 %5, %5, %16\n\t"       \
      "v_mul_f32 %6, %6, %16\n\tv_mul_f32 %7, %7, %16\n\tv_mul_f32 %8, %8, %16\n\tv_mul_f32 %9, %9, %16\n\tv_mul_f32 %10, %10, %16\n\tv_mul_f32 %11, %11, %16\n\t"   \
      "v_mul_f32 %12, %12, %16\n\tv_mul_f32 %13, %13, %16\n\tv_mul_f32 %14, %14, %16\n\tv_mul_f32 %15, %15, %16\n\ts_branch .Lxb%=\n\t.subsection 0"               \
      : "+v"(AV(G, 0)), "+v"(AV(G, 1)), "+v"(AV(G, 2)), "+v"(AV(G, 3)), "+v"(AV(G, 4)), "+v"(AV(G, 5)), "+v"(AV(G, 6)), "+v"(AV(G, 7)), "+v"(AV(G, 8)), "+v"(AV(G, 9)),  \
        "+v"(AV(G, 10)), "+v"(AV(G, 11)), "+v"(AV(G, 12)), "+v"(AV(G, 13)), "+v"(AV(G, 14)), "+v"(AV(G, 15)), "+v"(em[0]), "+v"(em[1]), "+v"(ek[0]), "+v"(ek[1])        \
      : "s"(nz), "s"(tb[3]) : "vcc", "scc")
    const char *xd0 = xb + ((q ^ 3) - q) * 24576, *xd1 = xb + ((q ^ 2) - q) * 24576 + 8192, *xd2 = xb + ((q ^ 1) - q) * 24576 + 16384;
    const char *xr0 = xb, *xr1 = xb + 8192, *xr2 = xb + 16384;
    asm volatile("" : "+s"(xd0), "+s"(xd1), "+s"(xd2), "+s"(xr0), "+s"(xr1), "+s"(xr2));
#define SEND(G, Q) asm volatile("global_store_dwordx4 %0, %1, %2 offset:%3 sc1\n\ts_nop 1" ::"v"(xg), "v"(u32x4{pw[4 * (Q)], pw[4 * (Q) + 1], pw[4 * (Q) + 2], pw[4 * (Q) + 3]}), "s"(xd##G), "i"((Q) * 1024) : "memory")
    unsigned oo;
#define OO() do { oo = (unsigned)(((ti * 128 + wid * 32 + fr) * D_ + e0 + (fq & 1) * 16 + (fq >> 1) * 8) * 2); asm volatile("" : "+v"(oo)); } while (0)
    unsigned sn = 9 * 4u;
    float gs[2];
    u32x4 rv[3][2];
    u32x2 dq[4];
    unsigned f2;
    const unsigned d2_rd = best_rd;
#define RV_LD(S, tail) asm volatile("global_load_dwordx4 %0, %2, %3 sc1\n\tglobal_load_dwordx4 %1, %2, %3 offset:1024 sc1" tail \
                                    : "=&v"(rv[S][0]), "=&v"(rv[S][1]) : "v"(xg), "s"(xr##S) : "memory")
#define D_RD(i) asm volatile("ds_read_b64 %0, %1 offset:%2" : "=v"(dq[i]) : "v"(hw_ ^ ((i) & 1) * 32u), "i"(((i) >> 1) * 1024) : "memory")
#define G_RD(i) asm volatile("ds_read_b32 %0, %1 offset:%2" : "=v"(gs[i]) : "v"(sb + RING + L_G + (wid * 32 + fr) * 4), "i"((i) * 64) : "memory")
#define SIGQ(W) do { __builtin_amdgcn_s_waitcnt(0xc07f | ((W) << 8)); SB(); asm volatile("global_load_lds_dword %0, %1" ::"v"(sn), "s"(tab) : "memory"); } while (0)
#define SIGN() asm volatile("v_add_u32 %0, 4, %0" : "+v"(sn))
    asm volatile("s_mov_b32 m0, %2" : "+v"(sn), "+v"(msk) : "s"(row_s) : "memory");
    SPINF(lf_rd, 9, 0); SB();
    lds_rd128<49152>(wr[0][0], wa0_0); SB();
    lds_rd128<51200>(wr[0][1], wa0_0); SB();
    lds_rd128<49152>(wr[0][2], wa1_0); SB();
    lds_rd128<51200>(wr[0][3], wa1_0); SB();
    lds_rd128<0>(wr[1][0], wa0_1); SB();
    lds_rd128<2048>(wr[1][1], wa0_1); SB();
    lds_rd128<0>(wr[1][2], wa1_1); SB();
    lds_rd128<2048>(wr[1][3], wa1_1); SB();
    asm volatile("s_waitcnt lgkmcnt(0)" : "+v"(wr[0][0]), "+v"(wr[0][1]), "+v"(wr[0][2]), "+v"(wr[0][3]), "+v"(wr[1][0]), "+v"(wr[1][1]), "+v"(wr[1][2]), "+v"(wr[1][3])::"memory"); SB();
    OWN_SIG(2); SB();
    TRYFLAG(0); SB();
    SPINF(lf_rd, 10, 0); SB();
    lds_rd128<16384>(wr[2][0], wa0_1); SB();
    lds_rd128<18432>(wr[2][1], wa0_1); SB();
    lds_rd128<16384>(wr[2][2], wa1_1); SB();
    lds_rd128<18432>(wr[2][3], wa1_1); SB();
    lds_rd128<0>(wr[3][0], wa0_0); SB();
    lds_rd128<2048>(wr[3][1], wa0_0); SB();
    lds_rd128<0>(wr[3][2], wa1_0); SB();
    lds_rd128<2048>(wr[3][3], wa1_0); SB();
    asm volatile("s_waitcnt lgkmcnt(0)" : "+v"(wr[2][0]), "+v"(wr[2][1]), "+v"(wr[2][2]), "+v"(wr[2][3]), "+v"(wr[3][0]), "+v"(wr[3][1]), "+v"(wr[3][2]), "+v"(wr[3][3])::"memory"); SB();
    OWN_SIG(3); SB();
    TRYFLAG(0); SB();
    SPINF(lf_rd, 11, 0); SB();
    lds_rd128<32768>(wr[4][0], wa0_1); SB();
    lds_rd128<34816>(wr[4][1], wa0_1); SB();
    lds_rd128<32768>(wr[4][2], wa1_1); SB();
    lds_rd128<34816>(wr[4][3], wa1_1); SB();
    lds_rd128<16384>(wr[5][0], wa0_0); SB();
    lds_rd128<18432>(wr[5][1], wa0_0); SB();
    lds_rd128<16384>(wr[5][2], wa1_0); SB();
    lds_rd128<18432>(wr[5][3], wa1_0); SB();
    asm volatile("s_waitcnt lgkmcnt(0)" : "+v"(wr[4][0]), "+v"(wr[4][1]), "+v"(wr[4][2]), "+v"(wr[4][3]), "+v"(wr[5][0]), "+v"(wr[5][1]), "+v"(wr[5][2]), "+v"(wr[5][3])::"memory"); SB();
    OWN_SIG(4); SB();
    TRYFLAG(0); SB();
    SPINF(lf_rd, 12, 0); SB();
    lds_rd128<49152>(wr[6][0], wa0_1); SB();
    lds_rd128<51200>(wr[6][1], wa0_1); SB();
    lds_rd128<49152>(wr[6][2], wa1_1); SB();
    lds_rd128<51200>(wr[6][3], wa1_1); SB();
    lds_rd128<32768>(wr[7][0], wa0_0); SB();
    lds_rd128<34816>(wr[7][1], wa0_0); SB();
    lds_rd128<32768>(wr[7][2], wa1_0); SB();
    lds_rd128<34816>(wr[7][3], wa1_0); SB();
    asm volatile("s_waitcnt lgkmcnt(0)" : "+v"(wr[6][0]), "+v"(wr[6][1]), "+v"(wr[6][2]), "+v"(wr[6][3]), "+v"(wr[7][0]), "+v"(wr[7][1]), "+v"(wr[7][2]), "+v"(wr[7][3])::"memory"); SB();
    OWN_SIG(5); SB();
    TRYFLAG(0); SB();
    FLAG(0); SB();
    OO();
    SPIN(lf_rd, 13); SB();
    lds_rd128<0>(fb[0], xa_0); SB();
    lds_rd128<1024>(fb[1], xa_0); SB();
    lds_rd128<2048>(fb[2], xa_0); SB();
    lds_rd128<3072>(fb[3], xa_0); SB();
    lds_rd128<4096>(fb[4], xa_0); SB();
    lds_rd128<5120>(fb[5], xa_0); SB();
    lds_rd32(fl, lf_rd); SB();
    mm0w<6>(ac[0][0][0], fb[0], wr[0][0]); SB();
    lds_rd128<6144>(fb[6], xa_0); SB();
    mm0(ac[0][0][1], fb[0], wr[0][1]); SB();
    mm0w<6>(ac[0][1][1], fb[1], wr[0][1]); SB();
    lds_rd128<7168>(fb[7], xa_0); SB();
    mm0(ac[0][1][0], fb[1], wr[0][0]); SB();
    mm0w<2>(ac[1][0][0], fb[2], wr[0][0]); SB();
    chk<14>(fl, lf_rd, bud); SB();
    lds_rd128<8192>(fb[8], xa_0); SB();
    mm0(ac[1][0][1], fb[2], wr[0][1]); SB();
    mm0(ac[1][1][1], fb[3], wr[0][1]); SB();
    lds_rd128<9216>(fb[0], xa_0); SB();
    mm0(ac[1][1][0], fb[3], wr[0][0]); SB();
    mm(ac[0][0][0], fb[4], wr[0][2]); SB();
    lds_rd128<10240>(fb[1], xa_0); SB();
    SIGQ(3); SB();
    mm(ac[0][0][1], fb[4], wr[0][3]); SB();
    mm(ac[0][1][1], fb[5], wr[0][3]); SB();
    lds_rd128<11264>(fb[2], xa_0); SB();
    SIGN(); SB();
    mm(ac[0][1][0], fb[5], wr[0][2]); SB();
    mm(ac[1][0][0], fb[6], wr[0][2]); SB();
    lds_rd128<12288>(fb[3], xa_0); SB();
    mm(ac[1][0][1], fb[6], wr[0][3]); SB();
    mm(ac[1][1][1], fb[7], wr[0][3]); SB();
    lds_rd128<13312>(fb[4], xa_0); SB();
    lds_rd32(fl, lf_rd); SB();
    mm(ac[1][1][0], fb[7], wr[0][2]); SB();
    mm0w<5>(ac[2][0][0], fb[8], wr[0][0]); SB();
    lds_rd128<14336>(fb[5], xa_0); SB();
    mm0(ac[2][0][1], fb[8], wr[0][1]); SB();
    mm0(ac[2][1][1], fb[0], wr[0][1]); SB();
    lds_rd128<15360>(fb[6], xa_0); SB();
    mm0(ac[2][1][0], fb[0], wr[0][0]); SB();
    mm0w<2>(ac[3][0][0], fb[1], wr[0][0]); SB();
    chk<15>(fl, lf_rd, bud); SB();
    lds_rd128<32768>(fb[7], xa_1); SB();
    mm0(ac[3][0][1], fb[1], wr[0][1]); SB();
    mm0(ac[3][1][1], fb[2], wr[0][1]); SB();
    lds_rd128<33792>(fb[8], xa_1); SB();
    mm0(ac[3][1][0], fb[2], wr[0][0]); SB();
    mm(ac[2][0][0], fb[3], wr[0][2]); SB();
    lds_rd128<34816>(fb[0], xa_1); SB();
    SIGQ(3); SB();
    mm(ac[2][0][1], fb[3], wr[0][3]); SB();
    mm(ac[2][1][1], fb[4], wr[0][3]); SB();
    lds_rd128<35840>(fb[1], xa_1); SB();
    SIGN(); SB();
    mm(ac[2][1][0], fb[4], wr[0][2]); SB();
    mm(ac[3][0][0], fb[5], wr[0][2]); SB();
    lds_rd128<36864>(fb[2], xa_1); SB();
    mm(ac[3][0][1], fb[5], wr[0][3]); SB();
    mm(ac[3][1][1], fb[6], wr[0][3]); SB();
    lds_rd128<37888>(fb[3], xa_1); SB();
    lds_rd32(fl, lf_rd); SB();
    mm(ac[3][1][0], fb[6], wr[0][2]); SB();
    mmw<5>(ac[0][0][0], fb[7], wr[1][0]); SB();
    lds_rd128<38912>(fb[4], xa_1); SB();
    mm(ac[0][0][1], fb[7], wr[1][1]); SB();
    mm(ac[0][1][1], fb[8], wr[1][1]); SB();
    lds_rd128<39936>(fb[5], xa_1); SB();
    mm(ac[0][1][0], fb[8], wr[1][0]); SB();
    mmchkw<16, 2>(ac[1][0][0], fb[0], wr[1][0], fl, lf_rd, bud); SB();
    lds_rd128<40960>(fb[6], xa_1); SB();
    mm(ac[1][0][1], fb[0], wr[1][1]); SB();
    lds_rd128<0>(wr[8][0], wa0_2); SB();
    mm(ac[1][1][1], fb[1], wr[1][1]); SB();
    lds_rd128<41984>(fb[7], xa_1); SB();
    lds_rd128<2048>(wr[8][1], wa0_2); SB();
    mm(ac[1][1][0], fb[1], wr[1][0]); SB();
    lds_rd128<0>(wr[8][2], wa1_2); SB();
    mm(ac[0][0][0], fb[2], wr[1][2]); SB();
    lds_rd128<43008>(fb[8], xa_1); SB();
    lds_rd128<2048>(wr[8][3], wa1_2); SB();
    mm(ac[0][0][1], fb[2], wr[1][3]); SB();
    mm(ac[0][1][1], fb[3], wr[1][3]); SB();
    lds_rd128<44032>(fb[0], xa_1); SB();
    mm(ac[0][1][0], fb[3], wr[1][2]); SB();
    mmw<8>(ac[1][0][0], fb[4], wr[1][2]); SB();
    lds_rd128<45056>(fb[1], xa_1); SB();
    mm(ac[1][0][1], fb[4], wr[1][3]); SB();
    mm(ac[1][1][1], fb[5], wr[1][3]); SB();
    lds_rd128<46080>(fb[2], xa_1); SB();
    lds_rd32(fl, lf_rd); SB();
    mm(ac[1][1][0], fb[5], wr[1][2]); SB();
    mmw<9>(ac[2][0][0], fb[6], wr[1][0]); SB();
    lds_rd128<47104>(fb[3], xa_1); SB();
    mm(ac[2][0][1], fb[6], wr[1][1]); SB();
    mmw<8>(ac[2][1][1], fb[7], wr[1][1]); SB();
    lds_rd128<48128>(fb[4], xa_1); SB();
    mm(ac[2][1][0], fb[7], wr[1][0]); SB();
    mmchkw<17, 2>(ac[3][0][0], fb[8], wr[1][0], fl, lf_rd, bud); SB();
    lds_rd128<16384>(fb[5], xa_0); SB();
    mm(ac[3][0][1], fb[8], wr[1][1]); SB();
    mm(ac[3][1][1], fb[0], wr[1][1]); SB();
    lds_rd128<17408>(fb[6], xa_0); SB();
    mm(ac[3][1][0], fb[0], wr[1][0]); SB();
    mm(ac[2][0][0], fb[1], wr[1][2]); SB();
    lds_rd128<18432>(fb[7], xa_0); SB();
    SIGQ(3); SB();
    mm(ac[2][0][1], fb[1], wr[1][3]); SB();
    mm(ac[2][1][1], fb[2], wr[1][3]); SB();
    lds_rd128<19456>(fb[8], xa_0); SB();
    SIGN(); SB();
    mm(ac[2][1][0], fb[2], wr[1][2]); SB();
    mm(ac[3][0][0], fb[3], wr[1][2]); SB();
    lds_rd128<20480>(fb[0], xa_0); SB();
    mm(ac[3][0][1], fb[3], wr[1][3]); SB();
    mm(ac[3][1][1], fb[4], wr[1][3]); SB();
    lds_rd128<21504>(fb[1], xa_0); SB();
    mm(ac[3][1][0], fb[4], wr[1][2]); SB();
    mmw<4>(ac[0][0][0], fb[5], wr[2][0]); SB();
    lds_rd128<22528>(fb[2], xa_0); SB();
    mm(ac[0][0][1], fb[5], wr[2][1]); SB();
    mm(ac[0][1][1], fb[6], wr[2][1]); SB();
    lds_rd128<23552>(fb[3], xa_0); SB();
    mm(ac[0][1][0], fb[6], wr[2][0]); SB();
    mmw<4>(ac[1][0][0], fb[7], wr[2][0]); SB();
    lds_rd128<24576>(fb[4], xa_0); SB();
    mm(ac[1][0][1], fb[7], wr[2][1]); SB();
    lds_rd128<49152>(wr[9][0], wa0_0); SB();
    mm(ac[1][1][1], fb[8], wr[2][1]); SB();
    lds_rd128<25600>(fb[5], xa_0); SB();
    lds_rd128<51200>(wr[9][1], wa0_0); SB();
    mm(ac[1][1][0], fb[8], wr[2][0]); SB();
    lds_rd128<49152>(wr[9][2], wa1_0); SB();
    mmw<7>(ac[0][0][0], fb[0], wr[2][2]); SB();
    lds_rd128<26624>(fb[6], xa_0); SB();
    lds_rd128<51200>(wr[9][3], wa1_0); SB();
    mm(ac[0][0][1], fb[0], wr[2][3]); SB();
    mm(ac[0][1][1], fb[1], wr[2][3]); SB();
    lds_rd128<27648>(fb[7], xa_0); SB();
    mm(ac[0][1][0], fb[1], wr[2][2]); SB();
    mmw<8>(ac[1][0][0], fb[2], wr[2][2]); SB();
    lds_rd128<28672>(fb[8], xa_0); SB();
    mm(ac[1][0][1], fb[2], wr[2][3]); SB();
    mm(ac[1][1][1], fb[3], wr[2][3]); SB();
    lds_rd128<29696>(fb[0], xa_0); SB();
    lds_rd32(fl, lf_rd); SB();
    mm(ac[1][1][0], fb[3], wr[2][2]); SB();
    mmw<9>(ac[2][0][0], fb[4], wr[2][0]); SB();
    lds_rd128<30720>(fb[1], xa_0); SB();
    mm(ac[2][0][1], fb[4], wr[2][1]); SB();
    mmw<8>(ac[2][1][1], fb[5], wr[2][1]); SB();
    lds_rd128<31744>(fb[2], xa_0); SB();
    mm(ac[2][1][0], fb[5], wr[2][0]); SB();
    mmchkw<18, 2>(ac[3][0][0], fb[6], wr[2][0], fl, lf_rd, bud); SB();
    lds_rd128<49152>(fb[3], xa_1); SB();
    mm(ac[3][0][1], fb[6], wr[2][1]); SB();
    mm(ac[3][1][1], fb[7], wr[2][1]); SB();
    lds_rd128<50176>(fb[4], xa_1); SB();
    mm(ac[3][1][0], fb[7], wr[2][0]); SB();
    mm(ac[2][0][0], fb[8], wr[2][2]); SB();
    lds_rd128<51200>(fb[5], xa_1); SB();
    SIGQ(3); SB();
    mm(ac[2][0][1], fb[8], wr[2][3]); SB();
    mm(ac[2][1][1], fb[0], wr[2][3]); SB();
    lds_rd128<52224>(fb[6], xa_1); SB();
    SIGN(); SB();
    mm(ac[2][1][0], fb[0], wr[2][2]); SB();
    mm(ac[3][0][0], fb[1], wr[2][2]); SB();
    lds_rd128<53248>(fb[7], xa_1); SB();
    mm(ac[3][0][1], fb[1], wr[2][3]); SB();
    mm(ac[3][1][1], fb[2], wr[2][3]); SB();
    lds_rd128<54272>(fb[8], xa_1); SB();
    mm(ac[3][1][0], fb[2], wr[2][2]); SB();
    mmw<4>(ac[0][0][0], fb[3], wr[3][0]); SB();
    lds_rd128<55296>(fb[0], xa_1); SB();
    mm(ac[0][0][1], fb[3], wr[3][1]); SB();
    mm(ac[0][1][1], fb[4], wr[3][1]); SB();
    lds_rd128<56320>(fb[1], xa_1); SB();
    mm(ac[0][1][0], fb[4], wr[3][0]); SB();
    mmw<4>(ac[1][0][0], fb[5], wr[3][0]); SB();
    lds_rd128<57344>(fb[2], xa_1); SB();
    mm(ac[1][0][1], fb[5], wr[3][1]); SB();
    lds_rd128<0>(wr[10][0], wa0_1); SB();
    mm(ac[1][1][1], fb[6], wr[3][1]); SB();
    lds_rd128<58368>(fb[3], xa_1); SB();
    lds_rd128<2048>(wr[10][1], wa0_1); SB();
    mm(ac[1][1][0], fb[6], wr[3][0]); SB();
    lds_rd128<0>(wr[10][2], wa1_1); SB();
    mmw<7>(ac[0][0][0], fb[7], wr[3][2]); SB();
    lds_rd128<59392>(fb[4], xa_1); SB();
    lds_rd128<2048>(wr[10][3], wa1_1); SB();
    mm(ac[0][0][1], fb[7], wr[3][3]); SB();
    mm(ac[0][1][1], fb[8], wr[3][3]); SB();
    lds_rd128<60416>(fb[5], xa_1); SB();
    mm(ac[0][1][0], fb[8], wr[3][2]); SB();
    mmw<8>(ac[1][0][0], fb[0], wr[3][2]); SB();
    lds_rd128<61440>(fb[6], xa_1); SB();
    mm(ac[1][0][1], fb[0], wr[3][3]); SB();
    mm(ac[1][1][1], fb[1], wr[3][3]); SB();
    lds_rd128<62464>(fb[7], xa_1); SB();
    lds_rd32(fl, lf_rd); SB();
    mm(ac[1][1][0], fb[1], wr[3][2]); SB();
    mmw<9>(ac[2][0][0], fb[2], wr[3][0]); SB();
    lds_rd128<63488>(fb[8], xa_1); SB();
    mm(ac[2][0][1], fb[2], wr[3][1]); SB();
    mmw<8>(ac[2][1][1], fb[3], wr[3][1]); SB();
    lds_rd128<64512>(fb[0], xa_1); SB();
    mm(ac[2][1][0], fb[3], wr[3][0]); SB();
    mmchkw<19, 2>(ac[3][0][0], fb[4], wr[3][0], fl, lf_rd, bud); SB();
    lds_rd128<32768>(fb[1], xa_0); SB();
    mm(ac[3][0][1], fb[4], wr[3][1]); SB();
    mm(ac[3][1][1], fb[5], wr[3][1]); SB();
    lds_rd128<33792>(fb[2], xa_0); SB();
    mm(ac[3][1][0], fb[5], wr[3][0]); SB();
    mm(ac[2][0][0], fb[6], wr[3][2]); SB();
    lds_rd128<34816>(fb[3], xa_0); SB();
    SIGQ(3); SB();
    mm(ac[2][0][1], fb[6], wr[3][3]); SB();
    mm(ac[2][1][1], fb[7], wr[3][3]); SB();
    lds_rd128<35840>(fb[4], xa_0); SB();
    SIGN(); SB();
    mm(ac[2][1][0], fb[7], wr[3][2]); SB();
    mm(ac[3][0][0], fb[8], wr[3][2]); SB();
    lds_rd128<36864>(fb[5], xa_0); SB();
    mm(ac[3][0][1], fb[8], wr[3][3]); SB();
    mm(ac[3][1][1], fb[0], wr[3][3]); SB();
    lds_rd128<37888>(fb[6], xa_0); SB();
    mm(ac[3][1][0], fb[0], wr[3][2]); SB();
    mmw<4>(ac[0][0][0], fb[1], wr[4][0]); SB();
    lds_rd128<38912>(fb[7], xa_0); SB();
    mm(ac[0][0][1], fb[1], wr[4][1]); SB();
    mm(ac[0][1][1], fb[2], wr[4][1]); SB();
    lds_rd128<39936>(fb[8], xa_0); SB();
    mm(ac[0][1][0], fb[2], wr[4][0]); SB();
    mmw<4>(ac[1][0][0], fb[3], wr[4][0]); SB();
    lds_rd128<40960>(fb[0], xa_0); SB();
    mm(ac[1][0][1], fb[3], wr[4][1]); SB();
    lds_rd128<16384>(wr[11][0], wa0_1); SB();
    mm(ac[1][1][1], fb[4], wr[4][1]); SB();
    lds_rd128<41984>(fb[1], xa_0); SB();
    lds_rd128<18432>(wr[11][1], wa0_1); SB();
    mm(ac[1][1][0], fb[4], wr[4][0]); SB();
    lds_rd128<16384>(wr[11][2], wa1_1); SB();
    mmw<7>(ac[0][0][0], fb[5], wr[4][2]); SB();
    lds_rd128<43008>(fb[2], xa_0); SB();
    lds_rd128<18432>(wr[11][3], wa1_1); SB();
    mm(ac[0][0][1], fb[5], wr[4][3]); SB();
    mm(ac[0][1][1], fb[6], wr[4][3]); SB();
    lds_rd128<44032>(fb[3], xa_0); SB();
    mm(ac[0][1][0], fb[6], wr[4][2]); SB();
    mmw<8>(ac[1][0][0], fb[7], wr[4][2]); SB();
    lds_rd128<45056>(fb[4], xa_0); SB();
    mm(ac[1][0][1], fb[7], wr[4][3]); SB();
    mm(ac[1][1][1], fb[8], wr[4][3]); SB();
    lds_rd128<46080>(fb[5], xa_0); SB();
    lds_rd32(fl, lf_rd); SB();
    mm(ac[1][1][0], fb[8], wr[4][2]); SB();
    mmw<9>(ac[2][0][0], fb[0], wr[4][0]); SB();
    lds_rd128<47104>(fb[6], xa_0); SB();
    mm(ac[2][0][1], fb[0], wr[4][1]); SB();
    mmw<8>(ac[2][1][1], fb[1], wr[4][1]); SB();
    lds_rd128<48128>(fb[7], xa_0); SB();
    mm(ac[2][1][0], fb[1], wr[4][0]); SB();
    mmchkw<20, 2>(ac[3][0][0], fb[2], wr[4][0], fl, lf_rd, bud); SB();
    lds_rd128<0>(fb[8], xa_0); SB();
    mm(ac[3][0][1], fb[2], wr[4][1]); SB();
    mm(ac[3][1][1], fb[3], wr[4][1]); SB();
    lds_rd128<1024>(fb[0], xa_0); SB();
    mm(ac[3][1][0], fb[3], wr[4][0]); SB();
    mm(ac[2][0][0], fb[4], wr[4][2]); SB();
    lds_rd128<2048>(fb[1], xa_0); SB();
    SIGQ(3); SB();
    mm(ac[2][0][1], fb[4], wr[4][3]); SB();
    mm(ac[2][1][1], fb[5], wr[4][3]); SB();
    lds_rd128<3072>(fb[2], xa_0); SB();
    SIGN(); SB();
    mm(ac[2][1][0], fb[5], wr[4][2]); SB();
    mm(ac[3][0][0], fb[6], wr[4][2]); SB();
    lds_rd128<4096>(fb[3], xa_0); SB();
    mm(ac[3][0][1], fb[6], wr[4][3]); SB();
    mm(ac[3][1][1], fb[7], wr[4][3]); SB();
    lds_rd128<5120>(fb[4], xa_0); SB();
    mm(ac[3][1][0], fb[7], wr[4][2]); SB();
    mmw<4>(ac[0][0][0], fb[8], wr[5][0]); SB();
    lds_rd128<6144>(fb[5], xa_0); SB();
    mm(ac[0][0][1], fb[8], wr[5][1]); SB();
    mm(ac[0][1][1], fb[0], wr[5][1]); SB();
    lds_rd128<7168>(fb[6], xa_0); SB();
    mm(ac[0][1][0], fb[0], wr[5][0]); SB();
    mmw<4>(ac[1][0][0], fb[1], wr[5][0]); SB();
    lds_rd128<8192>(fb[7], xa_0); SB();
    mm(ac[1][0][1], fb[1], wr[5][1]); SB();
    mm(ac[1][1][1], fb[2], wr[5][1]); SB();
    lds_rd128<9216>(fb[8], xa_0); SB();
    mm(ac[1][1][0], fb[2], wr[5][0]); SB();
    mmw<4>(ac[0][0][0], fb[3], wr[5][2]); SB();
    lds_rd128<10240>(fb[0], xa_0); SB();
    mm(ac[0][0][1], fb[3], wr[5][3]); SB();
    mm(ac[0][1][1], fb[4], wr[5][3]); SB();
    lds_rd128<11264>(fb[1], xa_0); SB();
    mm(ac[0][1][0], fb[4], wr[5][2]); SB();
    mmw<4>(ac[1][0][0], fb[5], wr[5][2]); SB();
    lds_rd128<12288>(fb[2], xa_0); SB();
    mm(ac[1][0][1], fb[5], wr[5][3]); SB();
    mm(ac[1][1][1], fb[6], wr[5][3]); SB();
    lds_rd128<13312>(fb[3], xa_0); SB();
    lds_rd32(fl, lf_rd); SB();
    mm(ac[1][1][0], fb[6], wr[5][2]); SB();
    mmw<5>(ac[2][0][0], fb[7], wr[5][0]); SB();
    lds_rd128<14336>(fb[4], xa_0); SB();
    mm(ac[2][0][1], fb[7], wr[5][1]); SB();
    mm(ac[2][1][1], fb[8], wr[5][1]); SB();
    lds_rd128<15360>(fb[5], xa_0); SB();
    mm(ac[2][1][0], fb[8], wr[5][0]); SB();
    mmchkw<21, 2>(ac[3][0][0], fb[0], wr[5][0], fl, lf_rd, bud); SB();
    lds_rd128<0>(fb[6], xa_2); SB();
    mm(ac[3][0][1], fb[0], wr[5][1]); SB();
    mm(ac[3][1][1], fb[1], wr[5][1]); SB();
    lds_rd128<1024>(fb[7], xa_2); SB();
    mm(ac[3][1][0], fb[1], wr[5][0]); SB();
    mm(ac[2][0][0], fb[2], wr[5][2]); SB();
    lds_rd128<2048>(fb[8], xa_2); SB();
    SIGQ(3); SB();
    mm(ac[2][0][1], fb[2], wr[5][3]); SB();
    mm(ac[2][1][1], fb[3], wr[5][3]); SB();
    lds_rd128<3072>(fb[0], xa_2); SB();
    SIGN(); SB();
    mm(ac[2][1][0], fb[3], wr[5][2]); SB();
    mm(ac[3][0][0], fb[4], wr[5][2]); SB();
    lds_rd128<4096>(fb[1], xa_2); SB();
    mm(ac[3][0][1], fb[4], wr[5][3]); SB();
    mm(ac[3][1][1], fb[5], wr[5][3]); SB();
    lds_rd128<5120>(fb[2], xa_2); SB();
    mm(ac[3][1][0], fb[5], wr[5][2]); SB();
    mmw<4>(ac[0][0][0], fb[6], wr[6][0]); SB();
    lds_rd128<6144>(fb[3], xa_2); SB();
    mm(ac[0][0][1], fb[6], wr[6][1]); SB();
    mm(ac[0][1][1], fb[7], wr[6][1]); SB();
    lds_rd128<7168>(fb[4], xa_2); SB();
    mm(ac[0][1][0], fb[7], wr[6][0]); SB();
    mmw<4>(ac[1][0][0], fb[8], wr[6][0]); SB();
    lds_rd128<8192>(fb[5], xa_2); SB();
    mm(ac[1][0][1], fb[8], wr[6][1]); SB();
    mm(ac[1][1][1], fb[0], wr[6][1]); SB();
    lds_rd128<9216>(fb[6], xa_2); SB();
    mm(ac[1][1][0], fb[0], wr[6][0]); SB();
    mmw<4>(ac[0][0][0], fb[1], wr[6][2]); SB();
    lds_rd128<10240>(fb[7], xa_2); SB();
    mm(ac[0][0][1], fb[1], wr[6][3]); SB();
    mm(ac[0][1][1], fb[2], wr[6][3]); SB();
    lds_rd128<11264>(fb[8], xa_2); SB();
    mm(ac[0][1][0], fb[2], wr[6][2]); SB();
    mmw<4>(ac[1][0][0], fb[3], wr[6][2]); SB();
    lds_rd128<12288>(fb[0], xa_2); SB();
    mm(ac[1][0][1], fb[3], wr[6][3]); SB();
    mm(ac[1][1][1], fb[4], wr[6][3]); SB();
    lds_rd128<13312>(fb[1], xa_2); SB();
    lds_rd32(fl, lf_rd); SB();
    mm(ac[1][1][0], fb[4], wr[6][2]); SB();
    mmw<5>(ac[0][0][0], fb[5], wr[7][0]); SB();
    lds_rd128<14336>(fb[2], xa_2); SB();
    mm(ac[0][0][1], fb[5], wr[7][1]); SB();
    mm(ac[0][1][1], fb[6], wr[7][1]); SB();
    lds_rd128<15360>(fb[3], xa_2); SB();
    mm(ac[0][1][0], fb[6], wr[7][0]); SB();
    mmchkw<22, 2>(ac[1][0][0], fb[7], wr[7][0], fl, lf_rd, bud); SB();
    lds_rd128<49152>(fb[4], xa_0); SB();
    mm(ac[1][0][1], fb[7], wr[7][1]); SB();
    mm(ac[1][1][1], fb[8], wr[7][1]); SB();
    lds_rd128<50176>(fb[5], xa_0); SB();
    mm(ac[1][1][0], fb[8], wr[7][0]); SB();
    mm(ac[0][0][0], fb[0], wr[7][2]); SB();
    lds_rd128<51200>(fb[6], xa_0); SB();
    SIGQ(3); SB();
    mm(ac[0][0][1], fb[0], wr[7][3]); SB();
    mm(ac[0][1][1], fb[1], wr[7][3]); SB();
    lds_rd128<52224>(fb[7], xa_0); SB();
    SIGN(); SB();
    mm(ac[0][1][0], fb[1], wr[7][2]); SB();
    mm(ac[1][0][0], fb[2], wr[7][2]); SB();
    lds_rd128<53248>(fb[8], xa_0); SB();
    mm(ac[1][0][1], fb[2], wr[7][3]); SB();
    mm(ac[1][1][1], fb[3], wr[7][3]); SB();
    lds_rd128<54272>(fb[0], xa_0); SB();
    mm(ac[1][1][0], fb[3], wr[7][2]); SB();
    mmw<4>(ac[2][0][0], fb[4], wr[6][0]); SB();
    lds_rd128<55296>(fb[1], xa_0); SB();
    mm(ac[2][0][1], fb[4], wr[6][1]); SB();
    mm(ac[2][1][1], fb[5], wr[6][1]); SB();
    lds_rd128<56320>(fb[2], xa_0); SB();
    mm(ac[2][1][0], fb[5], wr[6][0]); SB();
    mmw<4>(ac[2][0][0], fb[6], wr[6][2]); SB();
    lds_rd128<57344>(fb[3], xa_0); SB();
    mm(ac[2][0][1], fb[6], wr[6][3]); SB();
    lds_rd128<32768>(wr[12][0], wa0_1); SB();
    mm(ac[2][1][1], fb[7], wr[6][3]); SB();
    lds_rd128<58368>(fb[4], xa_0); SB();
    lds_rd128<34816>(wr[12][1], wa0_1); SB();
    mm(ac[2][1][0], fb[7], wr[6][2]); SB();
    lds_rd128<32768>(wr[12][2], wa1_1); SB();
    mmw<7>(ac[2][0][0], fb[8], wr[7][0]); SB();
    lds_rd128<59392>(fb[5], xa_0); SB();
    lds_rd128<34816>(wr[12][3], wa1_1); SB();
    mm(ac[2][0][1], fb[8], wr[7][1]); SB();
    mm(ac[2][1][1], fb[0], wr[7][1]); SB();
    lds_rd128<60416>(fb[6], xa_0); SB();
    mm(ac[2][1][0], fb[0], wr[7][0]); SB();
    mmw<8>(ac[2][0][0], fb[1], wr[7][2]); SB();
    lds_rd128<61440>(fb[7], xa_0); SB();
    mm(ac[2][0][1], fb[1], wr[7][3]); SB();
    mm(ac[2][1][1], fb[2], wr[7][3]); SB();
    lds_rd128<62464>(fb[8], xa_0); SB();
    lds_rd32(fl, lf_rd); SB();
    mm(ac[2][1][0], fb[2], wr[7][2]); SB();
    mmw<9>(ac[0][0][0], fb[3], wr[8][0]); SB();
    lds_rd128<63488>(fb[0], xa_0); SB();
    mm(ac[0][0][1], fb[3], wr[8][1]); SB();
    mmw<8>(ac[0][1][1], fb[4], wr[8][1]); SB();
    lds_rd128<64512>(fb[1], xa_0); SB();
    mm(ac[0][1][0], fb[4], wr[8][0]); SB();
    mmchkw<23, 2>(ac[0][0][0], fb[5], wr[8][2], fl, lf_rd, bud); SB();
    asm volatile(STOP ::: "memory"); SB();
    lds_rd128<49152>(fb[2], xa_1); SB();
    mm(ac[0][0][1], fb[5], wr[8][3]); SB();
    lds_rd128<16384>(wr[13][0], wa0_0); SB();
    mm(ac[0][1][1], fb[6], wr[8][3]); SB();
    lds_rd128<50176>(fb[3], xa_1); SB();
    lds_rd128<18432>(wr[13][1], wa0_0); SB();
    mm(ac[0][1][0], fb[6], wr[8][2]); SB();
    lds_rd128<16384>(wr[13][2], wa1_0); SB();
    mm(ac[0][0][0], fb[7], wr[9][0]); SB();
    lds_rd128<51200>(fb[4], xa_1); SB();
    lds_rd128<18432>(wr[13][3], wa1_0); SB();
    mm(ac[0][0][1], fb[7], wr[9][1]); SB();
    SIGQ(7); SB();
    mm(ac[0][1][1], fb[8], wr[9][1]); SB();
    lds_rd128<52224>(fb[5], xa_1); SB();
    mm(ac[0][1][0], fb[8], wr[9][0]); SB();
    SIGN(); SB();
    mm(ac[0][0][0], fb[0], wr[9][2]); SB();
    lds_rd128<53248>(fb[6], xa_1); SB();
    mm(ac[0][0][1], fb[0], wr[9][3]); SB();
    mm(ac[0][1][1], fb[1], wr[9][3]); SB();
    lds_rd128<54272>(fb[7], xa_1); SB();
    mm(ac[0][1][0], fb[1], wr[9][2]); SB();
    mmw<8>(ac[0][0][0], fb[2], wr[10][0]); SB();
    lds_rd128<55296>(fb[8], xa_1); SB();
    mm(ac[0][0][1], fb[2], wr[10][1]); SB();
    mmw<7>(ac[0][1][1], fb[3], wr[10][1]); SB();
    lds_rd128<56320>(fb[0], xa_1); SB();
    mm(ac[0][1][0], fb[3], wr[10][0]); SB();
    mmw<5>(ac[0][0][0], fb[4], wr[10][2]); SB();
    lds_rd128<57344>(fb[1], xa_1); SB();
    mm(ac[0][0][1], fb[4], wr[10][3]); SB();
    mmw<4>(ac[0][1][1], fb[5], wr[10][3]); SB();
    lds_rd128<58368>(fb[2], xa_1); SB();
    mm(ac[0][1][0], fb[5], wr[10][2]); SB();
    mm(ac[0][0][0], fb[6], wr[11][0]); SB();
    lds_rd128<59392>(fb[3], xa_1); SB();
    mm(ac[0][0][1], fb[6], wr[11][1]); SB();
    mmw<4>(ac[0][1][1], fb[7], wr[11][1]); SB();
    lds_rd128<60416>(fb[4], xa_1); SB();
    mm(ac[0][1][0], fb[7], wr[11][0]); SB();
    mm(ac[0][0][0], fb[8], wr[11][2]); SB();
    lds_rd128<61440>(fb[5], xa_1); SB();
    mm(ac[0][0][1], fb[8], wr[11][3]); SB();
    mmw<4>(ac[0][1][1], fb[0], wr[11][3]); SB();
    lds_rd128<62464>(fb[6], xa_1); SB();
    mm(ac[0][1][0], fb[0], wr[11][2]); SB();
    lds_rd32(fl, lf_rd); SB();
    mm(ac[0][0][0], fb[1], wr[12][0]); SB();
    lds_rd128<63488>(fb[7], xa_1); SB();
    mm(ac[0][0][1], fb[1], wr[12][1]); SB();
    mmw<5>(ac[0][1][1], fb[2], wr[12][1]); SB();
    lds_rd128<64512>(fb[8], xa_1); SB();
    mm(ac[0][1][0], fb[2], wr[12][0]); SB();
    mm(ac[0][0][0], fb[3], wr[12][2]); SB();
    lds_rd128<0>(fb[0], wa0_1); SB();
    mmchkw<24, 3>(ac[0][0][1], fb[3], wr[12][3], fl, lf_rd, bud); SB();
    lds_rd128<16384>(fb[1], xa_1); SB();
    mm(ac[0][1][1], fb[4], wr[12][3]); SB();
    lds_rd128<2048>(fb[2], wa0_1); SB();
    mm(ac[0][1][0], fb[4], wr[12][2]); SB();
    lds_rd128<17408>(fb[3], xa_1); SB();
    mm(ac[0][0][0], fb[5], wr[13][0]); SB();
    lds_rd128<0>(fb[4], wa1_1); SB();
    mm(ac[0][0][1], fb[5], wr[13][1]); SB();
    lds_rd128<18432>(fb[5], xa_1); SB();
    mm(ac[0][1][1], fb[6], wr[13][1]); SB();
    SIGQ(6); SB();
    mm(ac[0][1][0], fb[6], wr[13][0]); SB();
    lds_rd128<2048>(fb[6], wa1_1); SB();
    mm(ac[0][0][0], fb[7], wr[13][2]); SB();
    SIGN(); SB();
    mm(ac[0][0][1], fb[7], wr[13][3]); SB();
    lds_rd128<19456>(fb[7], xa_1); SB();
    mm(ac[0][1][1], fb[8], wr[13][3]); SB();
    mm(ac[0][1][0], fb[8], wr[13][2]); SB();
    lds_rd128<32768>(fb[8], wa0_0); SB();
    mmw<6>(ac[0][0][0], fb[1], fb[0]); SB();
    mm(ac[0][0][1], fb[1], fb[2]); SB();
    lds_rd128<20480>(fb[1], xa_1); SB();
    mmw<5>(ac[0][1][1], fb[3], fb[2]); SB();
    lds_rd128<34816>(fb[2], wa0_0); SB();
    mm(ac[0][1][0], fb[3], fb[0]); SB();
    lds_rd128<21504>(fb[0], xa_1); SB();
    mmw<5>(ac[0][0][0], fb[5], fb[4]); SB();
    lds_rd128<32768>(fb[3], wa1_0); SB();
    mm(ac[0][0][1], fb[5], fb[6]); SB();
    lds_rd128<22528>(fb[5], xa_1); SB();
    mmw<6>(ac[0][1][1], fb[7], fb[6]); SB();
    lds_rd128<34816>(fb[6], wa1_0); SB();
    mm(ac[0][1][0], fb[7], fb[4]); SB();
    lds_rd128<23552>(fb[4], xa_1); SB();
    mmw<6>(ac[0][0][0], fb[1], fb[8]); SB();
    lds_rd128<24576>(fb[7], xa_1); SB();
    mmw<6>(ac[0][0][1], fb[1], fb[2]); SB();
    lds_rd128<25600>(fb[1], xa_1); SB();
    mmw<6>(ac[0][1][1], fb[0], fb[2]); SB();
    lds_rd128<26624>(fb[2], xa_1); SB();
    mm(ac[0][1][0], fb[0], fb[8]); SB();
    lds_rd128<27648>(fb[0], xa_1); SB();
    mmw<6>(ac[0][0][0], fb[5], fb[3]); SB();
    lds_rd128<28672>(fb[8], xa_1); SB();
    mmw<6>(ac[0][0][1], fb[5], fb[6]); SB();
    mmw<5>(ac[0][1][1], fb[4], fb[6]); SB();
    lds_rd128<29696>(fb[5], xa_1); SB();
    lds_rd32(fl, lf_rd); SB();
    mm(ac[0][1][0], fb[4], fb[3]); SB();
    mmw<6>(ac[1][0][0], fb[7], wr[8][0]); SB();
    lds_rd128<30720>(fb[6], xa_1); SB();
    mm(ac[1][0][1], fb[7], wr[8][1]); SB();
    mmw<6>(ac[1][1][1], fb[1], wr[8][1]); SB();
    lds_rd128<31744>(fb[3], xa_1); SB();
    mm(ac[1][1][0], fb[1], wr[8][0]); SB();
    EM0(0, 0); SB();
    mmchkw<25, 2>(ac[1][0][0], fb[2], wr[8][2], fl, lf_rd, bud); SB();
    lds_rd128<0>(fb[4], xa_0); SB();
    EM0(0, 1); SB();
    mm(ac[1][0][1], fb[2], wr[8][3]); SB();
    EM1(0, 0, 3, 4); SB();
    EM1(0, 1, 11, 12); SB();
    EM1(0, 0, 5, 6); SB();
    mm(ac[1][1][1], fb[0], wr[8][3]); SB();
    lds_rd128<1024>(fb[7], xa_0); SB();
    EM1(0, 1, 13, 14); SB();
    EM1(0, 0, 7, 15); SB();
    mm(ac[1][1][0], fb[0], wr[8][2]); SB();
    EK(0); SB();
    EM2(); SB();
    EK(1); SB();
    mm(ac[1][0][0], fb[8], wr[9][0]); SB();
    lds_rd128<2048>(fb[1], xa_0); SB();
    ES0(); SB();
    ES1(); SB();
    mm(ac[1][0][1], fb[8], wr[9][1]); SB();
    mm(ac[1][1][1], fb[5], wr[9][1]); SB();
    lds_rd128<3072>(fb[2], xa_0); SB();
    EX(0); SB();
    EH(0, 0); SB();
    mm(ac[1][1][0], fb[5], wr[9][0]); SB();
    EH(0, 1); SB();
    EH(0, 2); SB();
    mmw<4>(ac[1][0][0], fb[6], wr[9][2]); SB();
    lds_rd128<4096>(fb[0], xa_0); SB();
    EH(0, 3); SB();
    ET(0); SB();
    mm(ac[1][0][1], fb[6], wr[9][3]); SB();
    ET(1); SB();
    ET(2); SB();
    EU(3); SB();
    mm(ac[1][1][1], fb[3], wr[9][3]); SB();
    lds_rd128<5120>(fb[8], xa_0); SB();
    mm(ac[1][1][0], fb[3], wr[9][2]); SB();
    SEND(0, 0); SB();
    mmw<4>(ac[1][0][0], fb[4], wr[10][0]); SB();
    lds_rd128<6144>(fb[5], xa_0); SB();
    EH(0, 4); SB();
    EH(0, 5); SB();
    mm(ac[1][0][1], fb[4], wr[10][1]); SB();
    EH(0, 6); SB();
    EH(0, 7); SB();
    ET(4); SB();
    mm(ac[1][1][1], fb[7], wr[10][1]); SB();
    lds_rd128<7168>(fb[6], xa_0); SB();
    ET(5); SB();
    ET(6); SB();
    mm(ac[1][1][0], fb[7], wr[10][0]); SB();
    EU(7); SB();
    mmw<4>(ac[1][0][0], fb[1], wr[10][2]); SB();
    lds_rd128<8192>(fb[3], xa_0); SB();
    SEND(0, 1); SB();
    mm(ac[1][0][1], fb[1], wr[10][3]); SB();
    mm(ac[1][1][1], fb[2], wr[10][3]); SB();
    lds_rd128<9216>(fb[4], xa_0); SB();
    mm(ac[1][1][0], fb[2], wr[10][2]); SB();
    mmw<4>(ac[1][0][0], fb[0], wr[11][0]); SB();
    lds_rd128<10240>(fb[7], xa_0); SB();
    mm(ac[1][0][1], fb[0], wr[11][1]); SB();
    mm(ac[1][1][1], fb[8], wr[11][1]); SB();
    lds_rd128<11264>(fb[1], xa_0); SB();
    mm(ac[1][1][0], fb[8], wr[11][0]); SB();
    mmw<4>(ac[1][0][0], fb[5], wr[11][2]); SB();
    lds_rd128<12288>(fb[2], xa_0); SB();
    mm(ac[1][0][1], fb[5], wr[11][3]); SB();
    mm(ac[1][1][1], fb[6], wr[11][3]); SB();
    lds_rd128<13312>(fb[0], xa_0); SB();
    mm(ac[1][1][0], fb[6], wr[11][2]); SB();
    lds_rd32(fl, lf_rd); SB();
    mmw<5>(ac[1][0][0], fb[3], wr[12][0]); SB();
    lds_rd128<14336>(fb[8], xa_0); SB();
    mm(ac[1][0][1], fb[3], wr[12][1]); SB();
    mm(ac[1][1][1], fb[4], wr[12][1]); SB();
    lds_rd128<15360>(fb[5], xa_0); SB();
    mm(ac[1][1][0], fb[4], wr[12][0]); SB();
    mmw<5>(ac[1][0][0], fb[7], wr[12][2]); SB();
    lds_rd128<0>(fb[6], wa0_1); SB();
    mmchkw<26, 3>(ac[1][0][1], fb[7], wr[12][3], fl, lf_rd, bud); SB();
    lds_rd128<0>(fb[3], xa_2); SB();
    mm(ac[1][1][1], fb[1], wr[12][3]); SB();
    lds_rd128<2048>(fb[4], wa0_1); SB();
    mm(ac[1][1][0], fb[1], wr[12][2]); SB();
    lds_rd128<1024>(fb[7], xa_2); SB();
    mm(ac[1][0][0], fb[2], wr[13][0]); SB();
    lds_rd128<0>(fb[1], wa1_1); SB();
    mm(ac[1][0][1], fb[2], wr[13][1]); SB();
    lds_rd128<2048>(fb[2], xa_2); SB();
    mm(ac[1][1][1], fb[0], wr[13][1]); SB();
    mm(ac[1][1][0], fb[0], wr[13][0]); SB();
    lds_rd128<2048>(fb[0], wa1_1); SB();
    mmw<7>(ac[1][0][0], fb[8], wr[13][2]); SB();
    mm(ac[1][0][1], fb[8], wr[13][3]); SB();
    lds_rd128<3072>(fb[8], xa_2); SB();
    mm(ac[1][1][1], fb[5], wr[13][3]); SB();
    mm(ac[1][1][0], fb[5], wr[13][2]); SB();
    lds_rd128<32768>(fb[5], wa0_0); SB();
    mmw<6>(ac[1][0][0], fb[3], fb[6]); SB();
    mm(ac[1][0][1], fb[3], fb[4]); SB();
    lds_rd128<4096>(fb[3], xa_2); SB();
    mmw<5>(ac[1][1][1], fb[7], fb[4]); SB();
    lds_rd128<34816>(fb[4], wa0_0); SB();
    mm(ac[1][1][0], fb[7], fb[6]); SB();
    lds_rd128<5120>(fb[6], xa_2); SB();
    mmw<5>(ac[1][0][0], fb[2], fb[1]); SB();
    lds_rd128<32768>(fb[7], wa1_0); SB();
    mm(ac[1][0][1], fb[2], fb[0]); SB();
    lds_rd128<6144>(fb[2], xa_2); SB();
    mmw<6>(ac[1][1][1], fb[8], fb[0]); SB();
    lds_rd128<34816>(fb[0], wa1_0); SB();
    mm(ac[1][1][0], fb[8], fb[1]); SB();
    lds_rd128<7168>(fb[1], xa_2); SB();
    mmw<6>(ac[1][0][0], fb[3], fb[5]); SB();
    lds_rd128<8192>(fb[8], xa_2); SB();
    mmw<6>(ac[1][0][1], fb[3], fb[4]); SB();
    lds_rd128<9216>(fb[3], xa_2); SB();
    mmw<6>(ac[1][1][1], fb[6], fb[4]); SB();
    lds_rd128<10240>(fb[4], xa_2); SB();
    mm(ac[1][1][0], fb[6], fb[5]); SB();
    lds_rd128<11264>(fb[5], xa_2); SB();
    mmw<6>(ac[1][0][0], fb[2], fb[7]); SB();
    lds_rd128<12288>(fb[6], xa_2); SB();
    mmw<6>(ac[1][0][1], fb[2], fb[0]); SB();
    mmw<5>(ac[1][1][1], fb[1], fb[0]); SB();
    lds_rd128<13312>(fb[2], xa_2); SB();
    lds_rd32(fl, lf_rd); SB();
    mm(ac[1][1][0], fb[1], fb[7]); SB();
    mmw<6>(ac[2][0][0], fb[8], wr[8][0]); SB();
    lds_rd128<14336>(fb[0], xa_2); SB();
    mm(ac[2][0][1], fb[8], wr[8][1]); SB();
    mmw<6>(ac[2][1][1], fb[3], wr[8][1]); SB();
    lds_rd128<15360>(fb[1], xa_2); SB();
    mm(ac[2][1][0], fb[3], wr[8][0]); SB();
    EM0(1, 0); SB();
    mmchkw<27, 2>(ac[2][0][0], fb[4], wr[8][2], fl, lf_rd, bud); SB();
    lds_rd128<49152>(fb[7], xa_0); SB();
    EM0(1, 1); SB();
    mm(ac[2][0][1], fb[4], wr[8][3]); SB();
    EM1(1, 0, 3, 4); SB();
    EM1(1, 1, 11, 12); SB();
    EM1(1, 0, 5, 6); SB();
    mm(ac[2][1][1], fb[5], wr[8][3]); SB();
    lds_rd128<50176>(fb[8], xa_0); SB();
    EM1(1, 1, 13, 14); SB();
    EM1(1, 0, 7, 15); SB();
    mm(ac[2][1][0], fb[5], wr[8][2]); SB();
    EK(0); SB();
    EM2(); SB();
    EK(1); SB();
    mm(ac[2][0][0], fb[6], wr[9][0]); SB();
    lds_rd128<51200>(fb[3], xa_0); SB();
    ES0(); SB();
    ES1(); SB();
    mm(ac[2][0][1], fb[6], wr[9][1]); SB();
    mm(ac[2][1][1], fb[2], wr[9][1]); SB();
    lds_rd128<52224>(fb[4], xa_0); SB();
    EX(1); SB();
    EH(1, 0); SB();
    mm(ac[2][1][0], fb[2], wr[9][0]); SB();
    EH(1, 1); SB();
    EH(1, 2); SB();
    mmw<4>(ac[2][0][0], fb[0], wr[9][2]); SB();
    lds_rd128<53248>(fb[5], xa_0); SB();
    EH(1, 3); SB();
    ET(0); SB();
    mm(ac[2][0][1], fb[0], wr[9][3]); SB();
    ET(1); SB();
    ET(2); SB();
    EU(3); SB();
    mm(ac[2][1][1], fb[1], wr[9][3]); SB();
    lds_rd128<54272>(fb[6], xa_0); SB();
    mm(ac[2][1][0], fb[1], wr[9][2]); SB();
    SEND(1, 0); SB();
    mmw<4>(ac[2][0][0], fb[7], wr[10][0]); SB();
    lds_rd128<55296>(fb[2], xa_0); SB();
    EH(1, 4); SB();
    EH(1, 5); SB();
    mm(ac[2][0][1], fb[7], wr[10][1]); SB();
    EH(1, 6); SB();
    EH(1, 7); SB();
    ET(4); SB();
    mm(ac[2][1][1], fb[8], wr[10][1]); SB();
    lds_rd128<56320>(fb[0], xa_0); SB();
    ET(5); SB();
    ET(6); SB();
    mm(ac[2][1][0], fb[8], wr[10][0]); SB();
    EU(7); SB();
    mmw<4>(ac[2][0][0], fb[3], wr[10][2]); SB();
    lds_rd128<57344>(fb[1], xa_0); SB();
    SEND(1, 1); SB();
    mm(ac[2][0][1], fb[3], wr[10][3]); SB();
    mm(ac[2][1][1], fb[4], wr[10][3]); SB();
    lds_rd128<58368>(fb[7], xa_0); SB();
    mm(ac[2][1][0], fb[4], wr[10][2]); SB();
    mmw<4>(ac[2][0][0], fb[5], wr[11][0]); SB();
    lds_rd128<59392>(fb[8], xa_0); SB();
    mm(ac[2][0][1], fb[5], wr[11][1]); SB();
    mm(ac[2][1][1], fb[6], wr[11][1]); SB();
    lds_rd128<60416>(fb[3], xa_0); SB();
    mm(ac[2][1][0], fb[6], wr[11][0]); SB();
    mmw<4>(ac[2][0][0], fb[2], wr[11][2]); SB();
    lds_rd128<61440>(fb[4], xa_0); SB();
    mm(ac[2][0][1], fb[2], wr[11][3]); SB();
    mm(ac[2][1][1], fb[0], wr[11][3]); SB();
    lds_rd128<62464>(fb[5], xa_0); SB();
    mm(ac[2][1][0], fb[0], wr[11][2]); SB();
    lds_rd32(fl, lf_rd); SB();
    mmw<5>(ac[2][0][0], fb[1], wr[12][0]); SB();
    lds_rd128<63488>(fb[6], xa_0); SB();
    mm(ac[2][0][1], fb[1], wr[12][1]); SB();
    mm(ac[2][1][1], fb[7], wr[12][1]); SB();
    lds_rd128<64512>(fb[2], xa_0); SB();
    mm(ac[2][1][0], fb[7], wr[12][0]); SB();
    mmw<5>(ac[2][0][0], fb[8], wr[12][2]); SB();
    lds_rd128<0>(fb[0], wa0_1); SB();
    mmchkw<28, 3>(ac[2][0][1], fb[8], wr[12][3], fl, lf_rd, bud); SB();
    lds_rd128<32768>(fb[1], xa_1); SB();
    mm(ac[2][1][1], fb[3], wr[12][3]); SB();
    lds_rd128<2048>(fb[7], wa0_1); SB();
    mm(ac[2][1][0], fb[3], wr[12][2]); SB();
    lds_rd128<33792>(fb[8], xa_1); SB();
    mm(ac[2][0][0], fb[4], wr[13][0]); SB();
    lds_rd128<0>(fb[3], wa1_1); SB();
    mm(ac[2][0][1], fb[4], wr[13][1]); SB();
    lds_rd128<34816>(fb[4], xa_1); SB();
    mm(ac[2][1][1], fb[5], wr[13][1]); SB();
    mm(ac[2][1][0], fb[5], wr[13][0]); SB();
    lds_rd128<2048>(fb[5], wa1_1); SB();
    mmw<7>(ac[2][0][0], fb[6], wr[13][2]); SB();
    mm(ac[2][0][1], fb[6], wr[13][3]); SB();
    lds_rd128<35840>(fb[6], xa_1); SB();
    mm(ac[2][1][1], fb[2], wr[13][3]); SB();
    mm(ac[2][1][0], fb[2], wr[13][2]); SB();
    lds_rd128<32768>(fb[2], wa0_0); SB();
    mmw<6>(ac[2][0][0], fb[1], fb[0]); SB();
    mm(ac[2][0][1], fb[1], fb[7]); SB();
    lds_rd128<36864>(fb[1], xa_1); SB();
    mmw<5>(ac[2][1][1], fb[8], fb[7]); SB();
    lds_rd128<34816>(fb[7], wa0_0); SB();
    mm(ac[2][1][0], fb[8], fb[0]); SB();
    lds_rd128<37888>(fb[0], xa_1); SB();
    mmw<5>(ac[2][0][0], fb[4], fb[3]); SB();
    lds_rd128<32768>(fb[8], wa1_0); SB();
    mm(ac[2][0][1], fb[4], fb[5]); SB();
    lds_rd128<38912>(fb[4], xa_1); SB();
    mmw<6>(ac[2][1][1], fb[6], fb[5]); SB();
    lds_rd128<34816>(fb[5], wa1_0); SB();
    mm(ac[2][1][0], fb[6], fb[3]); SB();
    lds_rd128<39936>(fb[3], xa_1); SB();
    mmw<6>(ac[2][0][0], fb[1], fb[2]); SB();
    lds_rd128<40960>(fb[6], xa_1); SB();
    mmw<6>(ac[2][0][1], fb[1], fb[7]); SB();
    lds_rd128<41984>(fb[1], xa_1); SB();
    mmw<6>(ac[2][1][1], fb[0], fb[7]); SB();
    lds_rd128<43008>(fb[7], xa_1); SB();
    mm(ac[2][1][0], fb[0], fb[2]); SB();
    lds_rd128<44032>(fb[0], xa_1); SB();
    mmw<6>(ac[2][0][0], fb[4], fb[8]); SB();
    lds_rd128<45056>(fb[2], xa_1); SB();
    mmw<6>(ac[2][0][1], fb[4], fb[5]); SB();
    mmw<5>(ac[2][1][1], fb[3], fb[5]); SB();
    lds_rd128<46080>(fb[4], xa_1); SB();
    lds_rd32(fl, lf_rd); SB();
    mm(ac[2][1][0], fb[3], fb[8]); SB();
    mmw<6>(ac[3][0][0], fb[6], wr[6][0]); SB();
    lds_rd128<47104>(fb[5], xa_1); SB();
    mm(ac[3][0][1], fb[6], wr[6][1]); SB();
    mmw<6>(ac[3][1][1], fb[1], wr[6][1]); SB();
    lds_rd128<48128>(fb[3], xa_1); SB();
    mm(ac[3][1][0], fb[1], wr[6][0]); SB();
    EM0(2, 0); SB();
    EM0(2, 1); SB();
    mmchkw<29, 2>(ac[3][0][0], fb[7], wr[6][2], fl, lf_rd, bud); SB();
    lds_rd128<16384>(fb[8], xa_0); SB();
    EM1(2, 0, 3, 4); SB();
    EM1(2, 1, 11, 12); SB();
    mm(ac[3][0][1], fb[7], wr[6][3]); SB();
    EM1(2, 0, 5, 6); SB();
    EM1(2, 1, 13, 14); SB();
    EM1(2, 0, 7, 15); SB();
    EK(0); SB();
    mm(ac[3][1][1], fb[0], wr[6][3]); SB();
    lds_rd128<17408>(fb[6], xa_0); SB();
    EM2(); SB();
    EK(1); SB();
    ES0(); SB();
    mm(ac[3][1][0], fb[0], wr[6][2]); SB();
    ES1(); SB();
    mm(ac[3][0][0], fb[2], wr[7][0]); SB();
    lds_rd128<18432>(fb[1], xa_0); SB();
    EX(2); SB();
    EH(2, 0); SB();
    EH(2, 1); SB();
    mm(ac[3][0][1], fb[2], wr[7][1]); SB();
    EH(2, 2); SB();
    EH(2, 3); SB();
    ET(0); SB();
    ET(1); SB();
    mm(ac[3][1][1], fb[4], wr[7][1]); SB();
    lds_rd128<19456>(fb[7], xa_0); SB();
    ET(2); SB();
    EU(3); SB();
    mm(ac[3][1][0], fb[4], wr[7][0]); SB();
    SEND(2, 0); SB();
    mmw<4>(ac[3][0][0], fb[5], wr[7][2]); SB();
    lds_rd128<20480>(fb[0], xa_0); SB();
    EH(2, 4); SB();
    EH(2, 5); SB();
    EH(2, 6); SB();
    mm(ac[3][0][1], fb[5], wr[7][3]); SB();
    EH(2, 7); SB();
    ET(4); SB();
    ET(5); SB();
    ET(6); SB();
    mm(ac[3][1][1], fb[3], wr[7][3]); SB();
    lds_rd128<21504>(fb[2], xa_0); SB();
    EU(7); SB();
    mm(ac[3][1][0], fb[3], wr[7][2]); SB();
    SEND(2, 1); SB();
    mmw<4>(ac[3][0][0], fb[8], wr[8][0]); SB();
    lds_rd128<22528>(fb[4], xa_0); SB();
    mm(ac[3][0][1], fb[8], wr[8][1]); SB();
    asm volatile(STOP ::: "memory"); SB();
    mm(ac[3][1][1], fb[6], wr[8][1]); SB();
    lds_rd128<23552>(fb[5], xa_0); SB();
    mm(ac[3][1][0], fb[6], wr[8][0]); SB();
    mmw<4>(ac[3][0][0], fb[1], wr[8][2]); SB();
    lds_rd128<24576>(fb[3], xa_0); SB();
    mm(ac[3][0][1], fb[1], wr[8][3]); SB();
    mm(ac[3][1][1], fb[7], wr[8][3]); SB();
    lds_rd128<25600>(fb[8], xa_0); SB();
    mm(ac[3][1][0], fb[7], wr[8][2]); SB();
    mmw<4>(ac[3][0][0], fb[0], wr[9][0]); SB();
    lds_rd128<26624>(fb[6], xa_0); SB();
    mm(ac[3][0][1], fb[0], wr[9][1]); SB();
    mm(ac[3][1][1], fb[2], wr[9][1]); SB();
    lds_rd128<27648>(fb[1], xa_0); SB();
    mm(ac[3][1][0], fb[2], wr[9][0]); SB();
    mmw<4>(ac[3][0][0], fb[4], wr[9][2]); SB();
    lds_rd128<28672>(fb[7], xa_0); SB();
    mm(ac[3][0][1], fb[4], wr[9][3]); SB();
    mm(ac[3][1][1], fb[5], wr[9][3]); SB();
    lds_rd128<29696>(fb[0], xa_0); SB();
    lds_rd32(fl, lf_rd); SB();
    mm(ac[3][1][0], fb[5], wr[9][2]); SB();
    mmw<5>(ac[3][0][0], fb[3], wr[10][0]); SB();
    lds_rd128<30720>(fb[2], xa_0); SB();
    mm(ac[3][0][1], fb[3], wr[10][1]); SB();
    mm(ac[3][1][1], fb[8], wr[10][1]); SB();
    lds_rd128<31744>(fb[4], xa_0); SB();
    mm(ac[3][1][0], fb[8], wr[10][0]); SB();
    mmchkw<30, 2>(ac[3][0][0], fb[6], wr[10][2], fl, lf_rd, bud); SB();
    lds_rd128<49152>(fb[5], xa_1); SB();
    mm(ac[3][0][1], fb[6], wr[10][3]); SB();
    mm(ac[3][1][1], fb[1], wr[10][3]); SB();
    lds_rd128<50176>(fb[3], xa_1); SB();
    mm(ac[3][1][0], fb[1], wr[10][2]); SB();
    mm(ac[3][0][0], fb[7], wr[11][0]); SB();
    lds_rd128<51200>(fb[8], xa_1); SB();
    mm(ac[3][0][1], fb[7], wr[11][1]); SB();
    mm(ac[3][1][1], fb[0], wr[11][1]); SB();
    lds_rd128<52224>(fb[6], xa_1); SB();
    mm(ac[3][1][0], fb[0], wr[11][0]); SB();
    mmw<4>(ac[3][0][0], fb[2], wr[11][2]); SB();
    lds_rd128<53248>(fb[1], xa_1); SB();
    mm(ac[3][0][1], fb[2], wr[11][3]); SB();
    mm(ac[3][1][1], fb[4], wr[11][3]); SB();
    lds_rd128<54272>(fb[7], xa_1); SB();
    mm(ac[3][1][0], fb[4], wr[11][2]); SB();
    mmw<4>(ac[3][0][0], fb[5], wr[12][0]); SB();
    lds_rd128<55296>(fb[0], xa_1); SB();
    RV_LD(0, ""); SB();
    mm(ac[3][0][1], fb[5], wr[12][1]); SB();
    mm(ac[3][1][1], fb[3], wr[12][1]); SB();
    lds_rd128<56320>(fb[2], xa_1); SB();
    mm(ac[3][1][0], fb[3], wr[12][0]); SB();
    mmw<4>(ac[3][0][0], fb[8], wr[12][2]); SB();
    lds_rd128<0>(fb[4], wa0_1); SB();
    mm(ac[3][0][1], fb[8], wr[12][3]); SB();
    lds_rd128<57344>(fb[5], xa_1); SB();
    mm(ac[3][1][1], fb[6], wr[12][3]); SB();
    lds_rd128<2048>(fb[3], wa0_1); SB();
    mm(ac[3][1][0], fb[6], wr[12][2]); SB();
    lds_rd128<58368>(fb[8], xa_1); SB();
    mmw<6>(ac[3][0][0], fb[1], wr[13][0]); SB();
    lds_rd128<0>(fb[6], wa1_1); SB();
    mm(ac[3][0][1], fb[1], wr[13][1]); SB();
    lds_rd128<59392>(fb[1], xa_1); SB();
    mm(ac[3][1][1], fb[7], wr[13][1]); SB();
    mm(ac[3][1][0], fb[7], wr[13][0]); SB();
    lds_rd128<2048>(fb[7], wa1_1); SB();
    mmw<7>(ac[3][0][0], fb[0], wr[13][2]); SB();
    mm(ac[3][0][1], fb[0], wr[13][3]); SB();
    lds_rd128<60416>(fb[0], xa_1); SB();
    mm(ac[3][1][1], fb[2], wr[13][3]); SB();
    lds_rd32(f2, d2_rd); SB();
    mm(ac[3][1][0], fb[2], wr[13][2]); SB();
    lds_rd128<32768>(fb[2], wa0_0); SB();
    mmw<7>(ac[3][0][0], fb[5], fb[4]); SB();
    RV_LD(1, ""); SB();
    mm(ac[3][0][1], fb[5], fb[3]); SB();
    lds_rd128<61440>(fb[5], xa_1); SB();
    mmw<6>(ac[3][1][1], fb[8], fb[3]); SB();
    lds_rd128<34816>(fb[3], wa0_0); SB();
    mm(ac[3][1][0], fb[8], fb[4]); SB();
    lds_rd128<62464>(fb[4], xa_1); SB();
    mmw<6>(ac[3][0][0], fb[1], fb[6]); SB();
    lds_rd128<32768>(fb[8], wa1_0); SB();
    asm volatile("s_waitcnt lgkmcnt(5)" : "+v"(fb[0]), "+v"(f2)::"memory"); SB();
    chk<1>(f2, d2_rd, bud); SB();
    D_RD(0); SB();
    D_RD(1); SB();
    D_RD(2); SB();
    D_RD(3); SB();
    G_RD(0); SB();
    G_RD(1); SB();
    mm(ac[3][0][1], fb[1], fb[7]); SB();
    lds_rd128<63488>(fb[1], xa_1); SB();
    mm(ac[3][1][1], fb[0], fb[7]); SB();
    lds_rd128<34816>(fb[7], wa1_0); SB();
    mm(ac[3][1][0], fb[0], fb[6]); SB();
    lds_rd128<64512>(fb[0], xa_1); SB();
    RV_LD(2, ""); SB();
    mmw<12>(ac[3][0][0], fb[5], fb[2]); SB();
    mmw<11>(ac[3][0][1], fb[5], fb[3]); SB();
    mmw<10>(ac[3][1][1], fb[4], fb[3]); SB();
    mm(ac[3][1][0], fb[4], fb[2]); SB();
    mmw<2>(ac[3][0][0], fb[1], fb[8]); SB();
    mmw<1>(ac[3][0][1], fb[1], fb[7]); SB();
    mmw<0>(ac[3][1][1], fb[0], fb[7]); SB();
    mm(ac[3][1][0], fb[0], fb[8]); SB();
    asm volatile(".subsection 1\n7777:\n\ts_setpc_b64 vcc\n\t.subsection 0");
    asm volatile("s_nop 7" : "+v"(ac[3][0][0]), "+v"(ac[3][0][1]), "+v"(ac[3][1][0]), "+v"(ac[3][1][1])::"memory");
    const float om[2] = {rbf(1.0f - gs[0]), rbf(1.0f - gs[1])};
    gs[0] = h ? -gs[0] : gs[0];
    gs[1] = h ? -gs[1] : gs[1];
    float y[16];
#pragma unroll
    for (int p = 0; p < 8; ++p) {
      const unsigned o = dq[p >> 1][p & 1];
      const f32x2 v = f32x2{AV(3, 2 * p), AV(3, 2 * p + 1)} * gs[p >> 2] + f32x2{lo2f(o), hi2f(o)} * om[p >> 2];
      y[2 * p] = v[0];
      y[2 * p + 1] = v[1];
    }
    asm volatile("" : "+v"(oo), "+s"(out), "+v"(gs[0]), "+v"(gs[1]));
    auto strip = [&](u32x4 (&r)[2]) {
      const u32x4 t = {tb[0] ^ 0x40004u, tb[1] ^ 0x40004u, tb[2] ^ 0x40004u, tb[3] ^ 0x40004u};
      r[0] ^= t;
      r[1] ^= t;
      const unsigned o = ((r[0].x | r[0].y | r[0].z | r[1].x | r[1].y | r[1].z) & 0x30003u) | ((r[0].w | r[1].w) & 0x30000u);
      const unsigned a = r[0].x & r[0].y & r[0].z & r[0].w & r[1].x & r[1].y & r[1].z & r[1].w & 0x40004u;
      return (o | a) == 0x40004u;
    };
#define RV_FIN(S, Y)                                                                                                     \
  asm volatile("s_waitcnt vmcnt(%18)" : "+v"(rv[S][0]), "+v"(rv[S][1]), "+v"(y[0]), "+v"(y[1]), "+v"(y[2]), "+v"(y[3]), "+v"(y[4]), "+v"(y[5]), \
               "+v"(y[6]), "+v"(y[7]), "+v"(y[8]), "+v"(y[9]), "+v"(y[10]), "+v"(y[11]), "+v"(y[12]), "+v"(y[13]), "+v"(y[14]), "+v"(y[15])     \
               : "i"(Y) : "memory");                                                                                     \
  if (__builtin_expect(!__all(strip(rv[S])), 0)) {                                                                       \
    _Pragma("clang loop unroll(disable)") for (int it = 0; it < SPIN_MAX; ++it) {                                        \
      RV_LD(S, "\n\ts_waitcnt vmcnt(0)");                                                                                \
      if (__all(strip(rv[S]))) break;                                                                                    \
    }                                                                                                                    \
  }                                                                                                                      \
  {                                                                                                                      \
    const float sc = __uint_as_float((((rv[S][0].w & 3u) | (rv[S][1].w & 3u) << 2) << 27) + ((S) == 2 ? 0xff800000u : 0x7f800000u)); \
    rv[S][0].w &= ~3u;                                                                                                   \
    rv[S][1].w &= ~3u;                                                                                                   \
    _Pragma("unroll") for (int p = 0; p < 8; ++p) {                                                                      \
      const float f = sc * gs[p >> 2];                                                                                   \
      unsigned o = rv[S][p >> 2][p & 3];                                                                                 \
      asm("v_fma_mix_f32 %0, %2, %3, %0 op_sel_hi:[1,0,0]\n\tv_fma_mix_f32 %1, %2, %3, %1 op_sel:[1,0,0] op_sel_hi:[1,0,0]" \
          : "+v"(y[2 * p]), "+v"(y[2 * p + 1]) : "v"(o), "v"(f));                                                        \
    }                                                                                                                    \
  }
    RV_FIN(0, 4);
    RV_FIN(1, 2);
    RV_FIN(2, 0);
    const unsigned idx = (4095u - (__builtin_amdgcn_readfirstlane(f2) & 0xfffu)) & (S_ - 1);
    if (__builtin_expect(idx >> 5 == (unsigned)(ti * 4 + wid), 0)) {
      SPIN(d_rd, 1);
      u32x2 dn_[2];
      asm volatile("ds_read_b64 %0, %2\n\tds_read_b64 %1, %2 offset:32\n\ts_waitcnt lgkmcnt(0)" : "=&v"(dn_[0]), "=&v"(dn_[1]) : "v"(sb + RING + L_UPD + fq * 8) : "memory");
#pragma unroll
      for (int p = 0; p < 8; ++p)
        if ((idx & 31u) == (unsigned)((p >> 2) * 16 + fr)) {
          const unsigned o = dq[p >> 1][p & 1], n = dn_[p >> 1 & 1][p & 1];
          y[2 * p] += om[p >> 2] * (lo2f(n) - lo2f(o));
          y[2 * p + 1] += om[p >> 2] * (hi2f(n) - hi2f(o));
        }
    }
#pragma unroll
    for (int mi = 0; mi < 2; ++mi) {
      const float* z = y + mi * 8;
      const auto s0 = __builtin_amdgcn_permlane16_swap(pk2(z[0], z[1]), pk2(z[4], z[5]), false, false);
      const auto s1 = __builtin_amdgcn_permlane16_swap(pk2(z[2], z[3]), pk2(z[6], z[7]), false, false);
      asm volatile("global_store_dwordx4 %0, %1, %2\n\ts_nop 1" ::"v"(oo + mi * (16u * D_ * 2)), "v"(u32x4{s0[0], s1[0], s0[1], s1[1]}), "s"(out) : "memory");
    }
  }
}

extern "C" void kernel_launch(void* const* d_in, const int* in_sizes, int n_in, void* d_out, int out_size,
                              void* d_ws, size_t ws_size, hipStream_t stream) {
  const u16* x = (const u16*)d_in[0];
  const u16* wsre = (const u16*)d_in[1];
  const u16* wsim = (const u16*)d_in[2];
  const u16* wdre = (const u16*)d_in[3];
  const u16* wdim = (const u16*)d_in[4];
  const u16* w_r = (const u16*)d_in[6];
  const u16* w_gate = (const u16*)d_in[7];
  const u16* w_up = (const u16*)d_in[8];
  const u16* w_down = (const u16*)d_in[9];
  const u16* gate_w = (const u16*)d_in[10];
  u16* out = (u16*)d_out;
  fused_k<<<256, 512, 0, stream>>>(wsre, wsim, x, w_r, gate_w, wdre, wdim, w_gate, w_up, w_down, (char*)d_ws, out);
}
```

```cpp
#include <hip/hip_runtime.h>
#include <hip/hip_bf16.h>
#include <stdint.h>
#include <type_traits>

using bf16 = __hip_bfloat16;
using bf16x8 = __attribute__((ext_vector_type(8))) short;
using f32x4 = __attribute__((ext_vector_type(4))) float;
using f32x2 = __attribute__((ext_vector_type(2))) float;
using u32x4 = __attribute__((ext_vector_type(4))) unsigned;
using u32x2 = __attribute__((ext_vector_type(2))) unsigned;
typedef __attribute__((ext_vector_type(2))) __bf16 bf16x2;
typedef unsigned short u16;
typedef unsigned long long u64;
typedef __attribute__((address_space(3))) unsigned lds_u32;

constexpr int S_ = 2048, D_ = 512, F_ = 1792;

__device__ __forceinline__ float bf2f(u16 v) { return __uint_as_float(((unsigned)v) << 16); }
__device__ __forceinline__ u16 f2bf(float f) {
  bf16 h = __float2bfloat16(f);
  return *reinterpret_cast<u16*>(&h);
}
__device__ __forceinline__ float rbf(float f) { return bf2f(f2bf(f)); }
__device__ __forceinline__ unsigned pk2(float a, float b) {
  return __builtin_bit_cast(unsigned, __builtin_convertvector(f32x2{a, b}, bf16x2));
}
__device__ __forceinline__ float lo2f(unsigned v) { return __uint_as_float(v << 16); }
__device__ __forceinline__ float hi2f(unsigned v) { return __uint_as_float(v & 0xffff0000u); }
__device__ __forceinline__ float sigmoidf_(float v) { return 1.0f / (1.0f + __expf(-v)); }

template <int CTRL>
__device__ __forceinline__ unsigned dpp(unsigned v) { return __builtin_amdgcn_update_dpp(0, v, CTRL, 0xf, 0xf, false); }
template <int CTRL>
__device__ __forceinline__ float dpp_add(float v) { return v + __uint_as_float(dpp<CTRL>(__float_as_uint(v))); }
constexpr int DPP_X1 = 0xb1, DPP_X2 = 0x4e, DPP_HM = 0x141, DPP_M = 0x140;
__device__ __forceinline__ unsigned row_max(unsigned v) {
  v = max(v, dpp<DPP_X1>(v));
  v = max(v, dpp<DPP_X2>(v));
  v = max(v, dpp<DPP_HM>(v));
  return max(v, dpp<DPP_M>(v));
}
__device__ __forceinline__ float sw32(float a, float b) {
  const auto r = __builtin_amdgcn_permlane32_swap(__float_as_uint(a), __float_as_uint(b), false, false);
  return __uint_as_float(r[0]) + __uint_as_float(r[1]);
}
__device__ __forceinline__ float sw16(float a, float b) {
  const auto r = __builtin_amdgcn_permlane16_swap(__float_as_uint(a), __float_as_uint(b), false, false);
  return __uint_as_float(r[0]) + __uint_as_float(r[1]);
}
__device__ __forceinline__ unsigned wave_max(unsigned v) {
  v = row_max(v);
  const auto r = __builtin_amdgcn_permlane16_swap(v, v, false, false);
  v = max(r[0], r[1]);
  const auto q = __builtin_amdgcn_permlane32_swap(v, v, false, false);
  return max(q[0], q[1]);
}

#define WAIT_V(n) asm volatile("s_waitcnt vmcnt(%0)" ::"i"(n) : "memory")
#define WAIT_L(n) asm volatile("s_waitcnt lgkmcnt(%0)" ::"i"(n) : "memory")
#define SB() __builtin_amdgcn_sched_barrier(0)

constexpr int A_B = 128 * 64 * 2;
constexpr int RING = 155648;
constexpr int SCR = 8192;
constexpr int SPIN_MAX = 1 << 16;
constexpr int L_LF = 0;
constexpr int L_CP = 1024;
constexpr int L_RG = 2048;
constexpr int L_G = 4096;
constexpr int L_FLAG = 256;
constexpr int L_H = 4608;
constexpr int L_UPD = 4736;
constexpr int L_RF = 4992;
constexpr int L_X = 7168;

constexpr size_t W_AB = 0;
constexpr size_t W_XB = (size_t)4 << 20;
constexpr size_t W_E = (size_t)16 << 20;
constexpr size_t W_BEST = W_E + 4096;
constexpr size_t W_SEEN = W_E + 8192;
constexpr size_t W_KEY = W_E + 16384;
constexpr size_t W_PA = W_E + 65536;
constexpr size_t W_PB = W_E + 524288;
constexpr unsigned C_SEEN = W_SEEN - W_E, C_BEST = W_BEST - W_E, C_KEY = W_KEY - W_E, C_PA = W_PA - W_E, C_PB = W_PB - W_E;
constexpr int PSLEEP = 4, LSLEEP = 1, GSLEEP = 1, FSTR = 64;
constexpr unsigned C_FL2 = 1u << 20;

#define STOP "7777:\n\ts_cbranch_execz 7777f\n"
#define PAGE_ "((. - _Z7fused_kPKtS0_S0_S0_S0_S0_S0_S0_S0_S0_PcPt + 1024) >> 12)"
#define PSTOP ".if " PAGE_ " != fk_page\n\t.set fk_page, " PAGE_ "\n" STOP "\t.endif\n\t"
#define TOUR() asm volatile("s_getpc_b64 vcc\n\ts_add_u32 vcc_lo, vcc_lo, 16\n\ts_addc_u32 vcc_hi, vcc_hi, 0\n\ts_mov_b64 exec, 0\n\ts_branch .Lfk_tour\n\ts_mov_b64 exec, -1" ::: "vcc", "scc", "memory")
template <int OFF>
__device__ __forceinline__ void lds_rd128(bf16x8& d, unsigned a) {
  asm volatile(PSTOP "ds_read_b128 %0, %1 offset:%2" : "=v"(d) : "v"(a), "i"(OFF) : "memory");
}
__device__ __forceinline__ void glds(unsigned lds, unsigned voff, const void* sbase) {
  asm volatile("s_mov_b32 m0, %0\n\ts_nop 0\n\tglobal_load_lds_dwordx4 %1, %2" ::"s"(lds), "v"(voff), "s"(sbase) : "memory");
}
#define GLDS4(POL) asm volatile(PSTOP "s_add_u32 m0, %0, %6\n\ts_nop 0\n\tglobal_load_lds_dwordx4 %1, %5" POL "\n\t"                        \
                                "global_load_lds_dwordx4 %2, %5 offset:1024" POL "\n\tglobal_load_lds_dwordx4 %3, %5 offset:2048" POL "\n\t" \
                                "global_load_lds_dwordx4 %4, %5 offset:3072" POL                                                       \
                                ::"s"(lds), "v"(v0), "v"(v1), "v"(v2), "v"(v3), "s"(sbase), "i"(OFF) : "scc", "memory")
template <int OFF>
__device__ __forceinline__ void glds4(unsigned lds, unsigned v0, unsigned v1, unsigned v2, unsigned v3, const void* sbase) { GLDS4(""); }
template <int OFF>
__device__ __forceinline__ void glds4_sc1(unsigned lds, unsigned v0, unsigned v1, unsigned v2, unsigned v3, const void* sbase) { GLDS4(" sc1"); }
template <int ADD, int MASK, int SH>
__device__ __forceinline__ const char* padd(const char* base, unsigned o) {
  const u64 bb = (u64)base;
  unsigned lo, hi;
  asm volatile(PSTOP "s_add_u32 %0, %4, %5\n\ts_and_b32 %0, %0, %6\n\ts_lshl_b32 %0, %0, %7\n\ts_add_u32 %0, %2, %0\n\ts_addc_u32 %1, %3, 0"
               : "=&s"(lo), "=s"(hi)
               : "s"((unsigned)bb), "s"((unsigned)(bb >> 32)), "s"(o), "i"(ADD), "i"(MASK), "i"(SH)
               : "scc");
  return (const char*)(((u64)hi << 32) | lo);
}
__device__ __forceinline__ void gate(u64& seen, u64 want, unsigned sh, unsigned fo, unsigned tag, const void* wc, unsigned& bud) {
  u64 t, w;
  unsigned v;
  asm volatile("s_lshl_b64 %[w], %[want], %[sh]\n\ts_andn2_b64 %[t], %[w], %[seen]\n\ts_cbranch_scc0 .Lsb%=\n"
               ".Lsa%=:\n\tglobal_load_dword %[v], %[fo], %[wc] sc1\n\ts_waitcnt vmcnt(0)\n\tv_cmp_eq_u32 %[t], %[tag], %[v]\n\t"
               "s_or_b64 %[seen], %[seen], %[t]\n\ts_andn2_b64 %[t], %[w], %[seen]\n\ts_cbranch_scc0 .Lsb%=\n\ts_sleep %[sl]\n\t"
               "s_sub_u32 %[bud], %[bud], 16\n\ts_cbranch_scc0 .Lsa%=\n\ts_mov_b32 %[bud], 0\n.Lsb%=:"
               : [seen] "+s"(seen), [t] "=&s"(t), [w] "=&s"(w), [v] "=&v"(v), [bud] "+s"(bud)
               : [want] "s"(want), [sh] "s"(sh), [fo] "v"(fo), [tag] "s"(tag), [wc] "s"(wc), [sl] "i"(GSLEEP)
               : "scc", "memory");
}
template <int S>
__device__ __forceinline__ const char* k1tile(const char* base, unsigned rot) { return padd<S * 128, 0x380, 0>(base, rot << 7); }
__device__ __forceinline__ void gst128_sc1(unsigned voff, u32x4 v, const void* sbase) {
  asm volatile("global_store_dwordx4 %0, %1, %2 sc1\n\ts_nop 1" ::"v"(voff), "v"(v), "s"(sbase) : "memory");
}

__device__ __forceinline__ void lds_rd32(unsigned& d, unsigned a) { asm volatile("ds_read_b32 %0, %1" : "=v"(d) : "v"(a) : "memory"); }
__device__ __forceinline__ void sigv(unsigned row, const void* tab, unsigned n4) {
  asm volatile("s_mov_b32 m0, %0\n\ts_nop 0\n\tglobal_load_lds_dword %2, %1" ::"s"(row), "s"(tab), "v"(n4) : "memory");
}
template <int N>
__device__ __forceinline__ void sig(unsigned row, const void* tab) {
  unsigned t;
  asm volatile(PSTOP "s_mov_b32 m0, %1\n\tv_mov_b32 %0, %3\n\tglobal_load_lds_dword %0, %2" : "=&v"(t) : "s"(row), "s"(tab), "i"(N * 4) : "memory");
}
template <int N>
__device__ __forceinline__ void spin(unsigned a, unsigned& bud) {
  unsigned t;
  asm volatile(".Lsa%=:\n\tds_read_b32 %0, %2\n\ts_waitcnt lgkmcnt(0)\n\tv_cmp_gt_u32 vcc, %3, %0\n\ts_cbranch_vccz .Lsb%=\n\t"
               "s_sub_u32 %1, %1, 1\n\ts_cbranch_scc0 .Lsa%=\n\ts_mov_b32 %1, 0\n.Lsb%=:"
               : "=&v"(t), "+s"(bud) : "v"(a), "i"(N) : "vcc", "scc", "memory");
}
#define FT0 ""
#define FT1 "\n"
#define FLAG_ASM "s_getreg_b32 %[ib], hwreg(HW_REG_IB_STS)\n\ts_and_b32 %[ib], %[ib], 0xc0000f\n\ts_or_b32 %[ib], %[ib], %[done]\n\t"   \
                 "s_cmp_gt_u32 %[ib], %[y]\n\ts_cbranch_scc1 .Lsk%=\n\ts_waitcnt vmcnt(%[y])\n\ts_mov_b64 exec, 0xffff\n\t"                     \
                 FT0 "global_store_dword %[fo], %[tag], %[wc] sc1\n\ts_mov_b64 exec, -1\n\ts_mov_b32 %[done], 64\n\t" FT1 ".Lsk%=:\n\t"
template <int Y>
__device__ __forceinline__ void tryflag(unsigned& done, unsigned fo, unsigned tag, const void* wc) {
  static_assert(Y < 16, "");
  unsigned ib;
  asm volatile(FLAG_ASM : [ib] "=&s"(ib), [done] "+s"(done) : [fo] "v"(fo), [tag] "v"(tag), [wc] "s"(wc), [y] "i"(Y) : "scc", "memory");
}
template <int N, int Y>
__device__ __forceinline__ void spinf(unsigned a, unsigned& bud, unsigned& done, unsigned fo, unsigned tag, const void* wc) {
  static_assert(Y < 16, "");
  unsigned t, ib;
  asm volatile(".Lsa%=:\n\tds_read_b32 %[t], %[a]\n\t" FLAG_ASM "s_waitcnt lgkmcnt(0)\n\tv_cmp_gt_u32 vcc, %[n], %[t]\n\ts_cbranch_vccz .Lsb%=\n\t"
               "s_sub_u32 %[bud], %[bud], 1\n\ts_cbranch_scc0 .Lsa%=\n\ts_mov_b32 %[bud], 0\n.Lsb%=:"
               : [t] "=&v"(t), [bud] "+s"(bud), [ib] "=&s"(ib), [done] "+s"(done)
               : [a] "v"(a), [n] "i"(N), [fo] "v"(fo), [tag] "v"(tag), [wc] "s"(wc), [y] "i"(Y)
               : "vcc", "scc", "memory");
}
template <int N, int SL>
__device__ __forceinline__ void serve(unsigned a, unsigned& bud) {
  unsigned t;
  asm volatile(PSTOP ".Lsa%=:\n\tds_read_b32 %0, %2\n\ts_waitcnt lgkmcnt(0)\n\tv_cmp_gt_u32 vcc, %3, %0\n\ts_cbranch_vccz .Lsb%=\n\ts_sleep %4\n\t"
               "s_sub_u32 %1, %1, 1\n\ts_cbranch_scc0 .Lsa%=\n\ts_mov_b32 %1, 0\n.Lsb%=:"
               : "=&v"(t), "+s"(bud) : "v"(a), "i"(N), "i"(SL) : "vcc", "scc", "memory");
}
__device__ __forceinline__ void mm(f32x4& d, const bf16x8& a, const bf16x8& b) {
  asm volatile(PSTOP "v_mfma_f32_16x16x32_bf16 %0, %1, %2, %0" : "+v"(d) : "v"(a), "v"(b));
}
template <int N>
__device__ __forceinline__ void mmw(f32x4& d, const bf16x8& a, const bf16x8& b) {
  __builtin_amdgcn_s_waitcnt(0xc07f | (N << 8));
  SB();
  asm volatile("v_mfma_f32_16x16x32_bf16 %0, %1, %2, %0" : "+v"(d) : "v"(a), "v"(b) : "memory");
}
__device__ __forceinline__ void mm0(f32x4& d, const bf16x8& a, const bf16x8& b) {
  asm volatile("v_mfma_f32_16x16x32_bf16 %0, %1, %2, 0" : "=&v"(d) : "v"(a), "v"(b));
}
#define CSTOP STOP
#define CHK_TAIL "s_cbranch_vccnz .Lsa%=\n.Lsb%=:\n\t.subsection 1\n" CSTOP ".Lsa%=:\n\tds_read_b32 %[fl], %[a]\n\ts_waitcnt lgkmcnt(0)\n\t"        \
                 "v_cmp_gt_u32 vcc, %[n], %[fl]\n\ts_cbranch_vccz .Lsb%=\n\ts_sub_u32 %[bud], %[bud], 1\n\ts_cbranch_scc0 .Lsa%=\n\t"       \
                 "s_mov_b32 %[bud], 0\n\ts_branch .Lsb%=\n\t.subsection 0"
template <int N>
__device__ __forceinline__ void mmchk(f32x4& d, const bf16x8& x, const bf16x8& y, unsigned& fl, unsigned a, unsigned& bud) {
  asm volatile("v_cmp_gt_u32 vcc, %[n], %[fl]\n\tv_mfma_f32_16x16x32_bf16 %[d], %[x], %[y], %[d]\n\t" CHK_TAIL
               : [d] "+v"(d), [fl] "+v"(fl), [bud] "+s"(bud) : [x] "v"(x), [y] "v"(y), [a] "v"(a), [n] "i"(N) : "vcc", "scc", "memory");
}
template <int N, int W>
__device__ __forceinline__ void mmchkw(f32x4& d, const bf16x8& x, const bf16x8& y, unsigned& fl, unsigned a, unsigned& bud) {
  __builtin_amdgcn_s_waitcnt(0xc07f | (W << 8));
  SB();
  asm volatile("v_cmp_gt_u32 vcc, %[n], %[fl]\n\tv_mfma_f32_16x16x32_bf16 %[d], %[x], %[y], %[d]\n\t" CHK_TAIL
               : [d] "+v"(d), [fl] "+v"(fl), [bud] "+s"(bud) : [x] "v"(x), [y] "v"(y), [a] "v"(a), [n] "i"(N) : "vcc", "scc", "memory");
}
template <int N>
__device__ __forceinline__ void mm0w(f32x4& d, const bf16x8& a, const bf16x8& b) {
  asm volatile("s_waitcnt lgkmcnt(%3)\n\tv_mfma_f32_16x16x32_bf16 %0, %1, %2, 0" : "=&v"(d) : "v"(a), "v"(b), "i"(N) : "memory");
}
template <int N>
__device__ __forceinline__ void chk(unsigned& fl, unsigned a, unsigned& bud) {
  asm volatile("v_cmp_gt_u32 vcc, %[n], %[fl]\n\t" CHK_TAIL : [fl] "+v"(fl), [bud] "+s"(bud) : [a] "v"(a), [n] "i"(N) : "vcc", "scc", "memory");
}

__device__ __attribute__((used, noinline, aligned(4096))) void fk_space() { asm volatile(".fill 248, 4, 0xbf800000"); }
__global__ __launch_bounds__(512, 2) void fused_k(const u16* __restrict__ wsre, const u16* __restrict__ wsim,
                                               const u16* __restrict__ x, const u16* __restrict__ w_r,
                                               const u16* __restrict__ gate_w, const u16* __restrict__ wdre,
                                               const u16* __restrict__ wdim, const u16* __restrict__ w_gate,
                                               const u16* __restrict__ w_up, const u16* __restrict__ w_down,
                                               char* ws, u16* __restrict__ out) {
  __shared__ __attribute__((aligned(16))) char smem[RING + SCR];
  static_assert(RING + SCR == 163840, "the whole LDS: one block per CU, the array starts at LDS address 0");
  asm volatile(".set fk_page, 0\n\t.subsection 1\n.Lfk_tour:\n\ts_branch 7777f\n\t.subsection 0\n\tds_write2st64_b32 %0, %1, %1 offset1:8\n\ts_waitcnt lgkmcnt(0)\n\ts_barrier" ::"v"((unsigned)(size_t)smem + RING + ((threadIdx.x ^ 256u) << 2)), "v"(0u) : "memory");
  asm volatile("" ::"s"(wsre), "s"(wsim), "s"(x), "s"(w_r), "s"(gate_w), "s"(wdre), "s"(wdim), "s"(w_gate), "s"(w_up), "s"(w_down), "s"(ws));
  const char* wc = ws + W_E;
  const unsigned c_in = (unsigned)__builtin_amdgcn_s_memtime(), r_in = (unsigned)__builtin_amdgcn_s_memrealtime();
#define UNTIL_(now, t0, lim) do { _Pragma("clang loop unroll(disable)") for (int i_ = 0; i_ < 4096; ++i_) { if ((unsigned)(now) - (t0) >= (lim)) break; __builtin_amdgcn_s_sleep(1); } } while (0)
#define UNTIL(ticks, cyc) do { UNTIL_(__builtin_amdgcn_s_memrealtime(), r_in, ticks); UNTIL_(__builtin_amdgcn_s_memtime(), c_in, cyc); } while (0)
  const int tid = threadIdx.x, lane = tid & 63;
  const int wid = __builtin_amdgcn_readfirstlane(tid >> 6);
  const int b = blockIdx.x;
  const int xcd = b & 7, off = b >> 3;
  const int q = xcd >> 1;
  const int h = q >> 1, kh = q & 1;
  const int ej = off & 3, tl = off >> 2;
  const int e0 = (ej >> 1) * 256 + q * 64 + (ej & 1) * 32;
  const bool isq = ej & 1;
  unsigned isp = (unsigned)(~ej & 1);
  asm volatile("" : "+s"(isp));
  const int ti = (xcd & 1) * 8 + tl;
  const int si = (xcd & 3) * 4 + (tl & 3), cp1 = (tl >> 2) * 2 + (ej >> 1);
  const int fr = lane & 15, fq = lane >> 4;
  const int r8 = lane >> 3, c8 = lane & 7;
  const unsigned sb = (unsigned)(size_t)smem;
  volatile lds_u32* lflag = (volatile lds_u32*)(smem + RING + L_FLAG);
  const char* ab = ws + W_AB;
  const unsigned rot = (unsigned)(tl), flip = (unsigned)((ej & 2) * 4);
#define KADDR(P, SH, base) padd<(P) & 7, 7, SH>((base) + ((((P) & 8) ^ flip) << (SH)), rot)
  const unsigned rot1 = (unsigned)((2 * (cp1 + tl) + (xcd & 1)) & 7);
  const unsigned row_s = __builtin_amdgcn_readfirstlane(sb + RING + ((wid ^ 4) << 8));
  u64 pc;
  unsigned tab_o;
  asm volatile("s_getpc_b64 %0\n.Lfk_here:\n\ts_mov_b32 %1, .Lfk_tab-.Lfk_here\n\t.subsection 2\n\t.p2align 8\n.Lfk_tab:\n\t"
               ".set fk_i, 0\n\t.rept 128\n\t.long fk_i\n\t.set fk_i, fk_i+1\n\t.endr\n\t.subsection 0"
               : "=s"(pc), "=s"(tab_o));
  const char* tab = reinterpret_cast<const char*>(pc + tab_o);
#define SIG(n) sig<n>(row_s, tab)
  const unsigned lf_rd = sb + RING + L_LF + (lane & 1) * 516, cp_rd = sb + RING + ((lane & 7) == 4 ? L_LF + 784 : L_CP + (lane & 3) * 260);
  const unsigned la_rd = sb + RING + L_LF, lb_rd = sb + RING + L_LF + 512;
  const unsigned own_rd = sb + RING + L_FLAG + 32 + (lane & 3) * 4;
#define IC(k) std::integral_constant<int, k>{}

  unsigned bud = 1u << 20;
#define SERVE(ADDR, N) serve<N, LSLEEP>(ADDR, bud)
  const unsigned best_rd = sb + RING + L_FLAG + 12, d_rd = sb + RING + L_FLAG + 48;
  const unsigned rf_rd = sb + RING + L_RG + 1024;
  const unsigned tg_rd = sb + RING + L_FLAG + 56, gt_rd = sb + RING + L_FLAG + 4;
#define TAG_WAIT() do { SERVE(tg_rd, 1); tag = __builtin_amdgcn_readfirstlane(lflag[15]); } while (0)
  const unsigned lbase = __builtin_amdgcn_readfirstlane(sb);
  auto row_addr = [&](unsigned (&va)[8]) {
    unsigned l = (unsigned)lane;
    asm volatile("" : "+v"(l));
#pragma unroll
    for (int i = 0; i < 8; ++i) va[i] = (i * 8 + (l >> 3)) * (D_ * 2) + (((l & 7) ^ (l >> 3)) << 4) - (i & 3) * 1024;
  };
  const char* xs = (const char*)x + (size_t)si * 128 * D_ * 2;
  const char* wds = (const char*)(h ? wdim : wdre) + (size_t)cp1 * 128 * D_ * 2;
  if ((wid | 2) == 6) {
    unsigned va[8];
    row_addr(va);
    const bool a = wid == 4;
    const char* base = a ? xs : wds;
    const unsigned n = isq ? (a ? 1 * 2u : 1 * 2u) : 2u;
    const unsigned r0 = rot1 + (isq ? 1u : 0u);
    unsigned l0 = lbase + (a ? 0 : isq ? 131072 : 81920);
    if (!a && !isq) {
      glds(lbase + RING + L_RF, (unsigned)(lane * 16), w_r);
      glds(lbase + RING + L_RF + 1152, (unsigned)(lane * 16), gate_w);
      sig<1>(lbase + RING + L_RG + 1024, tab);
    }
    if (isq) __builtin_amdgcn_s_sleep(20);
#pragma clang loop unroll(disable)
    for (unsigned u = 0; u < n; ++u) {
      const char* p = base + (((((u >> 1) + r0) & 7u) << 7) + ((u & 1u) << 16));
      glds4<0>(l0, va[0], va[1], va[2], va[3], p);
      glds4<4096>(l0, va[4], va[5], va[6], va[7], p);
      sigv(row_s, tab, ((u + 1u) >> 1) << 2);
      l0 += 8192u;
    }
  } else {
    const unsigned len = tab_o + 512u;
    const unsigned po = (unsigned)((wid * 32 + off) * 1024 + lane * 16);
    if (po + 16u <= len) glds(lbase + RING + L_X, po, reinterpret_cast<const char*>(pc & ~63ull));
  }
  unsigned E;
  asm volatile("s_cmp_lg_u32 %3, 7\n\ts_cbranch_scc1 .Le%=\n\tglobal_load_dword %0, %1, %2 sc1\n.Le%=:" : "=&v"(E) : "v"(0u), "s"(wc), "s"(wid) : "scc", "memory");
  if (wid >= 4) {
  unsigned vw[16], vx[16];
  auto X_ADDR = [&]() {
    unsigned va[8];
    row_addr(va);
#pragma unroll
    for (int i = 0; i < 16; ++i) vx[i] = va[i & 7] + (unsigned)((i >> 3) * 64 * D_ * 2);
  };
  const char* Ws = (const char*)(h ? wsim : wsre) + (size_t)ti * 128 * S_ * 2 + kh * S_;
  auto tile16 = [&](auto O, const char* p, const unsigned (&v)[16]) {
    glds4<decltype(O)::value>(lbase, v[0], v[1], v[2], v[3], p);
    glds4<decltype(O)::value + 4096>(lbase, v[4], v[5], v[6], v[7], p);
    glds4<decltype(O)::value + 8192>(lbase, v[8], v[9], v[10], v[11], p);
    glds4<decltype(O)::value + 12288>(lbase, v[12], v[13], v[14], v[15], p);
  };
  auto W_ADDR = [&]() {
    unsigned r8_ = (unsigned)r8;
    asm volatile("" : "+v"(r8_));
#pragma unroll
    for (int i = 0; i < 16; ++i) vw[i] = (i * 8 + r8_) * (S_ * 2) + (((unsigned)c8 ^ r8_) << 4) - (i & 3) * 1024;
  };
  auto stageW = [&](auto P, auto O) { tile16(O, KADDR(decltype(P)::value, 7, Ws), vw); };
  auto stageW2 = [&](auto P, auto O, auto O2) {
    const char *p = KADDR(decltype(P)::value, 7, Ws), *p2 = KADDR(decltype(P)::value + 1, 7, Ws);
    glds4<decltype(O)::value>(lbase, vw[0], vw[1], vw[2], vw[3], p);
    glds4<decltype(O2)::value>(lbase, vw[0], vw[1], vw[2], vw[3], p2);
    glds4<decltype(O)::value + 4096>(lbase, vw[4], vw[5], vw[6], vw[7], p);
    glds4<decltype(O2)::value + 4096>(lbase, vw[4], vw[5], vw[6], vw[7], p2);
    glds4<decltype(O)::value + 8192>(lbase, vw[8], vw[9], vw[10], vw[11], p);
    glds4<decltype(O2)::value + 8192>(lbase, vw[8], vw[9], vw[10], vw[11], p2);
    glds4<decltype(O)::value + 12288>(lbase, vw[12], vw[13], vw[14], vw[15], p);
    glds4<decltype(O2)::value + 12288>(lbase, vw[12], vw[13], vw[14], vw[15], p2);
  };
  const int qi = (off >> 1) * 8 + xcd;
  const unsigned g_o = (unsigned)(C_KEY + ti * 1024 + (lane >> 2 & 7) * 128 + (lane & 3) * 16);
#define G_PUT(gv, best) asm volatile("ds_write_b128 %0, %1\n\tds_write_b32 %2, %3" ::"v"(sb + RING + L_G + (lane & 31) * 16),                                    \
                                     "v"(u32x4{gv.x << 16, gv.x & 0xffff0000u, gv.y << 16, gv.y & 0xffff0000u}), "v"(best_rd), "v"(best) : "memory")
  if (wid == 4) {
    X_ADDR();
    auto stageX = [&](auto K, auto O) { tile16(O, k1tile<decltype(K)::value>(xs, rot1), vx); };
    W_ADDR();
    if (isp) {
    stageX(IC(1), IC(16384));
    SIG(2);
    stageX(IC(2), IC(32768));
    SIG(3);
    stageX(IC(3), IC(49152));
    SIG(4);
    stageX(IC(4), IC(65536));
    SIG(5);
    SERVE(cp_rd, 1);
    stageX(IC(5), IC(0));
    SIG(6);
    SERVE(cp_rd, 2);
    stageX(IC(6), IC(16384));
    SIG(7);
    SERVE(cp_rd, 3);
    stageX(IC(7), IC(32768));
    SIG(8);
    }
    if (isq) {
    }
    if (isp) SERVE(cp_rd, 4);
    stageW(IC(0), IC(49152));
    if (isp) SERVE(cp_rd, 5);
    stageW(IC(1), IC(65536));
    SIG(9);
    stageW(IC(2), IC(81920));
    if (isp) SERVE(cp_rd, 6);
    stageW(IC(3), IC(0));
    SIG(10);
    SERVE(own_rd, 1);
    stageW(IC(4), IC(98304));
    if (isp) SERVE(cp_rd, 7);
    stageW(IC(5), IC(16384));
    SIG(11);
    stageW(IC(6), IC(114688));
    if (isp) SERVE(cp_rd, 8);
    stageW(IC(7), IC(32768));
    SIG(15);
    if (isq) { SERVE(lb_rd, 12); }
    stageW(IC(8), IC(131072));
    SIG(16);
    SERVE(own_rd, 2);
    stageW(IC(9), IC(49152));
    SIG(17);
    stageW(IC(10), IC(65536));
    SIG(18);
    SERVE(own_rd, 3);
    stageW(IC(11), IC(81920));
    SIG(64);
    bud = 1u << 18;
    __builtin_amdgcn_s_setprio(3);
    unsigned tag;
    TAG_WAIT();
    if (isp) {
      UNTIL(855u, 17991u);
      u32x2 kb;
      u32x4 gv;
#pragma clang loop unroll(disable)
      for (int it = 0; it < SPIN_MAX; ++it) {
        asm volatile("global_load_dwordx2 %0, %2, %4 sc1\n\tglobal_load_dwordx4 %1, %3, %4 sc1\n\ts_waitcnt vmcnt(0)" : "=&v"(kb), "=&v"(gv) : "v"((unsigned)C_BEST), "v"(g_o), "s"(wc) : "memory");
        if (__all(kb.y == tag && gv.w == tag)) break;
        __builtin_amdgcn_s_sleep(PSLEEP);
      }
      G_PUT(gv, kb.x);
    }
    SERVE(best_rd, 1);
    const unsigned best = __builtin_amdgcn_readfirstlane(lflag[3]);
    const int idx = (4095 - (int)(best & 0xfffu)) & (S_ - 1);
    if ((idx >> 7) == ti) {
      const unsigned uo = best & 0xffff0000u;
      const float rsel = rbf(sigmoidf_(__uint_as_float((uo & 0x80000000u) ? (uo & 0x7fffffffu) : (~uo & 0xffff0000u))));
      const int col = (lane & 7) * 2 + (lane >> 5);
      unsigned xv[2];
      asm volatile("global_load_ushort %0, %2, %3\n\tglobal_load_ushort %1, %2, %3 offset:32\n\ts_waitcnt vmcnt(0)"
                   : "=&v"(xv[0]), "=&v"(xv[1]) : "v"((unsigned)((idx * D_ + e0 + col) * 2)), "s"(x) : "memory");
      float rs_ = rsel;
      unsigned pa = sb + RING + L_UPD + col * 2;
      u32x4 ov[8];
      unsigned go = (unsigned)(C_PB + r8 * 4096 + e0 * 8 + c8 * 16);
      unsigned g3 = lane < 32 ? go + 98304u : (unsigned)(C_SEEN + (lane & 7) * 256);
      asm volatile("" : "+v"(rs_), "+v"(pa), "+v"(go), "+v"(g3));
      SERVE(cp_rd, 18); UNTIL(1045u, 21373u);
#pragma clang loop unroll(disable)
      for (int it = 0; it < SPIN_MAX; ++it) {
        asm volatile("global_load_dwordx4 %0, %8, %12 sc1\n\tglobal_load_dwordx4 %1, %9, %12 sc1\n\t"
                     "global_load_dwordx4 %2, %10, %12 sc1\n\tglobal_load_dwordx4 %3, %11, %12 sc1\n\t"
                     "global_load_dwordx4 %4, %8, %12 offset:128 sc1\n\tglobal_load_dwordx4 %5, %9, %12 offset:128 sc1\n\t"
                     "global_load_dwordx4 %6, %10, %12 offset:128 sc1\n\tglobal_load_dwordx4 %7, %11, %12 offset:128 sc1\n\ts_waitcnt vmcnt(0)"
                     : "=&v"(ov[0]), "=&v"(ov[1]), "=&v"(ov[2]), "=&v"(ov[3]), "=&v"(ov[4]), "=&v"(ov[5]), "=&v"(ov[6]), "=&v"(ov[7])
                     : "v"(go), "v"(go + 32768u), "v"(go + 65536u), "v"(g3), "s"(wc)
                     : "memory");
        bool ok = true;
#pragma unroll
        for (int k = 0; k < 8; ++k) ok = ok && ov[k].y == tag && ov[k].w == tag;
        if (__all(ok)) break;
        __builtin_amdgcn_s_sleep(PSLEEP);
      }
      if (ej == 0 && q == 0 && lane == 0)
        asm volatile("global_store_dword %0, %1, %2 sc1\n\ts_nop 1" ::"v"(0u), "v"(tag), "s"(wc) : "memory");
      float u[2];
#pragma unroll
      for (int c = 0; c < 2; ++c) {
        if (lane >= 32) ov[c * 4 + 3].x = ov[c * 4 + 3].z = 0u;
        float s0 = (__uint_as_float(ov[c * 4].x) + __uint_as_float(ov[c * 4 + 1].x)) + (__uint_as_float(ov[c * 4 + 2].x) + __uint_as_float(ov[c * 4 + 3].x));
        float s1 = (__uint_as_float(ov[c * 4].z) + __uint_as_float(ov[c * 4 + 1].z)) + (__uint_as_float(ov[c * 4 + 2].z) + __uint_as_float(ov[c * 4 + 3].z));
        s0 = dpp_add<0x128>(s0); s1 = dpp_add<0x128>(s1);
        const float t2 = sw16(sw32(s0, s1), sw32(s0, s1));
        u[c] = rbf(rbf(t2) * rs_);
      }
      const unsigned p0 = f2bf(lo2f(xv[0]) + u[0]), p1 = f2bf(lo2f(xv[1]) + u[1]);
      if ((lane & 31) < 8)
        asm volatile("ds_write_b16 %0, %1\n\tds_write_b16 %0, %2 offset:32"
                     ::"v"(pa), "v"(p0), "v"(p1) : "memory");
    }
    asm volatile("ds_write_b32 %0, %1\n\ts_waitcnt vmcnt(0) lgkmcnt(0)" ::"v"(d_rd), "v"(1u) : "memory");
  } else if (wid == 6) {
    X_ADDR();
    const unsigned vb0 = (unsigned)(lane * 16), vb2 = vb0 + 2048u;
    const char* abg = ab + (size_t)((h * 4 + ej) * 32 + kh * 8) * A_B;
    const char* abA = abg + (h ^ 1) * 16 * A_B;
    const char* abH = abg + h * 16 * A_B;
    auto stageD = [&](auto K, auto O) { tile16(O, k1tile<decltype(K)::value>(wds, rot1), vx); };
    auto stageQ = [&](auto O) {
      const char* p = (const char*)x + (size_t)ti * 128 * D_ * 2 + e0 * 2;
      const unsigned v = (unsigned)((lane >> 2) * (D_ * 2) + ((lane & 3) ^ (lane >> 4)) * 16);
      glds4<decltype(O)::value>(lbase, v, v + 15360u, v + 30720u, v + 46080u, p);
      glds4<decltype(O)::value + 4096>(lbase, v, v + 15360u, v + 30720u, v + 46080u, p + 64 * (D_ * 2));
    };
    unsigned tag;
    u64 seen = 0ull;
    const unsigned flag_o = (unsigned)(C_FL2 + (((h * 4 + ej) * 2 + kh) * 32 + (lane & 31)) * FSTR);
    auto GATE_ALL = [&]() {
      gate(seen, ~0ull, 0u, flag_o, tag, wc, bud);
      if (isq) asm volatile("ds_write_b32 %0, %1" ::"v"(gt_rd), "v"(1u) : "memory");
    };
    const unsigned dl = (unsigned)(kh ^ 1) * 2048u;
    const unsigned vc0 = vb2 + dl, vc1 = vb2 - dl, vc2 = vc0 + 4096u, vc3 = vc1 + 4096u;
    auto stageA = [&](auto P, auto H, auto O, auto G) {
      constexpr int hf = decltype(H)::value;
      const char* p = KADDR(decltype(P)::value, 13, (hf ? abH : abA) - 2048);
      glds4_sc1<decltype(O)::value>(lbase, vc0, vc0, vc1, vc1, p);
      glds4_sc1<decltype(O)::value + 4096>(lbase, vc2, vc2, vc3, vc3, p);
    };
    auto stageN = [&](auto P, auto O, const char* base) {
      glds4_sc1<decltype(O)::value>(lbase, vb0, vb0, vb2, vb2, KADDR(decltype(P)::value, 13, base));
      glds4_sc1<decltype(O)::value + 4096>(lbase, vb0, vb0, vb2, vb2, KADDR(decltype(P)::value + 1, 13, base));
    };
    auto touch = [&]() {
      asm volatile("s_mov_b32 m0, %0\n\ts_nop 0\n\tglobal_load_lds_dword %1, %2 sc1\n\tglobal_load_lds_dword %1, %3 sc1"
                   ::"s"(lbase + RING + L_X), "v"((unsigned)(lane * 128)), "s"(KADDR(8, 13, abA)), "s"(KADDR(8, 13, abH)) : "memory");
    };
    if (isp) {
    stageD(IC(1), IC(98304));
    SIG(2);
    WAIT_V(18);
    stageD(IC(2), IC(114688));
    SIG(3);
    WAIT_V(18);
    stageD(IC(3), IC(131072));
    SIG(4);
    SERVE(cp_rd, 1);
    WAIT_V(18);
    stageD(IC(4), IC(81920));
    SIG(5);
    SERVE(cp_rd, 2);
    WAIT_V(18);
    stageD(IC(5), IC(98304));
    SIG(6);
    SERVE(cp_rd, 3);
    WAIT_V(18);
    stageD(IC(6), IC(114688));
    SIG(7);
    SERVE(cp_rd, 4);
    WAIT_V(18);
    stageD(IC(7), IC(131072));
    SIG(8);
    }
    if (isq) UNTIL(144u, 3075u);
    stageQ(IC(147456));
    sig<12>(row_s, tab);
    W_ADDR();
    TAG_WAIT();
    SERVE(own_rd, 1);
    SERVE(cp_rd, 8);
    SERVE(la_rd, 9);
    SERVE(own_rd, 3); if (isq) SERVE(own_rd, 5);
    UNTIL(569u, 11787u);
    GATE_ALL();
    SERVE(own_rd, 3);
    stageA(IC(0), IC(0), IC(0), IC(0));
    SIG(13);
    stageA(IC(0), IC(1), IC(8192), IC(0));
    SIG(14);
    SERVE(own_rd, 4);
    stageA(IC(1), IC(0), IC(98304), IC(0));
    SIG(15);
    stageA(IC(1), IC(1), IC(106496), IC(0));
    SIG(16);
    stageA(IC(2), IC(0), IC(16384), IC(0));
    stageA(IC(2), IC(1), IC(24576), IC(0));
    SIG(17);
    SERVE(own_rd, 5);
    stageA(IC(3), IC(0), IC(114688), IC(0));
    stageA(IC(3), IC(1), IC(122880), IC(0));
    SIG(18);
    stageA(IC(4), IC(0), IC(32768), IC(0));
    stageA(IC(4), IC(1), IC(40960), IC(0));
    SIG(19);
    SERVE(cp_rd, 9);
    stageA(IC(5), IC(0), IC(0), IC(0));
    SERVE(cp_rd, 10);
    stageA(IC(5), IC(1), IC(8192), IC(0));
    SIG(20);
    SERVE(cp_rd, 11);
    stageW(IC(12), IC(98304));
    stageA(IC(6), IC(0), IC(131072), IC(0));
    stageA(IC(7), IC(0), IC(139264), IC(0));
    SIG(21);
    SERVE(cp_rd, 12);
    stageW(IC(13), IC(16384));
    stageN(IC(6), IC(49152), abH + dl);
    stageN(IC(8), IC(57344), abA + dl);
    SIG(22);
    SERVE(cp_rd, 13);
    stageN(IC(10), IC(114688), abA + dl);
    stageN(IC(12), IC(122880), abA + dl);
    stageW(IC(14), IC(65536));
    SIG(23);
    SERVE(cp_rd, 14);
    stageW(IC(15), IC(32768));
    stageN(IC(14), IC(81920), abA + dl);
    stageN(IC(8), IC(90112), abA + (dl ^ 2048u));
    SIG(24);
    SERVE(cp_rd, 15);
    stageN(IC(10), IC(0), abA + (dl ^ 2048u));
    stageN(IC(12), IC(8192), abA + (dl ^ 2048u));
    SIG(25);
    SERVE(cp_rd, 16);
    stageN(IC(14), IC(131072), abA + (dl ^ 2048u));
    stageN(IC(8), IC(139264), abH + dl);
    SIG(26);
    SERVE(cp_rd, 17);
    stageN(IC(10), IC(49152), abH + dl);
    stageN(IC(12), IC(57344), abH + dl);
    SIG(27);
    stageN(IC(14), IC(98304), abH + dl);
    stageN(IC(6), IC(106496), abH + (dl ^ 2048u));
    SIG(28);
    SERVE(cp_rd, 18);
    stageN(IC(8), IC(16384), abH + (dl ^ 2048u));
    stageN(IC(10), IC(24576), abH + (dl ^ 2048u));
    SIG(29);
    stageN(IC(12), IC(114688), abH + (dl ^ 2048u));
    stageN(IC(14), IC(122880), abH + (dl ^ 2048u));
    SIG(64);
    WAIT_V(0);
  } else {
    const int sw = wid >> 1 & 1;
    __builtin_amdgcn_s_setprio(2);
    unsigned tag;
    int idx = 0;
    auto TAG_PUT = [&]() {
      unsigned es;
      asm volatile("s_waitcnt vmcnt(0)\n\tv_readfirstlane_b32 %0, %1" : "=s"(es) : "v"(E) : "memory");
      asm volatile("ds_write2_b32 %0, %1, %2 offset1:1" ::"v"(tg_rd), "v"(1u), "v"(es * 0x9E3779B1u + 0x7F4A7C15u) : "memory");
    };
    auto ROUTER = [&]() {
      const unsigned ra = sb + ((h * 4 + cp1) * 16 + fr) * 128 + ((fq ^ (fr & 7)) << 4), rb = sb + RING + L_RF + (fr & 1) * 1152 + fq * 16;
      f32x4 d;
      SERVE(rf_rd, 1);
#define RSTEP(s, o) do { bf16x8 a0, a1, b0, b1; SERVE(la_rd, (s) + 1);                                                                       \
      asm volatile("ds_read_b128 %0, %4\n\tds_read_b128 %1, %5\n\tds_read_b128 %2, %6\n\tds_read_b128 %3, %6 offset:64\n\ts_waitcnt lgkmcnt(0)"  \
                   : "=&v"(a0), "=&v"(a1), "=&v"(b0), "=&v"(b1) : "v"(ra + (o)), "v"((ra ^ 64u) + (o)), "v"(rb + (((s) + rot1) & 7u) * 128u) : "memory"); \
      sig<(s) == 7 ? 64 : (s) + 1>(lbase + RING + L_LF + 768, tab); if ((s) == 0) mm0(d, a0, b0); else mm(d, a0, b0); mm(d, a1, b1); } while (0)
      RSTEP(0, 0u);
      RSTEP(1, 16384u);
      RSTEP(2, 32768u);
      RSTEP(3, 49152u);
      RSTEP(4, 65536u);
      RSTEP(5, 0u);
      RSTEP(6, 16384u);
      RSTEP(7, 32768u);
      asm volatile("s_nop 7" : "+v"(d));
      const int rid = si * 8 + h * 4 + cp1;
      unsigned key = 0u;
      float g[4];
#pragma unroll
      for (int i = 0; i < 4; ++i) {
        const float r = rbf(d[i]);
        g[i] = sigmoidf_(r);
        const unsigned u = __float_as_uint(r);
        const unsigned uo = (u & 0x80000000u) ? ~u : (u | 0x80000000u);
        key = max(key, (uo & 0xffff0000u) | (unsigned)(4095 - (rid * 16 + fq * 4 + i)));
      }
      key = dpp<0xa0>(key);
      const auto k1 = __builtin_amdgcn_permlane16_swap(key, key, false, false);
      key = max(k1[0], k1[1]);
      const auto k2 = __builtin_amdgcn_permlane32_swap(key, key, false, false);
      key = max(k2[0], k2[1]);
      if ((fr | 2) == 3) gst128_sc1((unsigned)(C_KEY + rid * 128 + (fr >> 1) * 64 + fq * 16), u32x4{pk2(g[0], g[1]), pk2(g[2], g[3]), key, tag}, wc);
    };
    auto POLL_KEYS = [&]() {
      u32x2 k0, k1;
      u32x4 gv;
#pragma clang loop unroll(disable)
      for (int it = 0; it < SPIN_MAX; ++it) {
        asm volatile("global_load_dwordx2 %0, %3, %6 sc1\n\tglobal_load_dwordx2 %1, %4, %6 sc1\n\tglobal_load_dwordx4 %2, %5, %6 sc1\n\ts_waitcnt vmcnt(0)"
                     : "=&v"(k0), "=&v"(k1), "=&v"(gv) : "v"((unsigned)(C_KEY + lane * 128 + 8)), "v"((unsigned)(C_KEY + lane * 128 + 8200)), "v"(g_o), "s"(wc) : "memory");
        if (__all(k0.y == tag && k1.y == tag && gv.w == tag)) break;
        __builtin_amdgcn_s_sleep(PSLEEP);
      }
      const unsigned best = wave_max(max(k0.x, k1.x));
      G_PUT(gv, best);
      if (b == 8 && lane < 8) gst128_sc1((unsigned)(C_BEST + lane * 16), u32x4{best, tag, best, tag}, wc);
    };
    auto IDX_READ = [&]() { idx = (4095 - (int)(__builtin_amdgcn_readfirstlane(lflag[3]) & 0xfffu)) & (S_ - 1); };
    const int on = 111 - qi;
    const bool ffn = on >= 0;
    const int fo = ffn ? qi >> 2 : 0, fp = qi & 3;
    u32x4 fg[16];
    const u16* wgu = sw ? w_up : w_gate;
    const unsigned vg = (unsigned)((fp * 128 + r8 * 8) * (F_ * 2) + fo * 128 + c8 * 16);
#define LOAD4(d, v, step, base, on)                                                                                        \
  asm volatile("s_cmp_lt_i32 %9, 0\n\ts_cbranch_scc1 .Lq%=\n\tglobal_load_dwordx4 %0, %4, %8\n\tglobal_load_dwordx4 %1, %5, %8\n\t" \
               "global_load_dwordx4 %2, %6, %8\n\tglobal_load_dwordx4 %3, %7, %8\n.Lq%=:"                                   \
               : "+v"((d)[0]), "+v"((d)[1]), "+v"((d)[2]), "+v"((d)[3])                                                    \
               : "v"(v), "v"((v) + (step)), "v"((v) + 2u * (step)), "v"((v) + 3u * (step)), "s"(base), "s"(on) : "scc", "memory")
#define ANY8(w) asm volatile("" : "=v"((w)[0]), "=v"((w)[1]), "=v"((w)[2]), "=v"((w)[3]), "=v"((w)[4]), "=v"((w)[5]), "=v"((w)[6]), "=v"((w)[7]))
    ANY8(fg);
    ANY8(fg + 8);
#define FFN_LOAD(p) LOAD4(fg + (p) * 4, vg + (((p) & 1) * 4u + ((p) >> 1) * 64u) * (F_ * 2), (unsigned)(F_ * 2), wgu, on)
    auto turn = [&](u32x4* w) {
      u32x4 t[8];
#pragma unroll
      for (int d = 0; d < 4; ++d)
#pragma unroll
        for (int j = 0; j < 4; ++j) {
          t[2 * d][j] = __builtin_amdgcn_perm(w[2 * j + 1][d], w[2 * j][d], 0x05040100u);
          t[2 * d + 1][j] = __builtin_amdgcn_perm(w[2 * j + 1][d], w[2 * j][d], 0x07060302u);
        }
#pragma unroll
      for (int i = 0; i < 8; ++i) w[i] = t[i];
      asm volatile("" : "+v"(w[0]), "+v"(w[1]), "+v"(w[2]), "+v"(w[3]), "+v"(w[4]), "+v"(w[5]), "+v"(w[6]), "+v"(w[7]));
    };
#define PIN8(w) asm volatile("s_waitcnt vmcnt(0)" : "+v"((w)[0]), "+v"((w)[1]), "+v"((w)[2]), "+v"((w)[3]), "+v"((w)[4]), "+v"((w)[5]), "+v"((w)[6]), "+v"((w)[7])::"memory")
    unsigned pa_o = (unsigned)(C_PA + qi * 1024 + (sw * 64 + c8 * 8 + fq * 2) * 8);
    auto FFN_RUN = [&]() {
      bf16x8 xa_[2];
      asm volatile("global_load_dwordx4 %0, %2, %3\n\tglobal_load_dwordx4 %1, %2, %3 offset:128\n\ts_waitcnt vmcnt(0)"
                   : "=&v"(xa_[0]), "=&v"(xa_[1]) : "v"((unsigned)(idx * (D_ * 2) + (fp * 128 + (fq * 2 + (fr & 1)) * 8) * 2)), "s"(x) : "memory");
      f32x4 d[8];
#pragma unroll
      for (int j = 0; j < 8; ++j) mm0(d[j], xa_[0], __builtin_bit_cast(bf16x8, fg[j]));
#pragma unroll
      for (int j = 0; j < 8; ++j) mm(d[j], xa_[1], __builtin_bit_cast(bf16x8, fg[8 + j]));
      asm volatile("s_nop 7" : "+v"(d[0]), "+v"(d[1]), "+v"(d[2]), "+v"(d[3]), "+v"(d[4]), "+v"(d[5]), "+v"(d[6]), "+v"(d[7]));
      float dp[8];
#pragma unroll
      for (int j = 0; j < 8; ++j) dp[j] = fr < 8 ? d[j][0] : d[j][1];
      const float s0 = dpp_add<0x128>(lane < 32 ? (fq & 1 ? dp[2] : dp[0]) : (fq & 1 ? dp[6] : dp[4]));
      const float s1 = dpp_add<0x128>(lane < 32 ? (fq & 1 ? dp[3] : dp[1]) : (fq & 1 ? dp[7] : dp[5]));
      const u32x4 o = {__float_as_uint(s0), tag, __float_as_uint(s1), tag};
      SERVE(gt_rd, 1);
      if ((lane & 8) == 0)
        gst128_sc1(pa_o, o, wc);
    };
    u32x4 fd[16];
    const unsigned vd = (unsigned)((fo * 64 + fq * 16) * (D_ * 2) + fp * 256 + fr * 16);
    ANY8(fd);
    ANY8(fd + 8);
#define DOWN_LOAD(p) LOAD4(fd + (p) * 4, vd + (p) * 4096u, 1024u, w_down, on | (sw - 1))
    unsigned pb_o = (unsigned)(C_PB + fo * 4096 + (fp * 128 + fr * 8 + fq * 2) * 8), pvo = (unsigned)(C_PA + fo * 4096 + lane * 16);
    auto PREP_F = [&]() { PIN8(fg); PIN8(fg + 8); turn(fg); turn(fg + 8); asm volatile("" : "+v"(pa_o)); };
    auto PREP_D = [&]() { PIN8(fd); PIN8(fd + 8); turn(fd); turn(fd + 8); asm volatile("" : "+v"(pb_o), "+v"(pvo)); };
    auto SEEN = [&]() { if (lane < 8) gst128_sc1((unsigned)(C_SEEN + (qi - 112) * 128 + lane * 16), u32x4{tag, tag, tag, tag}, wc); };
    auto RED_AB = [&]() {
      u32x4 pv[4];
#pragma clang loop unroll(disable)
      for (int it = 0; it < SPIN_MAX; ++it) {
        asm volatile("global_load_dwordx4 %0, %4, %5 sc1\n\tglobal_load_dwordx4 %1, %4, %5 offset:1024 sc1\n\t"
                     "global_load_dwordx4 %2, %4, %5 offset:2048 sc1\n\tglobal_load_dwordx4 %3, %4, %5 offset:3072 sc1\n\ts_waitcnt vmcnt(0)"
                     : "=&v"(pv[0]), "=&v"(pv[1]), "=&v"(pv[2]), "=&v"(pv[3]) : "v"(pvo), "s"(wc) : "memory");
        bool ok = true;
#pragma unroll
        for (int k = 0; k < 4; ++k) ok = ok && pv[k].y == tag && pv[k].w == tag;
        if (__all(ok)) break;
        __builtin_amdgcn_s_sleep(PSLEEP);
      }
      float s0 = 0.f, s1 = 0.f;
#pragma unroll
      for (int k = 0; k < 4; ++k) { s0 += __uint_as_float(pv[k].x); s1 += __uint_as_float(pv[k].z); }
      const auto r = __builtin_amdgcn_permlane32_swap(__float_as_uint(s0), __float_as_uint(s1), false, false);
      const float gt = rbf(__uint_as_float(r[0]));
      const unsigned hb = f2bf(rbf(gt * sigmoidf_(gt)) * rbf(__uint_as_float(r[1])));
      asm volatile("ds_write_b16 %0, %1" ::"v"(sb + RING + L_H + (lane & 31) * 4 + (lane >> 5) * 2), "v"(hb) : "memory");
    };
    auto RED_C = [&]() {
      bf16x8 hp[2];
      asm volatile("ds_read_b128 %0, %2\n\tds_read_b128 %1, %2 offset:16\n\ts_waitcnt lgkmcnt(0)" : "=&v"(hp[0]), "=&v"(hp[1]) : "v"(sb + RING + L_H + fq * 32) : "memory");
      f32x4 d[8];
#pragma unroll
      for (int j = 0; j < 8; ++j) mm0(d[j], hp[0], __builtin_bit_cast(bf16x8, fd[j]));
#pragma unroll
      for (int j = 0; j < 8; ++j) mm(d[j], hp[1], __builtin_bit_cast(bf16x8, fd[8 + j]));
      asm volatile("s_nop 7" : "+v"(d[0]), "+v"(d[1]), "+v"(d[2]), "+v"(d[3]), "+v"(d[4]), "+v"(d[5]), "+v"(d[6]), "+v"(d[7]));
      const float dp[8] = {d[0][0], d[1][0], d[2][0], d[3][0], d[4][0], d[5][0], d[6][0], d[7][0]};
      const float v0 = lane < 32 ? (fq & 1 ? dp[2] : dp[0]) : (fq & 1 ? dp[6] : dp[4]);
      const float v1 = lane < 32 ? (fq & 1 ? dp[3] : dp[1]) : (fq & 1 ? dp[7] : dp[5]);
      gst128_sc1(pb_o, u32x4{__float_as_uint(v0), tag, __float_as_uint(v1), tag}, wc);
    };
    if (sw) TAG_PUT();
    if (isp) {
      if (sw) TOUR();
      TAG_WAIT();
      if (sw == 0) ROUTER();
    SERVE(lb_rd, 16);
      DOWN_LOAD(0);
      DOWN_LOAD(1);
      DOWN_LOAD(2);
      DOWN_LOAD(3);
      if (sw) PREP_D();
    SERVE(cp_rd, 14);
    UNTIL(855u, 17991u);
      if (ffn && sw) RED_AB();
      if (ffn && sw) RED_C();
    }
    if (isq) {
      TAG_WAIT();
      if (!ffn && sw) SEEN();
      if (sw == 0) sig<64>(lbase + RING + L_LF + 768, tab);
    SERVE(la_rd, 11);
      FFN_LOAD(0);
      FFN_LOAD(1);
      FFN_LOAD(2);
      FFN_LOAD(3);
    SERVE(own_rd, 5);
    UNTIL(574u, 11890u);
      if (sw == 0) POLL_KEYS();
      PREP_F();
      SERVE(best_rd, 1);
      IDX_READ();
      if (ffn) FFN_RUN();
    }
  }
  } else {
    const int wm = wid >> 1, wn = wid & 1;
    unsigned fl;
    unsigned flagged = (unsigned)(ej & 1) * 64u;
    const unsigned flag_o = (unsigned)(C_FL2 + (((h * 4 + (cp1 & 2) + wn) * 2 + kh) * 32 + ((si & 7) * 2 + (cp1 & 1)) * 2 + wm) * FSTR + lane * 4);
    unsigned tagv;
#define TRYFLAG(Y) tryflag<Y>(flagged, flag_o, tagv, wc)
#define FLAG(Y) do { WAIT_V(Y); TRYFLAG(Y); } while (0)
    const unsigned own_wr = sb + RING + L_FLAG + 32 + wid * 4;
#define OWN_SIG(n) do { unsigned t_; asm volatile("v_mov_b32 %0, %2\n\tds_write_b32 %1, %0" : "=&v"(t_) : "v"(own_wr), "i"(n) : "memory"); } while (0)
#define SPIN(ADDR, N) spin<N>(ADDR, bud)
#define SPINF(ADDR, N, Y) spinf<N, Y>(ADDR, bud, flagged, flag_o, tagv, wc)
    const unsigned wa0 = sb + (wid * 32 + fr) * 128 + ((fq ^ (fr & 7)) << 4), wa1 = wa0 ^ 64u;
    const unsigned xa = sb + lane * 16;
    const unsigned wa0_0 = wa0 + 0u;
    const unsigned wa0_1 = wa0 + 65536u;
    const unsigned wa0_2 = wa0 + 131072u;
    const unsigned wa1_0 = wa1 + 0u;
    const unsigned wa1_1 = wa1 + 65536u;
    const unsigned wa1_2 = wa1 + 131072u;
    const unsigned xa_0 = xa + 0u;
    const unsigned xa_1 = xa + 65536u;
    const unsigned xa_2 = xa + 131072u;
    bf16x8 wr[16][4], fb[9];
    if (isp) {
      const int row = wm * 64 + fr, xr = wn * 32 + fr;
      const unsigned pa0 = sb + row * 128 + ((fq ^ (row & 7)) << 4) + wn * 4096, pa1 = pa0 ^ 64u;
      const unsigned pb0 = sb + row * 128 + ((fq ^ (row & 7)) << 4) + (1 - wn) * 4096, pb1 = pb0 ^ 64u;
      const unsigned px0 = sb + xr * 128 + ((fq ^ (xr & 7)) << 4), px1 = px0 ^ 64u;
      f32x4 ac1[4][4];
      bf16x8 hw[2][4], hx[2][4];
      unsigned go[2];
      go[0] = (unsigned)((((h * 4 + (cp1 & 2) + wn) * 2 + (cp1 & 1)) * 16 + si) * A_B + wm * 8192 + wn * 4096 + (fq & 1) * 1024 + (fq >> 1) * 256 + fr * 16);
      go[1] = go[0] ^ 4096u;
#define P1_SETUP() asm volatile("" : "+v"(go[0]), "+v"(go[1]))
      unsigned pv[4];
#define PUB0(n, mi) asm volatile("v_cvt_pk_bf16_f32 %0, %2, %3\n\tv_cvt_pk_bf16_f32 %1, %4, %5" : "=&v"(pv[0]), "=&v"(pv[2])             \
                                 : "v"(ac1[n][mi][0]), "v"(ac1[n][mi][1]), "v"(ac1[(n) + 1][mi][0]), "v"(ac1[(n) + 1][mi][1]))
#define PUB1(n, mi) asm volatile("v_cvt_pk_bf16_f32 %0, %2, %3\n\tv_cvt_pk_bf16_f32 %1, %4, %5" : "=&v"(pv[1]), "=&v"(pv[3])             \
                                 : "v"(ac1[n][mi][2]), "v"(ac1[n][mi][3]), "v"(ac1[(n) + 1][mi][2]), "v"(ac1[(n) + 1][mi][3]))
#define PUB2() asm volatile("s_nop 1\n\tv_permlane16_swap_b32 %0, %2\n\tv_permlane16_swap_b32 %1, %3" : "+v"(pv[0]), "+v"(pv[1]), "+v"(pv[2]), "+v"(pv[3]))
#define PUB3(n, mi) asm volatile("global_store_dwordx4 %0, %1, %2 offset:%3 sc1\n\ts_nop 1" ::"v"(go[(mi) >> 1]), "v"(u32x4{pv[0], pv[1], pv[2], pv[3]}), "s"(ab), "i"(((mi) & 1) * 512 + (n) * 1024) : "memory")
      const unsigned pa0_0 = pa0 + 0u;
      const unsigned pa0_1 = pa0 + 65536u;
      const unsigned pa1_0 = pa1 + 0u;
      const unsigned pa1_1 = pa1 + 65536u;
      const unsigned pb0_0 = pb0 + 0u;
      const unsigned pb0_1 = pb0 + 65536u;
      const unsigned pb1_0 = pb1 + 0u;
      const unsigned pb1_1 = pb1 + 65536u;
      const unsigned px0_1 = px0 + 65536u;
      const unsigned px0_2 = px0 + 131072u;
      const unsigned px1_1 = px1 + 65536u;
      const unsigned px1_2 = px1 + 131072u;
      SPIN(lf_rd, 1); SB();
      lds_rd128<16384>(hx[0][0], px0_1); SB();
      lds_rd128<0>(hw[0][0], pa0_0); SB();
      lds_rd128<2048>(hw[0][1], pa0_0); SB();
      lds_rd128<0>(hw[0][2], pb0_0); SB();
      lds_rd128<2048>(hw[0][3], pb0_0); SB();
      lds_rd128<18432>(hx[0][1], px0_1); SB();
      lds_rd128<24576>(hx[0][2], px0_1); SB();
      lds_rd128<26624>(hx[0][3], px0_1); SB();
      mm0w<6>(ac1[0][0], hw[0][0], hx[0][0]); SB();
      lds_rd128<16384>(hx[1][0], px1_1); SB();
      mm0w<6>(ac1[0][1], hw[0][1], hx[0][0]); SB();
      lds_rd128<0>(hw[1][0], pa1_0); SB();
      mm0w<6>(ac1[0][2], hw[0][2], hx[0][0]); SB();
      lds_rd128<2048>(hw[1][1], pa1_0); SB();
      mm0w<6>(ac1[0][3], hw[0][3], hx[0][0]); SB();
      lds_rd128<0>(hw[1][2], pb1_0); SB();
      mm0w<6>(ac1[1][3], hw[0][3], hx[0][1]); SB();
      lds_rd128<2048>(hw[1][3], pb1_0); SB();
      mm0(ac1[1][2], hw[0][2], hx[0][1]); SB();
      lds_rd128<18432>(hx[1][1], px1_1); SB();
      mm0(ac1[1][1], hw[0][1], hx[0][1]); SB();
      lds_rd128<24576>(hx[1][2], px1_1); SB();
      mm0(ac1[1][0], hw[0][0], hx[0][1]); SB();
      lds_rd128<26624>(hx[1][3], px1_1); SB();
      mm0w<8>(ac1[2][0], hw[0][0], hx[0][2]); SB();
      mm0(ac1[2][1], hw[0][1], hx[0][2]); SB();
      mm0(ac1[2][2], hw[0][2], hx[0][2]); SB();
      mm0(ac1[2][3], hw[0][3], hx[0][2]); SB();
      mm0(ac1[3][3], hw[0][3], hx[0][3]); SB();
      mm0(ac1[3][2], hw[0][2], hx[0][3]); SB();
      asm volatile("s_waitcnt lgkmcnt(0)" : "+v"(hx[1][0]), "+v"(hw[1][0]), "+v"(hw[1][1]), "+v"(hw[1][2]), "+v"(hw[1][3]), "+v"(hx[1][1]), "+v"(hx[1][2]), "+v"(hx[1][3])::"memory"); SB();
      SIG(1); SB();
      mm0(ac1[3][1], hw[0][1], hx[0][3]); SB();
      mm0(ac1[3][0], hw[0][0], hx[0][3]); SB();
      mm(ac1[0][0], hw[1][0], hx[1][0]); SB();
      lds_rd32(fl, lf_rd); SB();
      mm(ac1[0][1], hw[1][1], hx[1][0]); SB();
      mm(ac1[0][2], hw[1][2], hx[1][0]); SB();
      mm(ac1[0][3], hw[1][3], hx[1][0]); SB();
      mm(ac1[1][3], hw[1][3], hx[1][1]); SB();
      mm(ac1[1][2], hw[1][2], hx[1][1]); SB();
      mmchkw<2, 0>(ac1[1][1], hw[1][1], hx[1][1], fl, lf_rd, bud); SB();
      lds_rd128<32768>(hx[0][0], px0_1); SB();
      mm(ac1[1][0], hw[1][0], hx[1][1]); SB();
      lds_rd128<16384>(hw[0][0], pa0_0); SB();
      mm(ac1[2][0], hw[1][0], hx[1][2]); SB();
      lds_rd128<18432>(hw[0][1], pa0_0); SB();
      mm(ac1[2][1], hw[1][1], hx[1][2]); SB();
      lds_rd128<16384>(hw[0][2], pb0_0); SB();
      mm(ac1[2][2], hw[1][2], hx[1][2]); SB();
      lds_rd128<18432>(hw[0][3], pb0_0); SB();
      mm(ac1[2][3], hw[1][3], hx[1][2]); SB();
      lds_rd128<34816>(hx[0][1], px0_1); SB();
      mm(ac1[3][3], hw[1][3], hx[1][3]); SB();
      lds_rd128<40960>(hx[0][2], px0_1); SB();
      mm(ac1[3][2], hw[1][2], hx[1][3]); SB();
      lds_rd128<43008>(hx[0][3], px0_1); SB();
      mm(ac1[3][1], hw[1][1], hx[1][3]); SB();
      mm(ac1[3][0], hw[1][0], hx[1][3]); SB();
      mmw<5>(ac1[0][0], hw[0][0], hx[0][0]); SB();
      lds_rd128<32768>(hx[1][0], px1_1); SB();
      mm(ac1[0][1], hw[0][1], hx[0][0]); SB();
      lds_rd128<16384>(hw[1][0], pa1_0); SB();
      mmw<5>(ac1[0][2], hw[0][2], hx[0][0]); SB();
      lds_rd128<18432>(hw[1][1], pa1_0); SB();
      mm(ac1[0][3], hw[0][3], hx[0][0]); SB();
      lds_rd128<16384>(hw[1][2], pb1_0); SB();
      mmw<5>(ac1[1][3], hw[0][3], hx[0][1]); SB();
      lds_rd128<18432>(hw[1][3], pb1_0); SB();
      mm(ac1[1][2], hw[0][2], hx[0][1]); SB();
      lds_rd128<34816>(hx[1][1], px1_1); SB();
      mm(ac1[1][1], hw[0][1], hx[0][1]); SB();
      lds_rd128<40960>(hx[1][2], px1_1); SB();
      mm(ac1[1][0], hw[0][0], hx[0][1]); SB();
      lds_rd128<43008>(hx[1][3], px1_1); SB();
      mm(ac1[2][0], hw[0][0], hx[0][2]); SB();
      mm(ac1[2][1], hw[0][1], hx[0][2]); SB();
      mm(ac1[2][2], hw[0][2], hx[0][2]); SB();
      mm(ac1[2][3], hw[0][3], hx[0][2]); SB();
      mmw<7>(ac1[3][3], hw[0][3], hx[0][3]); SB();
      mm(ac1[3][2], hw[0][2], hx[0][3]); SB();
      asm volatile("s_waitcnt lgkmcnt(0)" : "+v"(hw[1][0]), "+v"(hw[1][1]), "+v"(hw[1][2]), "+v"(hw[1][3]), "+v"(hx[1][1]), "+v"(hx[1][2]), "+v"(hx[1][3])::"memory"); SB();
      SIG(2); SB();
      mm(ac1[3][1], hw[0][1], hx[0][3]); SB();
      mm(ac1[3][0], hw[0][0], hx[0][3]); SB();
      mm(ac1[0][0], hw[1][0], hx[1][0]); SB();
      lds_rd32(fl, lf_rd); SB();
      mm(ac1[0][1], hw[1][1], hx[1][0]); SB();
      mm(ac1[0][2], hw[1][2], hx[1][0]); SB();
      mm(ac1[0][3], hw[1][3], hx[1][0]); SB();
      mm(ac1[1][3], hw[1][3], hx[1][1]); SB();
      mm(ac1[1][2], hw[1][2], hx[1][1]); SB();
      mmchkw<3, 0>(ac1[1][1], hw[1][1], hx[1][1], fl, lf_rd, bud); SB();
      lds_rd128<49152>(hx[0][0], px0_1); SB();
      mm(ac1[1][0], hw[1][0], hx[1][1]); SB();
      lds_rd128<32768>(hw[0][0], pa0_0); SB();
      mm(ac1[2][0], hw[1][0], hx[1][2]); SB();
      lds_rd128<34816>(hw[0][1], pa0_0); SB();
      mm(ac1[2][1], hw[1][1], hx[1][2]); SB();
      lds_rd128<32768>(hw[0][2], pb0_0); SB();
      mm(ac1[2][2], hw[1][2], hx[1][2]); SB();
      lds_rd128<34816>(hw[0][3], pb0_0); SB();
      mm(ac1[2][3], hw[1][3], hx[1][2]); SB();
      lds_rd128<51200>(hx[0][1], px0_1); SB();
      mm(ac1[3][3], hw[1][3], hx[1][3]); SB();
      lds_rd128<57344>(hx[0][2], px0_1); SB();
      mm(ac1[3][2], hw[1][2], hx[1][3]); SB();
      lds_rd128<59392>(hx[0][3], px0_1); SB();
      mm(ac1[3][1], hw[1][1], hx[1][3]); SB();
      mm(ac1[3][0], hw[1][0], hx[1][3]); SB();
      mmw<5>(ac1[0][0], hw[0][0], hx[0][0]); SB();
      lds_rd128<49152>(hx[1][0], px1_1); SB();
      mm(ac1[0][1], hw[0][1], hx[0][0]); SB();
      lds_rd128<32768>(hw[1][0], pa1_0); SB();
      mmw<5>(ac1[0][2], hw[0][2], hx[0][0]); SB();
      lds_rd128<34816>(hw[1][1], pa1_0); SB();
      mm(ac1[0][3], hw[0][3], hx[0][0]); SB();
      lds_rd128<32768>(hw[1][2], pb1_0); SB();
      mmw<5>(ac1[1][3], hw[0][3], hx[0][1]); SB();
      lds_rd128<34816>(hw[1][3], pb1_0); SB();
      mm(ac1[1][2], hw[0][2], hx[0][1]); SB();
      lds_rd128<51200>(hx[1][1], px1_1); SB();
      mm(ac1[1][1], hw[0][1], hx[0][1]); SB();
      lds_rd128<57344>(hx[1][2], px1_1); SB();
      mm(ac1[1][0], hw[0][0], hx[0][1]); SB();
      lds_rd128<59392>(hx[1][3], px1_1); SB();
      mm(ac1[2][0], hw[0][0], hx[0][2]); SB();
      mm(ac1[2][1], hw[0][1], hx[0][2]); SB();
      mm(ac1[2][2], hw[0][2], hx[0][2]); SB();
      mm(ac1[2][3], hw[0][3], hx[0][2]); SB();
      mmw<7>(ac1[3][3], hw[0][3], hx[0][3]); SB();
      mm(ac1[3][2], hw[0][2], hx[0][3]); SB();
      asm volatile("s_waitcnt lgkmcnt(0)" : "+v"(hw[1][0]), "+v"(hw[1][1]), "+v"(hw[1][2]), "+v"(hw[1][3]), "+v"(hx[1][1]), "+v"(hx[1][2]), "+v"(hx[1][3])::"memory"); SB();
      SIG(3); SB();
      mm(ac1[3][1], hw[0][1], hx[0][3]); SB();
      mm(ac1[3][0], hw[0][0], hx[0][3]); SB();
      mm(ac1[0][0], hw[1][0], hx[1][0]); SB();
      lds_rd32(fl, lf_rd); SB();
      mm(ac1[0][1], hw[1][1], hx[1][0]); SB();
      mm(ac1[0][2], hw[1][2], hx[1][0]); SB();
      mm(ac1[0][3], hw[1][3], hx[1][0]); SB();
      mm(ac1[1][3], hw[1][3], hx[1][1]); SB();
      mm(ac1[1][2], hw[1][2], hx[1][1]); SB();
      mmchkw<4, 0>(ac1[1][1], hw[1][1], hx[1][1], fl, lf_rd, bud); SB();
      lds_rd128<0>(hx[0][0], px0_2); SB();
      mm(ac1[1][0], hw[1][0], hx[1][1]); SB();
      lds_rd128<49152>(hw[0][0], pa0_0); SB();
      mm(ac1[2][0], hw[1][0], hx[1][2]); SB();
      lds_rd128<51200>(hw[0][1], pa0_0); SB();
      mm(ac1[2][1], hw[1][1], hx[1][2]); SB();
      lds_rd128<49152>(hw[0][2], pb0_0); SB();
      mm(ac1[2][2], hw[1][2], hx[1][2]); SB();
      lds_rd128<51200>(hw[0][3], pb0_0); SB();
      mm(ac1[2][3], hw[1][3], hx[1][2]); SB();
      lds_rd128<2048>(hx[0][1], px0_2); SB();
      mm(ac1[3][3], hw[1][3], hx[1][3]); SB();
      lds_rd128<8192>(hx[0][2], px0_2); SB();
      mm(ac1[3][2], hw[1][2], hx[1][3]); SB();
      lds_rd128<10240>(hx[0][3], px0_2); SB();
      mm(ac1[3][1], hw[1][1], hx[1][3]); SB();
      mm(ac1[3][0], hw[1][0], hx[1][3]); SB();
      mmw<5>(ac1[0][0], hw[0][0], hx[0][0]); SB();
      lds_rd128<0>(hx[1][0], px1_2); SB();
      mm(ac1[0][1], hw[0][1], hx[0][0]); SB();
      lds_rd128<49152>(hw[1][0], pa1_0); SB();
      mmw<5>(ac1[0][2], hw[0][2], hx[0][0]); SB();
      lds_rd128<51200>(hw[1][1], pa1_0); SB();
      mm(ac1[0][3], hw[0][3], hx[0][0]); SB();
      lds_rd128<49152>(hw[1][2], pb1_0); SB();
      mmw<5>(ac1[1][3], hw[0][3], hx[0][1]); SB();
      lds_rd128<51200>(hw[1][3], pb1_0); SB();
      mm(ac1[1][2], hw[0][2], hx[0][1]); SB();
      lds_rd128<2048>(hx[1][1], px1_2); SB();
      mm(ac1[1][1], hw[0][1], hx[0][1]); SB();
      lds_rd128<8192>(hx[1][2], px1_2); SB();
      mm(ac1[1][0], hw[0][0], hx[0][1]); SB();
      lds_rd128<10240>(hx[1][3], px1_2); SB();
      mm(ac1[2][0], hw[0][0], hx[0][2]); SB();
      mm(ac1[2][1], hw[0][1], hx[0][2]); SB();
      mm(ac1[2][2], hw[0][2], hx[0][2]); SB();
      mm(ac1[2][3], hw[0][3], hx[0][2]); SB();
      mmw<7>(ac1[3][3], hw[0][3], hx[0][3]); SB();
      mm(ac1[3][2], hw[0][2], hx[0][3]); SB();
      asm volatile("s_waitcnt lgkmcnt(0)" : "+v"(hw[1][0]), "+v"(hw[1][1]), "+v"(hw[1][2]), "+v"(hw[1][3]), "+v"(hx[1][1]), "+v"(hx[1][2]), "+v"(hx[1][3])::"memory"); SB();
      SIG(4); SB();
      mm(ac1[3][1], hw[0][1], hx[0][3]); SB();
      mm(ac1[3][0], hw[0][0], hx[0][3]); SB();
      mm(ac1[0][0], hw[1][0], hx[1][0]); SB();
      lds_rd32(fl, lf_rd); SB();
      mm(ac1[0][1], hw[1][1], hx[1][0]); SB();
      mm(ac1[0][2], hw[1][2], hx[1][0]); SB();
      mm(ac1[0][3], hw[1][3], hx[1][0]); SB();
      mm(ac1[1][3], hw[1][3], hx[1][1]); SB();
      mm(ac1[1][2], hw[1][2], hx[1][1]); SB();
      mmchkw<5, 0>(ac1[1][1], hw[1][1], hx[1][1], fl, lf_rd, bud); SB();
      lds_rd128<16384>(hx[0][0], px0_1); SB();
      mm(ac1[1][0], hw[1][0], hx[1][1]); SB();
      lds_rd128<0>(hw[0][0], pa0_1); SB();
      mm(ac1[2][0], hw[1][0], hx[1][2]); SB();
      lds_rd128<2048>(hw[0][1], pa0_1); SB();
      mm(ac1[2][1], hw[1][1], hx[1][2]); SB();
      lds_rd128<0>(hw[0][2], pb0_1); SB();
      mm(ac1[2][2], hw[1][2], hx[1][2]); SB();
      lds_rd128<2048>(hw[0][3], pb0_1); SB();
      mm(ac1[2][3], hw[1][3], hx[1][2]); SB();
      lds_rd128<18432>(hx[0][1], px0_1); SB();
      mm(ac1[3][3], hw[1][3], hx[1][3]); SB();
      lds_rd128<24576>(hx[0][2], px0_1); SB();
      mm(ac1[3][2], hw[1][2], hx[1][3]); SB();
      lds_rd128<26624>(hx[0][3], px0_1); SB();
      mm(ac1[3][1], hw[1][1], hx[1][3]); SB();
      mm(ac1[3][0], hw[1][0], hx[1][3]); SB();
      mmw<5>(ac1[0][0], hw[0][0], hx[0][0]); SB();
      lds_rd128<16384>(hx[1][0], px1_1); SB();
      mm(ac1[0][1], hw[0][1], hx[0][0]); SB();
      lds_rd128<0>(hw[1][0], pa1_1); SB();
      mmw<5>(ac1[0][2], hw[0][2], hx[0][0]); SB();
      lds_rd128<2048>(hw[1][1], pa1_1); SB();
      mm(ac1[0][3], hw[0][3], hx[0][0]); SB();
      lds_rd128<0>(hw[1][2], pb1_1); SB();
      mmw<5>(ac1[1][3], hw[0][3], hx[0][1]); SB();
      lds_rd128<2048>(hw[1][3], pb1_1); SB();
      mm(ac1[1][2], hw[0][2], hx[0][1]); SB();
      lds_rd128<18432>(hx[1][1], px1_1); SB();
      mm(ac1[1][1], hw[0][1], hx[0][1]); SB();
      lds_rd128<24576>(hx[1][2], px1_1); SB();
      mm(ac1[1][0], hw[0][0], hx[0][1]); SB();
      lds_rd128<26624>(hx[1][3], px1_1); SB();
      mm(ac1[2][0], hw[0][0], hx[0][2]); SB();
      mm(ac1[2][1], hw[0][1], hx[0][2]); SB();
      mm(ac1[2][2], hw[0][2], hx[0][2]); SB();
      mm(ac1[2][3], hw[0][3], hx[0][2]); SB();
      mmw<7>(ac1[3][3], hw[0][3], hx[0][3]); SB();
      mm(ac1[3][2], hw[0][2], hx[0][3]); SB();
      asm volatile("s_waitcnt lgkmcnt(0)" : "+v"(hw[1][0]), "+v"(hw[1][1]), "+v"(hw[1][2]), "+v"(hw[1][3]), "+v"(hx[1][1]), "+v"(hx[1][2]), "+v"(hx[1][3])::"memory"); SB();
      SIG(5); SB();
      mm(ac1[3][1], hw[0][1], hx[0][3]); SB();
      mm(ac1[3][0], hw[0][0], hx[0][3]); SB();
      P1_SETUP(); SB();
      mm(ac1[0][0], hw[1][0], hx[1][0]); SB();
      lds_rd32(fl, lf_rd); SB();
      mm(ac1[0][1], hw[1][1], hx[1][0]); SB();
      mm(ac1[0][2], hw[1][2], hx[1][0]); SB();
      mm(ac1[0][3], hw[1][3], hx[1][0]); SB();
      mm(ac1[1][3], hw[1][3], hx[1][1]); SB();
      mm(ac1[1][2], hw[1][2], hx[1][1]); SB();
      mmchkw<6, 0>(ac1[1][1], hw[1][1], hx[1][1], fl, lf_rd, bud); SB();
      lds_rd128<32768>(hx[0][0], px0_1); SB();
      mm(ac1[1][0], hw[1][0], hx[1][1]); SB();
      lds_rd128<0>(hw[0][0], pa0_0); SB();
      mm(ac1[2][0], hw[1][0], hx[1][2]); SB();
      lds_rd128<2048>(hw[0][1], pa0_0); SB();
      mm(ac1[2][1], hw[1][1], hx[1][2]); SB();
      lds_rd128<0>(hw[0][2], pb0_0); SB();
      mm(ac1[2][2], hw[1][2], hx[1][2]); SB();
      lds_rd128<2048>(hw[0][3], pb0_0); SB();
      mm(ac1[2][3], hw[1][3], hx[1][2]); SB();
      lds_rd128<34816>(hx[0][1], px0_1); SB();
      mm(ac1[3][3], hw[1][3], hx[1][3]); SB();
      lds_rd128<40960>(hx[0][2], px0_1); SB();
      mm(ac1[3][2], hw[1][2], hx[1][3]); SB();
      lds_rd128<43008>(hx[0][3], px0_1); SB();
      mm(ac1[3][1], hw[1][1], hx[1][3]); SB();
      mm(ac1[3][0], hw[1][0], hx[1][3]); SB();
      mmw<5>(ac1[0][0], hw[0][0], hx[0][0]); SB();
      lds_rd128<32768>(hx[1][0], px1_1); SB();
      mm(ac1[0][1], hw[0][1], hx[0][0]); SB();
      lds_rd128<0>(hw[1][0], pa1_0); SB();
      mmw<5>(ac1[0][2], hw[0][2], hx[0][0]); SB();
      lds_rd128<2048>(hw[1][1], pa1_0); SB();
      mm(ac1[0][3], hw[0][3], hx[0][0]); SB();
      lds_rd128<0>(hw[1][2], pb1_0); SB();
      mmw<5>(ac1[1][3], hw[0][3], hx[0][1]); SB();
      lds_rd128<2048>(hw[1][3], pb1_0); SB();
      mm(ac1[1][2], hw[0][2], hx[0][1]); SB();
      lds_rd128<34816>(hx[1][1], px1_1); SB();
      mm(ac1[1][1], hw[0][1], hx[0][1]); SB();
      lds_rd128<40960>(hx[1][2], px1_1); SB();
      mm(ac1[1][0], hw[0][0], hx[0][1]); SB();
      lds_rd128<43008>(hx[1][3], px1_1); SB();
      mm(ac1[2][0], hw[0][0], hx[0][2]); SB();
      mm(ac1[2][1], hw[0][1], hx[0][2]); SB();
      mm(ac1[2][2], hw[0][2], hx[0][2]); SB();
      mm(ac1[2][3], hw[0][3], hx[0][2]); SB();
      mmw<7>(ac1[3][3], hw[0][3], hx[0][3]); SB();
      mm(ac1[3][2], hw[0][2], hx[0][3]); SB();
      asm volatile("s_waitcnt lgkmcnt(0)" : "+v"(hw[1][0]), "+v"(hw[1][1]), "+v"(hw[1][2]), "+v"(hw[1][3]), "+v"(hx[1][1]), "+v"(hx[1][2]), "+v"(hx[1][3])::"memory"); SB();
      SIG(6); SB();
      mm(ac1[3][1], hw[0][1], hx[0][3]); SB();
      mm(ac1[3][0], hw[0][0], hx[0][3]); SB();
      mm(ac1[0][0], hw[1][0], hx[1][0]); SB();
      lds_rd32(fl, lf_rd); SB();
      mm(ac1[0][1], hw[1][1], hx[1][0]); SB();
      mm(ac1[0][2], hw[1][2], hx[1][0]); SB();
      mm(ac1[0][3], hw[1][3], hx[1][0]); SB();
      mm(ac1[1][3], hw[1][3], hx[1][1]); SB();
      mm(ac1[1][2], hw[1][2], hx[1][1]); SB();
      mmchkw<7, 0>(ac1[1][1], hw[1][1], hx[1][1], fl, lf_rd, bud); SB();
      lds_rd128<49152>(hx[0][0], px0_1); SB();
      mm(ac1[1][0], hw[1][0], hx[1][1]); SB();
      lds_rd128<16384>(hw[0][0], pa0_0); SB();
      mm(ac1[2][0], hw[1][0], hx[1][2]); SB();
      lds_rd128<18432>(hw[0][1], pa0_0); SB();
      mm(ac1[2][1], hw[1][1], hx[1][2]); SB();
      lds_rd128<16384>(hw[0][2], pb0_0); SB();
      mm(ac1[2][2], hw[1][2], hx[1][2]); SB();
      lds_rd128<18432>(hw[0][3], pb0_0); SB();
      mm(ac1[2][3], hw[1][3], hx[1][2]); SB();
      lds_rd128<51200>(hx[0][1], px0_1); SB();
      mm(ac1[3][3], hw[1][3], hx[1][3]); SB();
      lds_rd128<57344>(hx[0][2], px0_1); SB();
      mm(ac1[3][2], hw[1][2], hx[1][3]); SB();
      lds_rd128<59392>(hx[0][3], px0_1); SB();
      mm(ac1[3][1], hw[1][1], hx[1][3]); SB();
      mm(ac1[3][0], hw[1][0], hx[1][3]); SB();
      mmw<5>(ac1[0][0], hw[0][0], hx[0][0]); SB();
      lds_rd128<49152>(hx[1][0], px1_1); SB();
      mm(ac1[0][1], hw[0][1], hx[0][0]); SB();
      lds_rd128<16384>(hw[1][0], pa1_0); SB();
      mmw<5>(ac1[0][2], hw[0][2], hx[0][0]); SB();
      lds_rd128<18432>(hw[1][1], pa1_0); SB();
      mm(ac1[0][3], hw[0][3], hx[0][0]); SB();
      lds_rd128<16384>(hw[1][2], pb1_0); SB();
      mmw<5>(ac1[1][3], hw[0][3], hx[0][1]); SB();
      lds_rd128<18432>(hw[1][3], pb1_0); SB();
      mm(ac1[1][2], hw[0][2], hx[0][1]); SB();
      lds_rd128<51200>(hx[1][1], px1_1); SB();
      mm(ac1[1][1], hw[0][1], hx[0][1]); SB();
      lds_rd128<57344>(hx[1][2], px1_1); SB();
      mm(ac1[1][0], hw[0][0], hx[0][1]); SB();
      lds_rd128<59392>(hx[1][3], px1_1); SB();
      mm(ac1[2][0], hw[0][0], hx[0][2]); SB();
      mm(ac1[2][1], hw[0][1], hx[0][2]); SB();
      mm(ac1[2][2], hw[0][2], hx[0][2]); SB();
      mm(ac1[2][3], hw[0][3], hx[0][2]); SB();
      mmw<7>(ac1[3][3], hw[0][3], hx[0][3]); SB();
      mm(ac1[3][2], hw[0][2], hx[0][3]); SB();
      asm volatile("s_waitcnt lgkmcnt(0)" : "+v"(hw[1][0]), "+v"(hw[1][1]), "+v"(hw[1][2]), "+v"(hw[1][3]), "+v"(hx[1][1]), "+v"(hx[1][2]), "+v"(hx[1][3])::"memory"); SB();
      SIG(7); SB();
      mm(ac1[3][1], hw[0][1], hx[0][3]); SB();
      mm(ac1[3][0], hw[0][0], hx[0][3]); SB();
      mm(ac1[0][0], hw[1][0], hx[1][0]); SB();
      lds_rd32(fl, lf_rd); SB();
      mm(ac1[0][1], hw[1][1], hx[1][0]); SB();
      mm(ac1[0][2], hw[1][2], hx[1][0]); SB();
      mm(ac1[0][3], hw[1][3], hx[1][0]); SB();
      mm(ac1[1][3], hw[1][3], hx[1][1]); SB();
      mm(ac1[1][2], hw[1][2], hx[1][1]); SB();
      mmchkw<8, 0>(ac1[1][1], hw[1][1], hx[1][1], fl, lf_rd, bud); SB();
      lds_rd128<0>(hx[0][0], px0_2); SB();
      mm(ac1[1][0], hw[1][0], hx[1][1]); SB();
      lds_rd128<32768>(hw[0][0], pa0_0); SB();
      mm(ac1[2][0], hw[1][0], hx[1][2]); SB();
      lds_rd128<34816>(hw[0][1], pa0_0); SB();
      mm(ac1[2][1], hw[1][1], hx[1][2]); SB();
      lds_rd128<32768>(hw[0][2], pb0_0); SB();
      mm(ac1[2][2], hw[1][2], hx[1][2]); SB();
      lds_rd128<34816>(hw[0][3], pb0_0); SB();
      mm(ac1[2][3], hw[1][3], hx[1][2]); SB();
      lds_rd128<2048>(hx[0][1], px0_2); SB();
      mm(ac1[3][3], hw[1][3], hx[1][3]); SB();
      lds_rd128<8192>(hx[0][2], px0_2); SB();
      mm(ac1[3][2], hw[1][2], hx[1][3]); SB();
      lds_rd128<10240>(hx[0][3], px0_2); SB();
      mm(ac1[3][1], hw[1][1], hx[1][3]); SB();
      mm(ac1[3][0], hw[1][0], hx[1][3]); SB();
      mmw<5>(ac1[0][0], hw[0][0], hx[0][0]); SB();
      lds_rd128<0>(hx[1][0], px1_2); SB();
      mmw<3>(ac1[1][0], hw[0][0], hx[0][1]); SB();
      lds_rd128<32768>(hw[1][0], pa1_0); SB();
      mm(ac1[0][1], hw[0][1], hx[0][0]); SB();
      lds_rd128<34816>(hw[1][1], pa1_0); SB();
      mm(ac1[1][1], hw[0][1], hx[0][1]); SB();
      lds_rd128<32768>(hw[1][2], pb1_0); SB();
      mm(ac1[0][2], hw[0][2], hx[0][0]); SB();
      lds_rd128<34816>(hw[1][3], pb1_0); SB();
      mm(ac1[1][2], hw[0][2], hx[0][1]); SB();
      lds_rd128<2048>(hx[1][1], px1_2); SB();
      mm(ac1[0][3], hw[0][3], hx[0][0]); SB();
      lds_rd128<8192>(hx[1][2], px1_2); SB();
      mm(ac1[1][3], hw[0][3], hx[0][1]); SB();
      lds_rd128<10240>(hx[1][3], px1_2); SB();
      mmw<6>(ac1[0][0], hw[1][0], hx[1][0]); SB();
      mmw<2>(ac1[1][0], hw[1][0], hx[1][1]); SB();
      mm(ac1[0][1], hw[1][1], hx[1][0]); SB();
      mm(ac1[1][1], hw[1][1], hx[1][1]); SB();
      mm(ac1[0][2], hw[1][2], hx[1][0]); SB();
      asm volatile("s_waitcnt lgkmcnt(0)" : "+v"(hx[1][2]), "+v"(hx[1][3])::"memory"); SB();
      SIG(8); SB();
      mm(ac1[1][2], hw[1][2], hx[1][1]); SB();
      PUB0(0, 0); SB();
      mm(ac1[0][3], hw[1][3], hx[1][0]); SB();
      PUB1(0, 0); SB();
      mm(ac1[1][3], hw[1][3], hx[1][1]); SB();
      PUB2(); SB();
      mm(ac1[2][0], hw[0][0], hx[0][2]); SB();
      PUB3(0, 0); SB();
      mm(ac1[3][0], hw[0][0], hx[0][3]); SB();
      PUB0(0, 1); SB();
      mm(ac1[2][0], hw[1][0], hx[1][2]); SB();
      PUB1(0, 1); SB();
      mm(ac1[3][0], hw[1][0], hx[1][3]); SB();
      PUB2(); SB();
      mm(ac1[2][1], hw[0][1], hx[0][2]); SB();
      PUB3(0, 1); SB();
      mm(ac1[3][1], hw[0][1], hx[0][3]); SB();
      PUB0(0, 2); SB();
      mm(ac1[2][1], hw[1][1], hx[1][2]); SB();
      PUB1(0, 2); SB();
      mm(ac1[3][1], hw[1][1], hx[1][3]); SB();
      PUB2(); SB();
      mm(ac1[2][2], hw[0][2], hx[0][2]); SB();
      PUB3(0, 2); SB();
      mm(ac1[3][2], hw[0][2], hx[0][3]); SB();
      PUB0(0, 3); SB();
      mm(ac1[2][2], hw[1][2], hx[1][2]); SB();
      PUB1(0, 3); SB();
      mm(ac1[3][2], hw[1][2], hx[1][3]); SB();
      PUB2(); SB();
      mm(ac1[2][3], hw[0][3], hx[0][2]); SB();
      PUB3(0, 3); SB();
      mm(ac1[3][3], hw[0][3], hx[0][3]); SB();
      PUB0(2, 0); SB();
      mm(ac1[2][3], hw[1][3], hx[1][2]); SB();
      PUB1(2, 0); SB();
      mm(ac1[3][3], hw[1][3], hx[1][3]); SB();
      PUB2(); SB();
      asm volatile("s_nop 7" : "+v"(ac1[2][3]), "+v"(ac1[3][3]), "+v"(ac1[2][2]), "+v"(ac1[3][2])); SB();
      PUB3(2, 0); SB();
      PUB0(2, 1); SB();
      PUB1(2, 1); SB();
      PUB2(); SB();
      PUB3(2, 1); SB();
      PUB0(2, 2); SB();
      PUB1(2, 2); SB();
      PUB2(); SB();
      PUB3(2, 2); SB();
      PUB0(2, 3); SB();
      PUB1(2, 3); SB();
      PUB2(); SB();
      PUB3(2, 3); SB();
      OWN_SIG(1);
    }
    if (isq) {
      SIG(8);
      OWN_SIG(1);
      if (wid == 0) { UNTIL(144u, 3075u); TOUR(); }
    }
    SPIN(tg_rd, 1);
    const unsigned tag = __builtin_amdgcn_readfirstlane(lflag[15]);
    tagv = tag;
    asm volatile("" : "+v"(tagv));
    f32x4 ac[4][2][2];
    const unsigned xg = (unsigned)((ti * 4 + ej) * 98304 + q * 24576 + wid * 2048 + lane * 16);
    const char* xb = ws + W_XB;
    const unsigned hw_ = sb + 147456 + (wid * 32 + fr) * 64 + (((fq >> 1) ^ (fr >> 2)) << 4) + (fq & 1) * 8;
    const unsigned tb[4] = {tag & 0x70007u, tag >> 3 & 0x70007u, tag >> 6 & 0x70007u, tag >> 9 & 0x70004u};
    unsigned pw[8], em[2], ek[2], msk = 0x70007u;
    u64 nz;
#define AV(G, i) ac[G][(i) >> 2 & 1][(i) >> 3][(i) & 3]
#define EM0(G, c) asm volatile("v_max3_f32 %0, |%1|, |%2|, |%3|" : "=v"(em[c]) : "v"(AV(G, 8 * (c))), "v"(AV(G, 8 * (c) + 1)), "v"(AV(G, 8 * (c) + 2)))
#define EM1(G, c, i, j) asm volatile("v_max3_f32 %0, %0, |%1|, |%2|" : "+v"(em[c]) : "v"(AV(G, i)), "v"(AV(G, j)))
#define EM2() asm volatile("v_max_f32 %0, %0, %1" : "+v"(em[0]) : "v"(em[1]))
#define ES0() asm volatile("v_subrev_u32 %0, 0x39800000, %1" : "=v"(em[1]) : "v"(em[0]))
#define ES1() asm volatile("v_cmp_ne_u32 %0, 0, %1" : "=s"(nz) : "v"(em[0]))
#define EK(c) asm volatile("v_or_b32 %0, %1, %2" : "=v"(ek[c]) : "i"(2 * (c)), "s"(tb[3]))
#define EH(G, P) asm volatile("v_cvt_pk_f16_f32 %0, %1, %2" : "=v"(pw[P]) : "v"(AV(G, 2 * (P))), "v"(AV(G, 2 * (P) + 1)))
#define ET(P) asm volatile("v_bfi_b32 %0, %1, %2, %0" : "+v"(pw[P]) : "v"(msk), "s"(tb[(P) & 3]))
#define EU(P) asm volatile("v_bfi_b32 %0, %1, %2, %0" : "+v"(pw[P]) : "v"(msk), "v"(ek[(P) >> 2]))
#define EX(G) asm volatile("v_cmp_le_u32 vcc, 0x0d800000, %17\n\ts_and_b64 vcc, vcc, %20\n\ts_cbranch_scc1 .Lxa%=\n.Lxb%=:\n\t.subsection 1\n.Lxa%=:\n\t"            \
      "v_lshrrev_b32 %17, 23, %16\n\tv_add_u32 %17, 6, %17\n\tv_lshrrev_b32 %17, 4, %17\n\tv_med3_u32 %17, %17, 1, 15\n\tv_lshlrev_b32 %16, 27, %17\n\t"              \
      "v_sub_u32 %16, 0x7f800000, %16\n\tv_and_or_b32 %18, %17, 3, %21\n\tv_lshrrev_b32 %17, 2, %17\n\tv_or_b32 %19, %21, %17\n\t"                                  \
      "v_mul_f32 %0, %0, %16\n\tv_mul_f32 %1, %1, %16\n\tv_mul_f32 %2, %2, %16\n\tv_mul_f32 %3, %3, %16\n\tv_mul_f32 %4, %4, %16\n\tv_mul_f32 %5, %5, %16\n\t"       \
      "v_mul_f32 %6, %6, %16\n\tv_mul_f32 %7, %7, %16\n\tv_mul_f32 %8, %8, %16\n\tv_mul_f32 %9, %9, %16\n\tv_mul_f32 %10, %10, %16\n\tv_mul_f32 %11, %11, %16\n\t"   \
      "v_mul_f32 %12, %12, %16\n\tv_mul_f32 %13, %13, %16\n\tv_mul_f32 %14, %14, %16\n\tv_mul_f32 %15, %15, %16\n\ts_branch .Lxb%=\n\t.subsection 0"               \
      : "+v"(AV(G, 0)), "+v"(AV(G, 1)), "+v"(AV(G, 2)), "+v"(AV(G, 3)), "+v"(AV(G, 4)), "+v"(AV(G, 5)), "+v"(AV(G, 6)), "+v"(AV(G, 7)), "+v"(AV(G, 8)), "+v"(AV(G, 9)),  \
        "+v"(AV(G, 10)), "+v"(AV(G, 11)), "+v"(AV(G, 12)), "+v"(AV(G, 13)), "+v"(AV(G, 14)), "+v"(AV(G, 15)), "+v"(em[0]), "+v"(em[1]), "+v"(ek[0]), "+v"(ek[1])        \
      : "s"(nz), "s"(tb[3]) : "vcc", "scc")
    const char *xd0 = xb + ((q ^ 3) - q) * 24576, *xd1 = xb + ((q ^ 2) - q) * 24576 + 8192, *xd2 = xb + ((q ^ 1) - q) * 24576 + 16384;
    const char *xr0 = xb, *xr1 = xb + 8192, *xr2 = xb + 16384;
    asm volatile("" : "+s"(xd0), "+s"(xd1), "+s"(xd2), "+s"(xr0), "+s"(xr1), "+s"(xr2));
#define SEND(G, Q) asm volatile("global_store_dwordx4 %0, %1, %2 offset:%3 sc1\n\ts_nop 1" ::"v"(xg), "v"(u32x4{pw[4 * (Q)], pw[4 * (Q) + 1], pw[4 * (Q) + 2], pw[4 * (Q) + 3]}), "s"(xd##G), "i"((Q) * 1024) : "memory")
    unsigned oo;
#define OO() do { oo = (unsigned)(((ti * 128 + wid * 32 + fr) * D_ + e0 + (fq & 1) * 16 + (fq >> 1) * 8) * 2); asm volatile("" : "+v"(oo)); } while (0)
    unsigned sn = 9 * 4u;
    float gs[2];
    u32x4 rv[3][2];
    u32x2 dq[4];
    unsigned f2;
    const unsigned d2_rd = best_rd;
#define RV_LD(S, tail) asm volatile("global_load_dwordx4 %0, %2, %3 sc1\n\tglobal_load_dwordx4 %1, %2, %3 offset:1024 sc1" tail \
                                    : "=&v"(rv[S][0]), "=&v"(rv[S][1]) : "v"(xg), "s"(xr##S) : "memory")
#define D_RD(i) asm volatile("ds_read_b64 %0, %1 offset:%2" : "=v"(dq[i]) : "v"(hw_ ^ ((i) & 1) * 32u), "i"(((i) >> 1) * 1024) : "memory")
#define G_RD(i) asm volatile("ds_read_b32 %0, %1 offset:%2" : "=v"(gs[i]) : "v"(sb + RING + L_G + (wid * 32 + fr) * 4), "i"((i) * 64) : "memory")
#define SIGQ(W) do { __builtin_amdgcn_s_waitcnt(0xc07f | ((W) << 8)); SB(); asm volatile("global_load_lds_dword %0, %1" ::"v"(sn), "s"(tab) : "memory"); } while (0)
#define SIGN() asm volatile("v_add_u32 %0, 4, %0" : "+v"(sn))
    asm volatile("s_mov_b32 m0, %2" : "+v"(sn), "+v"(msk) : "s"(row_s) : "memory");
    SPINF(lf_rd, 9, 0); SB();
    lds_rd128<49152>(wr[0][0], wa0_0); SB();
    lds_rd128<51200>(wr[0][1], wa0_0); SB();
    lds_rd128<49152>(wr[0][2], wa1_0); SB();
    lds_rd128<51200>(wr[0][3], wa1_0); SB();
    lds_rd128<0>(wr[1][0], wa0_1); SB();
    lds_rd128<2048>(wr[1][1], wa0_1); SB();
    lds_rd128<0>(wr[1][2], wa1_1); SB();
    lds_rd128<2048>(wr[1][3], wa1_1); SB();
    asm volatile("s_waitcnt lgkmcnt(0)" : "+v"(wr[0][0]), "+v"(wr[0][1]), "+v"(wr[0][2]), "+v"(wr[0][3]), "+v"(wr[1][0]), "+v"(wr[1][1]), "+v"(wr[1][2]), "+v"(wr[1][3])::"memory"); SB();
    OWN_SIG(2); SB();
    TRYFLAG(0); SB();
    SPINF(lf_rd, 10, 0); SB();
    lds_rd128<16384>(wr[2][0], wa0_1); SB();
    lds_rd128<18432>(wr[2][1], wa0_1); SB();
    lds_rd128<16384>(wr[2][2], wa1_1); SB();
    lds_rd128<18432>(wr[2][3], wa1_1); SB();
    lds_rd128<0>(wr[3][0], wa0_0); SB();
    lds_rd128<2048>(wr[3][1], wa0_0); SB();
    lds_rd128<0>(wr[3][2], wa1_0); SB();
    lds_rd128<2048>(wr[3][3], wa1_0); SB();
    asm volatile("s_waitcnt lgkmcnt(0)" : "+v"(wr[2][0]), "+v"(wr[2][1]), "+v"(wr[2][2]), "+v"(wr[2][3]), "+v"(wr[3][0]), "+v"(wr[3][1]), "+v"(wr[3][2]), "+v"(wr[3][3])::"memory"); SB();
    OWN_SIG(3); SB();
    TRYFLAG(0); SB();
    SPINF(lf_rd, 11, 0); SB();
    lds_rd128<32768>(wr[4][0], wa0_1); SB();
    lds_rd128<34816>(wr[4][1], wa0_1); SB();
    lds_rd128<32768>(wr[4][2], wa1_1); SB();
    lds_rd128<34816>(wr[4][3], wa1_1); SB();
    lds_rd128<16384>(wr[5][0], wa0_0); SB();
    lds_rd128<18432>(wr[5][1], wa0_0); SB();
    lds_rd128<16384>(wr[5][2], wa1_0); SB();
    lds_rd128<18432>(wr[5][3], wa1_0); SB();
    asm volatile("s_waitcnt lgkmcnt(0)" : "+v"(wr[4][0]), "+v"(wr[4][1]), "+v"(wr[4][2]), "+v"(wr[4][3]), "+v"(wr[5][0]), "+v"(wr[5][1]), "+v"(wr[5][2]), "+v"(wr[5][3])::"memory"); SB();
    OWN_SIG(4); SB();
    TRYFLAG(0); SB();
    SPINF(lf_rd, 12, 0); SB();
    lds_rd128<49152>(wr[6][0], wa0_1); SB();
    lds_rd128<51200>(wr[6][1], wa0_1); SB();
    lds_rd128<49152>(wr[6][2], wa1_1); SB();
    lds_rd128<51200>(wr[6][3], wa1_1); SB();
    lds_rd128<32768>(wr[7][0], wa0_0); SB();
    lds_rd128<34816>(wr[7][1], wa0_0); SB();
    lds_rd128<32768>(wr[7][2], wa1_0); SB();
    lds_rd128<34816>(wr[7][3], wa1_0); SB();
    asm volatile("s_waitcnt lgkmcnt(0)" : "+v"(wr[6][0]), "+v"(wr[6][1]), "+v"(wr[6][2]), "+v"(wr[6][3]), "+v"(wr[7][0]), "+v"(wr[7][1]), "+v"(wr[7][2]), "+v"(wr[7][3])::"memory"); SB();
    OWN_SIG(5); SB();
    TRYFLAG(0); SB();
    FLAG(0); SB();
    OO();
    SPIN(lf_rd, 13); SB();
    lds_rd128<0>(fb[0], xa_0); SB();
    lds_rd128<1024>(fb[1], xa_0); SB();
    lds_rd128<2048>(fb[2], xa_0); SB();
    lds_rd128<3072>(fb[3], xa_0); SB();
    lds_rd128<4096>(fb[4], xa_0); SB();
    lds_rd128<5120>(fb[5], xa_0); SB();
    lds_rd32(fl, lf_rd); SB();
    mm0w<6>(ac[0][0][0], fb[0], wr[0][0]); SB();
    lds_rd128<6144>(fb[6], xa_0); SB();
    mm0(ac[0][0][1], fb[0], wr[0][1]); SB();
    mm0w<6>(ac[0][1][1], fb[1], wr[0][1]); SB();
    lds_rd128<7168>(fb[7], xa_0); SB();
    mm0(ac[0][1][0], fb[1], wr[0][0]); SB();
    mm0w<2>(ac[1][0][0], fb[2], wr[0][0]); SB();
    chk<14>(fl, lf_rd, bud); SB();
    lds_rd128<8192>(fb[8], xa_0); SB();
    mm0(ac[1][0][1], fb[2], wr[0][1]); SB();
    mm0(ac[1][1][1], fb[3], wr[0][1]); SB();
    lds_rd128<9216>(fb[0], xa_0); SB();
    mm0(ac[1][1][0], fb[3], wr[0][0]); SB();
    mm(ac[0][0][0], fb[4], wr[0][2]); SB();
    lds_rd128<10240>(fb[1], xa_0); SB();
    SIGQ(3); SB();
    mm(ac[0][0][1], fb[4], wr[0][3]); SB();
    mm(ac[0][1][1], fb[5], wr[0][3]); SB();
    lds_rd128<11264>(fb[2], xa_0); SB();
    SIGN(); SB();
    mm(ac[0][1][0], fb[5], wr[0][2]); SB();
    mm(ac[1][0][0], fb[6], wr[0][2]); SB();
    lds_rd128<12288>(fb[3], xa_0); SB();
    mm(ac[1][0][1], fb[6], wr[0][3]); SB();
    mm(ac[1][1][1], fb[7], wr[0][3]); SB();
    lds_rd128<13312>(fb[4], xa_0); SB();
    lds_rd32(fl, lf_rd); SB();
    mm(ac[1][1][0], fb[7], wr[0][2]); SB();
    mm0w<5>(ac[2][0][0], fb[8], wr[0][0]); SB();
    lds_rd128<14336>(fb[5], xa_0); SB();
    mm0(ac[2][0][1], fb[8], wr[0][1]); SB();
    mm0(ac[2][1][1], fb[0], wr[0][1]); SB();
    lds_rd128<15360>(fb[6], xa_0); SB();
    mm0(ac[2][1][0], fb[0], wr[0][0]); SB();
    mm0w<2>(ac[3][0][0], fb[1], wr[0][0]); SB();
    chk<15>(fl, lf_rd, bud); SB();
    lds_rd128<32768>(fb[7], xa_1); SB();
    mm0(ac[3][0][1], fb[1], wr[0][1]); SB();
    mm0(ac[3][1][1], fb[2], wr[0][1]); SB();
    lds_rd128<33792>(fb[8], xa_1); SB();
    mm0(ac[3][1][0], fb[2], wr[0][0]); SB();
    mm(ac[2][0][0], fb[3], wr[0][2]); SB();
    lds_rd128<34816>(fb[0], xa_1); SB();
    SIGQ(3); SB();
    mm(ac[2][0][1], fb[3], wr[0][3]); SB();
    mm(ac[2][1][1], fb[4], wr[0][3]); SB();
    lds_rd128<35840>(fb[1], xa_1); SB();
    SIGN(); SB();
    mm(ac[2][1][0], fb[4], wr[0][2]); SB();
    mm(ac[3][0][0], fb[5], wr[0][2]); SB();
    lds_rd128<36864>(fb[2], xa_1); SB();
    mm(ac[3][0][1], fb[5], wr[0][3]); SB();
    mm(ac[3][1][1], fb[6], wr[0][3]); SB();
    lds_rd128<37888>(fb[3], xa_1); SB();
    lds_rd32(fl, lf_rd); SB();
    mm(ac[3][1][0], fb[6], wr[0][2]); SB();
    mmw<5>(ac[0][0][0], fb[7], wr[1][0]); SB();
    lds_rd128<38912>(fb[4], xa_1); SB();
    mm(ac[0][0][1], fb[7], wr[1][1]); SB();
    mm(ac[0][1][1], fb[8], wr[1][1]); SB();
    lds_rd128<39936>(fb[5], xa_1); SB();
    mm(ac[0][1][0], fb[8], wr[1][0]); SB();
    mmchkw<16, 2>(ac[1][0][0], fb[0], wr[1][0], fl, lf_rd, bud); SB();
    lds_rd128<40960>(fb[6], xa_1); SB();
    mm(ac[1][0][1], fb[0], wr[1][1]); SB();
    lds_rd128<0>(wr[8][0], wa0_2); SB();
    mm(ac[1][1][1], fb[1], wr[1][1]); SB();
    lds_rd128<41984>(fb[7], xa_1); SB();
    lds_rd128<2048>(wr[8][1], wa0_2); SB();
    mm(ac[1][1][0], fb[1], wr[1][0]); SB();
    lds_rd128<0>(wr[8][2], wa1_2); SB();
    mm(ac[0][0][0], fb[2], wr[1][2]); SB();
    lds_rd128<43008>(fb[8], xa_1); SB();
    lds_rd128<2048>(wr[8][3], wa1_2); SB();
    mm(ac[0][0][1], fb[2], wr[1][3]); SB();
    mm(ac[0][1][1], fb[3], wr[1][3]); SB();
    lds_rd128<44032>(fb[0], xa_1); SB();
    mm(ac[0][1][0], fb[3], wr[1][2]); SB();
    mmw<8>(ac[1][0][0], fb[4], wr[1][2]); SB();
    lds_rd128<45056>(fb[1], xa_1); SB();
    mm(ac[1][0][1], fb[4], wr[1][3]); SB();
    mm(ac[1][1][1], fb[5], wr[1][3]); SB();
    lds_rd128<46080>(fb[2], xa_1); SB();
    lds_rd32(fl, lf_rd); SB();
    mm(ac[1][1][0], fb[5], wr[1][2]); SB();
    mmw<9>(ac[2][0][0], fb[6], wr[1][0]); SB();
    lds_rd128<47104>(fb[3], xa_1); SB();
    mm(ac[2][0][1], fb[6], wr[1][1]); SB();
    mmw<8>(ac[2][1][1], fb[7], wr[1][1]); SB();
    lds_rd128<48128>(fb[4], xa_1); SB();
    mm(ac[2][1][0], fb[7], wr[1][0]); SB();
    mmchkw<17, 2>(ac[3][0][0], fb[8], wr[1][0], fl, lf_rd, bud); SB();
    lds_rd128<16384>(fb[5], xa_0); SB();
    mm(ac[3][0][1], fb[8], wr[1][1]); SB();
    mm(ac[3][1][1], fb[0], wr[1][1]); SB();
    lds_rd128<17408>(fb[6], xa_0); SB();
    mm(ac[3][1][0], fb[0], wr[1][0]); SB();
    mm(ac[2][0][0], fb[1], wr[1][2]); SB();
    lds_rd128<18432>(fb[7], xa_0); SB();
    SIGQ(3); SB();
    mm(ac[2][0][1], fb[1], wr[1][3]); SB();
    mm(ac[2][1][1], fb[2], wr[1][3]); SB();
    lds_rd128<19456>(fb[8], xa_0); SB();
    SIGN(); SB();
    mm(ac[2][1][0], fb[2], wr[1][2]); SB();
    mm(ac[3][0][0], fb[3], wr[1][2]); SB();
    lds_rd128<20480>(fb[0], xa_0); SB();
    mm(ac[3][0][1], fb[3], wr[1][3]); SB();
    mm(ac[3][1][1], fb[4], wr[1][3]); SB();
    lds_rd128<21504>(fb[1], xa_0); SB();
    mm(ac[3][1][0], fb[4], wr[1][2]); SB();
    mmw<4>(ac[0][0][0], fb[5], wr[2][0]); SB();
    lds_rd128<22528>(fb[2], xa_0); SB();
    mm(ac[0][0][1], fb[5], wr[2][1]); SB();
    mm(ac[0][1][1], fb[6], wr[2][1]); SB();
    lds_rd128<23552>(fb[3], xa_0); SB();
    mm(ac[0][1][0], fb[6], wr[2][0]); SB();
    mmw<4>(ac[1][0][0], fb[7], wr[2][0]); SB();
    lds_rd128<24576>(fb[4], xa_0); SB();
    mm(ac[1][0][1], fb[7], wr[2][1]); SB();
    lds_rd128<49152>(wr[9][0], wa0_0); SB();
    mm(ac[1][1][1], fb[8], wr[2][1]); SB();
    lds_rd128<25600>(fb[5], xa_0); SB();
    lds_rd128<51200>(wr[9][1], wa0_0); SB();
    mm(ac[1][1][0], fb[8], wr[2][0]); SB();
    lds_rd128<49152>(wr[9][2], wa1_0); SB();
    mmw<7>(ac[0][0][0], fb[0], wr[2][2]); SB();
    lds_rd128<26624>(fb[6], xa_0); SB();
    lds_rd128<51200>(wr[9][3], wa1_0); SB();
    mm(ac[0][0][1], fb[0], wr[2][3]); SB();
    mm(ac[0][1][1], fb[1], wr[2][3]); SB();
    lds_rd128<27648>(fb[7], xa_0); SB();
    mm(ac[0][1][0], fb[1], wr[2][2]); SB();
    mmw<8>(ac[1][0][0], fb[2], wr[2][2]); SB();
    lds_rd128<28672>(fb[8], xa_0); SB();
    mm(ac[1][0][1], fb[2], wr[2][3]); SB();
    mm(ac[1][1][1], fb[3], wr[2][3]); SB();
    lds_rd128<29696>(fb[0], xa_0); SB();
    lds_rd32(fl, lf_rd); SB();
    mm(ac[1][1][0], fb[3], wr[2][2]); SB();
    mmw<9>(ac[2][0][0], fb[4], wr[2][0]); SB();
    lds_rd128<30720>(fb[1], xa_0); SB();
    mm(ac[2][0][1], fb[4], wr[2][1]); SB();
    mmw<8>(ac[2][1][1], fb[5], wr[2][1]); SB();
    lds_rd128<31744>(fb[2], xa_0); SB();
    mm(ac[2][1][0], fb[5], wr[2][0]); SB();
    mmchkw<18, 2>(ac[3][0][0], fb[6], wr[2][0], fl, lf_rd, bud); SB();
    lds_rd128<49152>(fb[3], xa_1); SB();
    mm(ac[3][0][1], fb[6], wr[2][1]); SB();
    mm(ac[3][1][1], fb[7], wr[2][1]); SB();
    lds_rd128<50176>(fb[4], xa_1); SB();
    mm(ac[3][1][0], fb[7], wr[2][0]); SB();
    mm(ac[2][0][0], fb[8], wr[2][2]); SB();
    lds_rd128<51200>(fb[5], xa_1); SB();
    SIGQ(3); SB();
    mm(ac[2][0][1], fb[8], wr[2][3]); SB();
    mm(ac[2][1][1], fb[0], wr[2][3]); SB();
    lds_rd128<52224>(fb[6], xa_1); SB();
    SIGN(); SB();
    mm(ac[2][1][0], fb[0], wr[2][2]); SB();
    mm(ac[3][0][0], fb[1], wr[2][2]); SB();
    lds_rd128<53248>(fb[7], xa_1); SB();
    mm(ac[3][0][1], fb[1], wr[2][3]); SB();
    mm(ac[3][1][1], fb[2], wr[2][3]); SB();
    lds_rd128<54272>(fb[8], xa_1); SB();
    mm(ac[3][1][0], fb[2], wr[2][2]); SB();
    mmw<4>(ac[0][0][0], fb[3], wr[3][0]); SB();
    lds_rd128<55296>(fb[0], xa_1); SB();
    mm(ac[0][0][1], fb[3], wr[3][1]); SB();
    mm(ac[0][1][1], fb[4], wr[3][1]); SB();
    lds_rd128<56320>(fb[1], xa_1); SB();
    mm(ac[0][1][0], fb[4], wr[3][0]); SB();
    mmw<4>(ac[1][0][0], fb[5], wr[3][0]); SB();
    lds_rd128<57344>(fb[2], xa_1); SB();
    mm(ac[1][0][1], fb[5], wr[3][1]); SB();
    lds_rd128<0>(wr[10][0], wa0_1); SB();
    mm(ac[1][1][1], fb[6], wr[3][1]); SB();
    lds_rd128<58368>(fb[3], xa_1); SB();
    lds_rd128<2048>(wr[10][1], wa0_1); SB();
    mm(ac[1][1][0], fb[6], wr[3][0]); SB();
    lds_rd128<0>(wr[10][2], wa1_1); SB();
    mmw<7>(ac[0][0][0], fb[7], wr[3][2]); SB();
    lds_rd128<59392>(fb[4], xa_1); SB();
    lds_rd128<2048>(wr[10][3], wa1_1); SB();
    mm(ac[0][0][1], fb[7], wr[3][3]); SB();
    mm(ac[0][1][1], fb[8], wr[3][3]); SB();
    lds_rd128<60416>(fb[5], xa_1); SB();
    mm(ac[0][1][0], fb[8], wr[3][2]); SB();
    mmw<8>(ac[1][0][0], fb[0], wr[3][2]); SB();
    lds_rd128<61440>(fb[6], xa_1); SB();
    mm(ac[1][0][1], fb[0], wr[3][3]); SB();
    mm(ac[1][1][1], fb[1], wr[3][3]); SB();
    lds_rd128<62464>(fb[7], xa_1); SB();
    lds_rd32(fl, lf_rd); SB();
    mm(ac[1][1][0], fb[1], wr[3][2]); SB();
    mmw<9>(ac[2][0][0], fb[2], wr[3][0]); SB();
    lds_rd128<63488>(fb[8], xa_1); SB();
    mm(ac[2][0][1], fb[2], wr[3][1]); SB();
    mmw<8>(ac[2][1][1], fb[3], wr[3][1]); SB();
    lds_rd128<64512>(fb[0], xa_1); SB();
    mm(ac[2][1][0], fb[3], wr[3][0]); SB();
    mmchkw<19, 2>(ac[3][0][0], fb[4], wr[3][0], fl, lf_rd, bud); SB();
    lds_rd128<32768>(fb[1], xa_0); SB();
    mm(ac[3][0][1], fb[4], wr[3][1]); SB();
    mm(ac[3][1][1], fb[5], wr[3][1]); SB();
    lds_rd128<33792>(fb[2], xa_0); SB();
    mm(ac[3][1][0], fb[5], wr[3][0]); SB();
    mm(ac[2][0][0], fb[6], wr[3][2]); SB();
    lds_rd128<34816>(fb[3], xa_0); SB();
    SIGQ(3); SB();
    mm(ac[2][0][1], fb[6], wr[3][3]); SB();
    mm(ac[2][1][1], fb[7], wr[3][3]); SB();
    lds_rd128<35840>(fb[4], xa_0); SB();
    SIGN(); SB();
    mm(ac[2][1][0], fb[7], wr[3][2]); SB();
    mm(ac[3][0][0], fb[8], wr[3][2]); SB();
    lds_rd128<36864>(fb[5], xa_0); SB();
    mm(ac[3][0][1], fb[8], wr[3][3]); SB();
    mm(ac[3][1][1], fb[0], wr[3][3]); SB();
    lds_rd128<37888>(fb[6], xa_0); SB();
    mm(ac[3][1][0], fb[0], wr[3][2]); SB();
    mmw<4>(ac[0][0][0], fb[1], wr[4][0]); SB();
    lds_rd128<38912>(fb[7], xa_0); SB();
    mm(ac[0][0][1], fb[1], wr[4][1]); SB();
    mm(ac[0][1][1], fb[2], wr[4][1]); SB();
    lds_rd128<39936>(fb[8], xa_0); SB();
    mm(ac[0][1][0], fb[2], wr[4][0]); SB();
    mmw<4>(ac[1][0][0], fb[3], wr[4][0]); SB();
    lds_rd128<40960>(fb[0], xa_0); SB();
    mm(ac[1][0][1], fb[3], wr[4][1]); SB();
    lds_rd128<16384>(wr[11][0], wa0_1); SB();
    mm(ac[1][1][1], fb[4], wr[4][1]); SB();
    lds_rd128<41984>(fb[1], xa_0); SB();
    lds_rd128<18432>(wr[11][1], wa0_1); SB();
    mm(ac[1][1][0], fb[4], wr[4][0]); SB();
    lds_rd128<16384>(wr[11][2], wa1_1); SB();
    mmw<7>(ac[0][0][0], fb[5], wr[4][2]); SB();
    lds_rd128<43008>(fb[2], xa_0); SB();
    lds_rd128<18432>(wr[11][3], wa1_1); SB();
    mm(ac[0][0][1], fb[5], wr[4][3]); SB();
    mm(ac[0][1][1], fb[6], wr[4][3]); SB();
    lds_rd128<44032>(fb[3], xa_0); SB();
    mm(ac[0][1][0], fb[6], wr[4][2]); SB();
    mmw<8>(ac[1][0][0], fb[7], wr[4][2]); SB();
    lds_rd128<45056>(fb[4], xa_0); SB();
    mm(ac[1][0][1], fb[7], wr[4][3]); SB();
    mm(ac[1][1][1], fb[8], wr[4][3]); SB();
    lds_rd128<46080>(fb[5], xa_0); SB();
    lds_rd32(fl, lf_rd); SB();
    mm(ac[1][1][0], fb[8], wr[4][2]); SB();
    mmw<9>(ac[2][0][0], fb[0], wr[4][0]); SB();
    lds_rd128<47104>(fb[6], xa_0); SB();
    mm(ac[2][0][1], fb[0], wr[4][1]); SB();
    mmw<8>(ac[2][1][1], fb[1], wr[4][1]); SB();
    lds_rd128<48128>(fb[7], xa_0); SB();
    mm(ac[2][1][0], fb[1], wr[4][0]); SB();
    mmchkw<20, 2>(ac[3][0][0], fb[2], wr[4][0], fl, lf_rd, bud); SB();
    lds_rd128<0>(fb[8], xa_0); SB();
    mm(ac[3][0][1], fb[2], wr[4][1]); SB();
    mm(ac[3][1][1], fb[3], wr[4][1]); SB();
    lds_rd128<1024>(fb[0], xa_0); SB();
    mm(ac[3][1][0], fb[3], wr[4][0]); SB();
    mm(ac[2][0][0], fb[4], wr[4][2]); SB();
    lds_rd128<2048>(fb[1], xa_0); SB();
    SIGQ(3); SB();
    mm(ac[2][0][1], fb[4], wr[4][3]); SB();
    mm(ac[2][1][1], fb[5], wr[4][3]); SB();
    lds_rd128<3072>(fb[2], xa_0); SB();
    SIGN(); SB();
    mm(ac[2][1][0], fb[5], wr[4][2]); SB();
    mm(ac[3][0][0], fb[6], wr[4][2]); SB();
    lds_rd128<4096>(fb[3], xa_0); SB();
    mm(ac[3][0][1], fb[6], wr[4][3]); SB();
    mm(ac[3][1][1], fb[7], wr[4][3]); SB();
    lds_rd128<5120>(fb[4], xa_0); SB();
    mm(ac[3][1][0], fb[7], wr[4][2]); SB();
    mmw<4>(ac[0][0][0], fb[8], wr[5][0]); SB();
    lds_rd128<6144>(fb[5], xa_0); SB();
    mm(ac[0][0][1], fb[8], wr[5][1]); SB();
    mm(ac[0][1][1], fb[0], wr[5][1]); SB();
    lds_rd128<7168>(fb[6], xa_0); SB();
    mm(ac[0][1][0], fb[0], wr[5][0]); SB();
    mmw<4>(ac[1][0][0], fb[1], wr[5][0]); SB();
    lds_rd128<8192>(fb[7], xa_0); SB();
    mm(ac[1][0][1], fb[1], wr[5][1]); SB();
    mm(ac[1][1][1], fb[2], wr[5][1]); SB();
    lds_rd128<9216>(fb[8], xa_0); SB();
    mm(ac[1][1][0], fb[2], wr[5][0]); SB();
    mmw<4>(ac[0][0][0], fb[3], wr[5][2]); SB();
    lds_rd128<10240>(fb[0], xa_0); SB();
    mm(ac[0][0][1], fb[3], wr[5][3]); SB();
    mm(ac[0][1][1], fb[4], wr[5][3]); SB();
    lds_rd128<11264>(fb[1], xa_0); SB();
    mm(ac[0][1][0], fb[4], wr[5][2]); SB();
    mmw<4>(ac[1][0][0], fb[5], wr[5][2]); SB();
    lds_rd128<12288>(fb[2], xa_0); SB();
    mm(ac[1][0][1], fb[5], wr[5][3]); SB();
    mm(ac[1][1][1], fb[6], wr[5][3]); SB();
    lds_rd128<13312>(fb[3], xa_0); SB();
    lds_rd32(fl, lf_rd); SB();
    mm(ac[1][1][0], fb[6], wr[5][2]); SB();
    mmw<5>(ac[2][0][0], fb[7], wr[5][0]); SB();
    lds_rd128<14336>(fb[4], xa_0); SB();
    mm(ac[2][0][1], fb[7], wr[5][1]); SB();
    mm(ac[2][1][1], fb[8], wr[5][1]); SB();
    lds_rd128<15360>(fb[5], xa_0); SB();
    mm(ac[2][1][0], fb[8], wr[5][0]); SB();
    mmchkw<21, 2>(ac[3][0][0], fb[0], wr[5][0], fl, lf_rd, bud); SB();
    lds_rd128<0>(fb[6], xa_2); SB();
    mm(ac[3][0][1], fb[0], wr[5][1]); SB();
    mm(ac[3][1][1], fb[1], wr[5][1]); SB();
    lds_rd128<1024>(fb[7], xa_2); SB();
    mm(ac[3][1][0], fb[1], wr[5][0]); SB();
    mm(ac[2][0][0], fb[2], wr[5][2]); SB();
    lds_rd128<2048>(fb[8], xa_2); SB();
    SIGQ(3); SB();
    mm(ac[2][0][1], fb[2], wr[5][3]); SB();
    mm(ac[2][1][1], fb[3], wr[5][3]); SB();
    lds_rd128<3072>(fb[0], xa_2); SB();
    SIGN(); SB();
    mm(ac[2][1][0], fb[3], wr[5][2]); SB();
    mm(ac[3][0][0], fb[4], wr[5][2]); SB();
    lds_rd128<4096>(fb[1], xa_2); SB();
    mm(ac[3][0][1], fb[4], wr[5][3]); SB();
    mm(ac[3][1][1], fb[5], wr[5][3]); SB();
    lds_rd128<5120>(fb[2], xa_2); SB();
    mm(ac[3][1][0], fb[5], wr[5][2]); SB();
    mmw<4>(ac[0][0][0], fb[6], wr[6][0]); SB();
    lds_rd128<6144>(fb[3], xa_2); SB();
    mm(ac[0][0][1], fb[6], wr[6][1]); SB();
    mm(ac[0][1][1], fb[7], wr[6][1]); SB();
    lds_rd128<7168>(fb[4], xa_2); SB();
    mm(ac[0][1][0], fb[7], wr[6][0]); SB();
    mmw<4>(ac[1][0][0], fb[8], wr[6][0]); SB();
    lds_rd128<8192>(fb[5], xa_2); SB();
    mm(ac[1][0][1], fb[8], wr[6][1]); SB();
    mm(ac[1][1][1], fb[0], wr[6][1]); SB();
    lds_rd128<9216>(fb[6], xa_2); SB();
    mm(ac[1][1][0], fb[0], wr[6][0]); SB();
    mmw<4>(ac[0][0][0], fb[1], wr[6][2]); SB();
    lds_rd128<10240>(fb[7], xa_2); SB();
    mm(ac[0][0][1], fb[1], wr[6][3]); SB();
    mm(ac[0][1][1], fb[2], wr[6][3]); SB();
    lds_rd128<11264>(fb[8], xa_2); SB();
    mm(ac[0][1][0], fb[2], wr[6][2]); SB();
    mmw<4>(ac[1][0][0], fb[3], wr[6][2]); SB();
    lds_rd128<12288>(fb[0], xa_2); SB();
    mm(ac[1][0][1], fb[3], wr[6][3]); SB();
    mm(ac[1][1][1], fb[4], wr[6][3]); SB();
    lds_rd128<13312>(fb[1], xa_2); SB();
    lds_rd32(fl, lf_rd); SB();
    mm(ac[1][1][0], fb[4], wr[6][2]); SB();
    mmw<5>(ac[0][0][0], fb[5], wr[7][0]); SB();
    lds_rd128<14336>(fb[2], xa_2); SB();
    mm(ac[0][0][1], fb[5], wr[7][1]); SB();
    mm(ac[0][1][1], fb[6], wr[7][1]); SB();
    lds_rd128<15360>(fb[3], xa_2); SB();
    mm(ac[0][1][0], fb[6], wr[7][0]); SB();
    mmchkw<22, 2>(ac[1][0][0], fb[7], wr[7][0], fl, lf_rd, bud); SB();
    lds_rd128<49152>(fb[4], xa_0); SB();
    mm(ac[1][0][1], fb[7], wr[7][1]); SB();
    mm(ac[1][1][1], fb[8], wr[7][1]); SB();
    lds_rd128<50176>(fb[5], xa_0); SB();
    mm(ac[1][1][0], fb[8], wr[7][0]); SB();
    mm(ac[0][0][0], fb[0], wr[7][2]); SB();
    lds_rd128<51200>(fb[6], xa_0); SB();
    SIGQ(3); SB();
    mm(ac[0][0][1], fb[0], wr[7][3]); SB();
    mm(ac[0][1][1], fb[1], wr[7][3]); SB();
    lds_rd128<52224>(fb[7], xa_0); SB();
    SIGN(); SB();
    mm(ac[0][1][0], fb[1], wr[7][2]); SB();
    mm(ac[1][0][0], fb[2], wr[7][2]); SB();
    lds_rd128<53248>(fb[8], xa_0); SB();
    mm(ac[1][0][1], fb[2], wr[7][3]); SB();
    mm(ac[1][1][1], fb[3], wr[7][3]); SB();
    lds_rd128<54272>(fb[0], xa_0); SB();
    mm(ac[1][1][0], fb[3], wr[7][2]); SB();
    mmw<4>(ac[2][0][0], fb[4], wr[6][0]); SB();
    lds_rd128<55296>(fb[1], xa_0); SB();
    mm(ac[2][0][1], fb[4], wr[6][1]); SB();
    mm(ac[2][1][1], fb[5], wr[6][1]); SB();
    lds_rd128<56320>(fb[2], xa_0); SB();
    mm(ac[2][1][0], fb[5], wr[6][0]); SB();
    mmw<4>(ac[2][0][0], fb[6], wr[6][2]); SB();
    lds_rd128<57344>(fb[3], xa_0); SB();
    mm(ac[2][0][1], fb[6], wr[6][3]); SB();
    lds_rd128<32768>(wr[12][0], wa0_1); SB();
    mm(ac[2][1][1], fb[7], wr[6][3]); SB();
    lds_rd128<58368>(fb[4], xa_0); SB();
    lds_rd128<34816>(wr[12][1], wa0_1); SB();
    mm(ac[2][1][0], fb[7], wr[6][2]); SB();
    lds_rd128<32768>(wr[12][2], wa1_1); SB();
    mmw<7>(ac[2][0][0], fb[8], wr[7][0]); SB();
    lds_rd128<59392>(fb[5], xa_0); SB();
    lds_rd128<34816>(wr[12][3], wa1_1); SB();
    mm(ac[2][0][1], fb[8], wr[7][1]); SB();
    mm(ac[2][1][1], fb[0], wr[7][1]); SB();
    lds_rd128<60416>(fb[6], xa_0); SB();
    mm(ac[2][1][0], fb[0], wr[7][0]); SB();
    mmw<8>(ac[2][0][0], fb[1], wr[7][2]); SB();
    lds_rd128<61440>(fb[7], xa_0); SB();
    mm(ac[2][0][1], fb[1], wr[7][3]); SB();
    mm(ac[2][1][1], fb[2], wr[7][3]); SB();
    lds_rd128<62464>(fb[8], xa_0); SB();
    lds_rd32(fl, lf_rd); SB();
    mm(ac[2][1][0], fb[2], wr[7][2]); SB();
    mmw<9>(ac[0][0][0], fb[3], wr[8][0]); SB();
    lds_rd128<63488>(fb[0], xa_0); SB();
    mm(ac[0][0][1], fb[3], wr[8][1]); SB();
    mmw<8>(ac[0][1][1], fb[4], wr[8][1]); SB();
    lds_rd128<64512>(fb[1], xa_0); SB();
    mm(ac[0][1][0], fb[4], wr[8][0]); SB();
    mmchkw<23, 2>(ac[0][0][0], fb[5], wr[8][2], fl, lf_rd, bud); SB();
    lds_rd128<49152>(fb[2], xa_1); SB();
    mm(ac[0][0][1], fb[5], wr[8][3]); SB();
    lds_rd128<16384>(wr[13][0], wa0_0); SB();
    mm(ac[0][1][1], fb[6], wr[8][3]); SB();
    lds_rd128<50176>(fb[3], xa_1); SB();
    lds_rd128<18432>(wr[13][1], wa0_0); SB();
    mm(ac[0][1][0], fb[6], wr[8][2]); SB();
    lds_rd128<16384>(wr[13][2], wa1_0); SB();
    mm(ac[0][0][0], fb[7], wr[9][0]); SB();
    lds_rd128<51200>(fb[4], xa_1); SB();
    lds_rd128<18432>(wr[13][3], wa1_0); SB();
    mm(ac[0][0][1], fb[7], wr[9][1]); SB();
    SIGQ(7); SB();
    mm(ac[0][1][1], fb[8], wr[9][1]); SB();
    lds_rd128<52224>(fb[5], xa_1); SB();
    mm(ac[0][1][0], fb[8], wr[9][0]); SB();
    SIGN(); SB();
    mm(ac[0][0][0], fb[0], wr[9][2]); SB();
    lds_rd128<53248>(fb[6], xa_1); SB();
    mm(ac[0][0][1], fb[0], wr[9][3]); SB();
    mm(ac[0][1][1], fb[1], wr[9][3]); SB();
    lds_rd128<54272>(fb[7], xa_1); SB();
    mm(ac[0][1][0], fb[1], wr[9][2]); SB();
    mmw<8>(ac[0][0][0], fb[2], wr[10][0]); SB();
    lds_rd128<55296>(fb[8], xa_1); SB();
    mm(ac[0][0][1], fb[2], wr[10][1]); SB();
    mmw<7>(ac[0][1][1], fb[3], wr[10][1]); SB();
    lds_rd128<56320>(fb[0], xa_1); SB();
    mm(ac[0][1][0], fb[3], wr[10][0]); SB();
    mmw<5>(ac[0][0][0], fb[4], wr[10][2]); SB();
    lds_rd128<57344>(fb[1], xa_1); SB();
    mm(ac[0][0][1], fb[4], wr[10][3]); SB();
    mmw<4>(ac[0][1][1], fb[5], wr[10][3]); SB();
    lds_rd128<58368>(fb[2], xa_1); SB();
    mm(ac[0][1][0], fb[5], wr[10][2]); SB();
    mm(ac[0][0][0], fb[6], wr[11][0]); SB();
    lds_rd128<59392>(fb[3], xa_1); SB();
    mm(ac[0][0][1], fb[6], wr[11][1]); SB();
    mmw<4>(ac[0][1][1], fb[7], wr[11][1]); SB();
    lds_rd128<60416>(fb[4], xa_1); SB();
    mm(ac[0][1][0], fb[7], wr[11][0]); SB();
    mm(ac[0][0][0], fb[8], wr[11][2]); SB();
    lds_rd128<61440>(fb[5], xa_1); SB();
    mm(ac[0][0][1], fb[8], wr[11][3]); SB();
    mmw<4>(ac[0][1][1], fb[0], wr[11][3]); SB();
    lds_rd128<62464>(fb[6], xa_1); SB();
    mm(ac[0][1][0], fb[0], wr[11][2]); SB();
    lds_rd32(fl, lf_rd); SB();
    mm(ac[0][0][0], fb[1], wr[12][0]); SB();
    lds_rd128<63488>(fb[7], xa_1); SB();
    mm(ac[0][0][1], fb[1], wr[12][1]); SB();
    mmw<5>(ac[0][1][1], fb[2], wr[12][1]); SB();
    lds_rd128<64512>(fb[8], xa_1); SB();
    mm(ac[0][1][0], fb[2], wr[12][0]); SB();
    mm(ac[0][0][0], fb[3], wr[12][2]); SB();
    lds_rd128<0>(fb[0], wa0_1); SB();
    mmchkw<24, 3>(ac[0][0][1], fb[3], wr[12][3], fl, lf_rd, bud); SB();
    lds_rd128<16384>(fb[1], xa_1); SB();
    mm(ac[0][1][1], fb[4], wr[12][3]); SB();
    lds_rd128<2048>(fb[2], wa0_1); SB();
    mm(ac[0][1][0], fb[4], wr[12][2]); SB();
    lds_rd128<17408>(fb[3], xa_1); SB();
    mm(ac[0][0][0], fb[5], wr[13][0]); SB();
    lds_rd128<0>(fb[4], wa1_1); SB();
    mm(ac[0][0][1], fb[5], wr[13][1]); SB();
    lds_rd128<18432>(fb[5], xa_1); SB();
    mm(ac[0][1][1], fb[6], wr[13][1]); SB();
    SIGQ(6); SB();
    mm(ac[0][1][0], fb[6], wr[13][0]); SB();
    lds_rd128<2048>(fb[6], wa1_1); SB();
    mm(ac[0][0][0], fb[7], wr[13][2]); SB();
    SIGN(); SB();
    mm(ac[0][0][1], fb[7], wr[13][3]); SB();
    lds_rd128<19456>(fb[7], xa_1); SB();
    mm(ac[0][1][1], fb[8], wr[13][3]); SB();
    mm(ac[0][1][0], fb[8], wr[13][2]); SB();
    lds_rd128<32768>(fb[8], wa0_0); SB();
    mmw<6>(ac[0][0][0], fb[1], fb[0]); SB();
    mm(ac[0][0][1], fb[1], fb[2]); SB();
    lds_rd128<20480>(fb[1], xa_1); SB();
    mmw<5>(ac[0][1][1], fb[3], fb[2]); SB();
    lds_rd128<34816>(fb[2], wa0_0); SB();
    mm(ac[0][1][0], fb[3], fb[0]); SB();
    lds_rd128<21504>(fb[0], xa_1); SB();
    mmw<5>(ac[0][0][0], fb[5], fb[4]); SB();
    lds_rd128<32768>(fb[3], wa1_0); SB();
    mm(ac[0][0][1], fb[5], fb[6]); SB();
    lds_rd128<22528>(fb[5], xa_1); SB();
    mmw<6>(ac[0][1][1], fb[7], fb[6]); SB();
    lds_rd128<34816>(fb[6], wa1_0); SB();
    mm(ac[0][1][0], fb[7], fb[4]); SB();
    lds_rd128<23552>(fb[4], xa_1); SB();
    mmw<6>(ac[0][0][0], fb[1], fb[8]); SB();
    lds_rd128<24576>(fb[7], xa_1); SB();
    mmw<6>(ac[0][0][1], fb[1], fb[2]); SB();
    lds_rd128<25600>(fb[1], xa_1); SB();
    mmw<6>(ac[0][1][1], fb[0], fb[2]); SB();
    lds_rd128<26624>(fb[2], xa_1); SB();
    mm(ac[0][1][0], fb[0], fb[8]); SB();
    lds_rd128<27648>(fb[0], xa_1); SB();
    mmw<6>(ac[0][0][0], fb[5], fb[3]); SB();
    lds_rd128<28672>(fb[8], xa_1); SB();
    mmw<6>(ac[0][0][1], fb[5], fb[6]); SB();
    mmw<5>(ac[0][1][1], fb[4], fb[6]); SB();
    lds_rd128<29696>(fb[5], xa_1); SB();
    lds_rd32(fl, lf_rd); SB();
    mm(ac[0][1][0], fb[4], fb[3]); SB();
    mmw<6>(ac[1][0][0], fb[7], wr[8][0]); SB();
    lds_rd128<30720>(fb[6], xa_1); SB();
    mm(ac[1][0][1], fb[7], wr[8][1]); SB();
    mmw<6>(ac[1][1][1], fb[1], wr[8][1]); SB();
    lds_rd128<31744>(fb[3], xa_1); SB();
    mm(ac[1][1][0], fb[1], wr[8][0]); SB();
    EM0(0, 0); SB();
    mmchkw<25, 2>(ac[1][0][0], fb[2], wr[8][2], fl, lf_rd, bud); SB();
    lds_rd128<0>(fb[4], xa_0); SB();
    EM0(0, 1); SB();
    mm(ac[1][0][1], fb[2], wr[8][3]); SB();
    EM1(0, 0, 3, 4); SB();
    EM1(0, 1, 11, 12); SB();
    EM1(0, 0, 5, 6); SB();
    mm(ac[1][1][1], fb[0], wr[8][3]); SB();
    lds_rd128<1024>(fb[7], xa_0); SB();
    EM1(0, 1, 13, 14); SB();
    EM1(0, 0, 7, 15); SB();
    mm(ac[1][1][0], fb[0], wr[8][2]); SB();
    EK(0); SB();
    EM2(); SB();
    EK(1); SB();
    mm(ac[1][0][0], fb[8], wr[9][0]); SB();
    lds_rd128<2048>(fb[1], xa_0); SB();
    ES0(); SB();
    ES1(); SB();
    mm(ac[1][0][1], fb[8], wr[9][1]); SB();
    mm(ac[1][1][1], fb[5], wr[9][1]); SB();
    lds_rd128<3072>(fb[2], xa_0); SB();
    EX(0); SB();
    EH(0, 0); SB();
    mm(ac[1][1][0], fb[5], wr[9][0]); SB();
    EH(0, 1); SB();
    EH(0, 2); SB();
    mmw<4>(ac[1][0][0], fb[6], wr[9][2]); SB();
    lds_rd128<4096>(fb[0], xa_0); SB();
    EH(0, 3); SB();
    ET(0); SB();
    mm(ac[1][0][1], fb[6], wr[9][3]); SB();
    ET(1); SB();
    ET(2); SB();
    EU(3); SB();
    mm(ac[1][1][1], fb[3], wr[9][3]); SB();
    lds_rd128<5120>(fb[8], xa_0); SB();
    mm(ac[1][1][0], fb[3], wr[9][2]); SB();
    SEND(0, 0); SB();
    mmw<4>(ac[1][0][0], fb[4], wr[10][0]); SB();
    lds_rd128<6144>(fb[5], xa_0); SB();
    EH(0, 4); SB();
    EH(0, 5); SB();
    mm(ac[1][0][1], fb[4], wr[10][1]); SB();
    EH(0, 6); SB();
    EH(0, 7); SB();
    ET(4); SB();
    mm(ac[1][1][1], fb[7], wr[10][1]); SB();
    lds_rd128<7168>(fb[6], xa_0); SB();
    ET(5); SB();
    ET(6); SB();
    mm(ac[1][1][0], fb[7], wr[10][0]); SB();
    EU(7); SB();
    mmw<4>(ac[1][0][0], fb[1], wr[10][2]); SB();
    lds_rd128<8192>(fb[3], xa_0); SB();
    SEND(0, 1); SB();
    mm(ac[1][0][1], fb[1], wr[10][3]); SB();
    mm(ac[1][1][1], fb[2], wr[10][3]); SB();
    lds_rd128<9216>(fb[4], xa_0); SB();
    mm(ac[1][1][0], fb[2], wr[10][2]); SB();
    mmw<4>(ac[1][0][0], fb[0], wr[11][0]); SB();
    lds_rd128<10240>(fb[7], xa_0); SB();
    mm(ac[1][0][1], fb[0], wr[11][1]); SB();
    mm(ac[1][1][1], fb[8], wr[11][1]); SB();
    lds_rd128<11264>(fb[1], xa_0); SB();
    mm(ac[1][1][0], fb[8], wr[11][0]); SB();
    mmw<4>(ac[1][0][0], fb[5], wr[11][2]); SB();
    lds_rd128<12288>(fb[2], xa_0); SB();
    mm(ac[1][0][1], fb[5], wr[11][3]); SB();
    mm(ac[1][1][1], fb[6], wr[11][3]); SB();
    lds_rd128<13312>(fb[0], xa_0); SB();
    mm(ac[1][1][0], fb[6], wr[11][2]); SB();
    lds_rd32(fl, lf_rd); SB();
    mmw<5>(ac[1][0][0], fb[3], wr[12][0]); SB();
    lds_rd128<14336>(fb[8], xa_0); SB();
    mm(ac[1][0][1], fb[3], wr[12][1]); SB();
    mm(ac[1][1][1], fb[4], wr[12][1]); SB();
    lds_rd128<15360>(fb[5], xa_0); SB();
    mm(ac[1][1][0], fb[4], wr[12][0]); SB();
    mmw<5>(ac[1][0][0], fb[7], wr[12][2]); SB();
    lds_rd128<0>(fb[6], wa0_1); SB();
    mmchkw<26, 3>(ac[1][0][1], fb[7], wr[12][3], fl, lf_rd, bud); SB();
    lds_rd128<0>(fb[3], xa_2); SB();
    mm(ac[1][1][1], fb[1], wr[12][3]); SB();
    lds_rd128<2048>(fb[4], wa0_1); SB();
    mm(ac[1][1][0], fb[1], wr[12][2]); SB();
    lds_rd128<1024>(fb[7], xa_2); SB();
    mm(ac[1][0][0], fb[2], wr[13][0]); SB();
    lds_rd128<0>(fb[1], wa1_1); SB();
    mm(ac[1][0][1], fb[2], wr[13][1]); SB();
    lds_rd128<2048>(fb[2], xa_2); SB();
    mm(ac[1][1][1], fb[0], wr[13][1]); SB();
    mm(ac[1][1][0], fb[0], wr[13][0]); SB();
    lds_rd128<2048>(fb[0], wa1_1); SB();
    mmw<7>(ac[1][0][0], fb[8], wr[13][2]); SB();
    mm(ac[1][0][1], fb[8], wr[13][3]); SB();
    lds_rd128<3072>(fb[8], xa_2); SB();
    mm(ac[1][1][1], fb[5], wr[13][3]); SB();
    mm(ac[1][1][0], fb[5], wr[13][2]); SB();
    lds_rd128<32768>(fb[5], wa0_0); SB();
    mmw<6>(ac[1][0][0], fb[3], fb[6]); SB();
    mm(ac[1][0][1], fb[3], fb[4]); SB();
    lds_rd128<4096>(fb[3], xa_2); SB();
    mmw<5>(ac[1][1][1], fb[7], fb[4]); SB();
    lds_rd128<34816>(fb[4], wa0_0); SB();
    mm(ac[1][1][0], fb[7], fb[6]); SB();
    lds_rd128<5120>(fb[6], xa_2); SB();
    mmw<5>(ac[1][0][0], fb[2], fb[1]); SB();
    lds_rd128<32768>(fb[7], wa1_0); SB();
    mm(ac[1][0][1], fb[2], fb[0]); SB();
    lds_rd128<6144>(fb[2], xa_2); SB();
    mmw<6>(ac[1][1][1], fb[8], fb[0]); SB();
    lds_rd128<34816>(fb[0], wa1_0); SB();
    mm(ac[1][1][0], fb[8], fb[1]); SB();
    lds_rd128<7168>(fb[1], xa_2); SB();
    mmw<6>(ac[1][0][0], fb[3], fb[5]); SB();
    lds_rd128<8192>(fb[8], xa_2); SB();
    mmw<6>(ac[1][0][1], fb[3], fb[4]); SB();
    lds_rd128<9216>(fb[3], xa_2); SB();
    mmw<6>(ac[1][1][1], fb[6], fb[4]); SB();
    lds_rd128<10240>(fb[4], xa_2); SB();
    mm(ac[1][1][0], fb[6], fb[5]); SB();
    lds_rd128<11264>(fb[5], xa_2); SB();
    mmw<6>(ac[1][0][0], fb[2], fb[7]); SB();
    lds_rd128<12288>(fb[6], xa_2); SB();
    mmw<6>(ac[1][0][1], fb[2], fb[0]); SB();
    mmw<5>(ac[1][1][1], fb[1], fb[0]); SB();
    lds_rd128<13312>(fb[2], xa_2); SB();
    lds_rd32(fl, lf_rd); SB();
    mm(ac[1][1][0], fb[1], fb[7]); SB();
    mmw<6>(ac[2][0][0], fb[8], wr[8][0]); SB();
    lds_rd128<14336>(fb[0], xa_2); SB();
    mm(ac[2][0][1], fb[8], wr[8][1]); SB();
    mmw<6>(ac[2][1][1], fb[3], wr[8][1]); SB();
    lds_rd128<15360>(fb[1], xa_2); SB();
    mm(ac[2][1][0], fb[3], wr[8][0]); SB();
    EM0(1, 0); SB();
    mmchkw<27, 2>(ac[2][0][0], fb[4], wr[8][2], fl, lf_rd, bud); SB();
    lds_rd128<49152>(fb[7], xa_0); SB();
    EM0(1, 1); SB();
    mm(ac[2][0][1], fb[4], wr[8][3]); SB();
    EM1(1, 0, 3, 4); SB();
    EM1(1, 1, 11, 12); SB();
    EM1(1, 0, 5, 6); SB();
    mm(ac[2][1][1], fb[5], wr[8][3]); SB();
    lds_rd128<50176>(fb[8], xa_0); SB();
    EM1(1, 1, 13, 14); SB();
    EM1(1, 0, 7, 15); SB();
    mm(ac[2][1][0], fb[5], wr[8][2]); SB();
    EK(0); SB();
    EM2(); SB();
    EK(1); SB();
    mm(ac[2][0][0], fb[6], wr[9][0]); SB();
    lds_rd128<51200>(fb[3], xa_0); SB();
    ES0(); SB();
    ES1(); SB();
    mm(ac[2][0][1], fb[6], wr[9][1]); SB();
    mm(ac[2][1][1], fb[2], wr[9][1]); SB();
    lds_rd128<52224>(fb[4], xa_0); SB();
    EX(1); SB();
    EH(1, 0); SB();
    mm(ac[2][1][0], fb[2], wr[9][0]); SB();
    EH(1, 1); SB();
    EH(1, 2); SB();
    mmw<4>(ac[2][0][0], fb[0], wr[9][2]); SB();
    lds_rd128<53248>(fb[5], xa_0); SB();
    EH(1, 3); SB();
    ET(0); SB();
    mm(ac[2][0][1], fb[0], wr[9][3]); SB();
    ET(1); SB();
    ET(2); SB();
    EU(3); SB();
    mm(ac[2][1][1], fb[1], wr[9][3]); SB();
    lds_rd128<54272>(fb[6], xa_0); SB();
    mm(ac[2][1][0], fb[1], wr[9][2]); SB();
    SEND(1, 0); SB();
    mmw<4>(ac[2][0][0], fb[7], wr[10][0]); SB();
    lds_rd128<55296>(fb[2], xa_0); SB();
    EH(1, 4); SB();
    EH(1, 5); SB();
    mm(ac[2][0][1], fb[7], wr[10][1]); SB();
    EH(1, 6); SB();
    EH(1, 7); SB();
    ET(4); SB();
    mm(ac[2][1][1], fb[8], wr[10][1]); SB();
    lds_rd128<56320>(fb[0], xa_0); SB();
    ET(5); SB();
    ET(6); SB();
    mm(ac[2][1][0], fb[8], wr[10][0]); SB();
    EU(7); SB();
    mmw<4>(ac[2][0][0], fb[3], wr[10][2]); SB();
    lds_rd128<57344>(fb[1], xa_0); SB();
    SEND(1, 1); SB();
    mm(ac[2][0][1], fb[3], wr[10][3]); SB();
    mm(ac[2][1][1], fb[4], wr[10][3]); SB();
    lds_rd128<58368>(fb[7], xa_0); SB();
    mm(ac[2][1][0], fb[4], wr[10][2]); SB();
    mmw<4>(ac[2][0][0], fb[5], wr[11][0]); SB();
    lds_rd128<59392>(fb[8], xa_0); SB();
    mm(ac[2][0][1], fb[5], wr[11][1]); SB();
    mm(ac[2][1][1], fb[6], wr[11][1]); SB();
    lds_rd128<60416>(fb[3], xa_0); SB();
    mm(ac[2][1][0], fb[6], wr[11][0]); SB();
    mmw<4>(ac[2][0][0], fb[2], wr[11][2]); SB();
    lds_rd128<61440>(fb[4], xa_0); SB();
    mm(ac[2][0][1], fb[2], wr[11][3]); SB();
    mm(ac[2][1][1], fb[0], wr[11][3]); SB();
    lds_rd128<62464>(fb[5], xa_0); SB();
    mm(ac[2][1][0], fb[0], wr[11][2]); SB();
    lds_rd32(fl, lf_rd); SB();
    mmw<5>(ac[2][0][0], fb[1], wr[12][0]); SB();
    lds_rd128<63488>(fb[6], xa_0); SB();
    mm(ac[2][0][1], fb[1], wr[12][1]); SB();
    mm(ac[2][1][1], fb[7], wr[12][1]); SB();
    lds_rd128<64512>(fb[2], xa_0); SB();
    mm(ac[2][1][0], fb[7], wr[12][0]); SB();
    mmw<5>(ac[2][0][0], fb[8], wr[12][2]); SB();
    lds_rd128<0>(fb[0], wa0_1); SB();
    mmchkw<28, 3>(ac[2][0][1], fb[8], wr[12][3], fl, lf_rd, bud); SB();
    lds_rd128<32768>(fb[1], xa_1); SB();
    mm(ac[2][1][1], fb[3], wr[12][3]); SB();
    lds_rd128<2048>(fb[7], wa0_1); SB();
    mm(ac[2][1][0], fb[3], wr[12][2]); SB();
    lds_rd128<33792>(fb[8], xa_1); SB();
    mm(ac[2][0][0], fb[4], wr[13][0]); SB();
    lds_rd128<0>(fb[3], wa1_1); SB();
    mm(ac[2][0][1], fb[4], wr[13][1]); SB();
    lds_rd128<34816>(fb[4], xa_1); SB();
    mm(ac[2][1][1], fb[5], wr[13][1]); SB();
    mm(ac[2][1][0], fb[5], wr[13][0]); SB();
    lds_rd128<2048>(fb[5], wa1_1); SB();
    mmw<7>(ac[2][0][0], fb[6], wr[13][2]); SB();
    mm(ac[2][0][1], fb[6], wr[13][3]); SB();
    lds_rd128<35840>(fb[6], xa_1); SB();
    mm(ac[2][1][1], fb[2], wr[13][3]); SB();
    mm(ac[2][1][0], fb[2], wr[13][2]); SB();
    lds_rd128<32768>(fb[2], wa0_0); SB();
    mmw<6>(ac[2][0][0], fb[1], fb[0]); SB();
    mm(ac[2][0][1], fb[1], fb[7]); SB();
    lds_rd128<36864>(fb[1], xa_1); SB();
    mmw<5>(ac[2][1][1], fb[8], fb[7]); SB();
    lds_rd128<34816>(fb[7], wa0_0); SB();
    mm(ac[2][1][0], fb[8], fb[0]); SB();
    lds_rd128<37888>(fb[0], xa_1); SB();
    mmw<5>(ac[2][0][0], fb[4], fb[3]); SB();
    lds_rd128<32768>(fb[8], wa1_0); SB();
    mm(ac[2][0][1], fb[4], fb[5]); SB();
    lds_rd128<38912>(fb[4], xa_1); SB();
    mmw<6>(ac[2][1][1], fb[6], fb[5]); SB();
    lds_rd128<34816>(fb[5], wa1_0); SB();
    mm(ac[2][1][0], fb[6], fb[3]); SB();
    lds_rd128<39936>(fb[3], xa_1); SB();
    mmw<6>(ac[2][0][0], fb[1], fb[2]); SB();
    lds_rd128<40960>(fb[6], xa_1); SB();
    mmw<6>(ac[2][0][1], fb[1], fb[7]); SB();
    lds_rd128<41984>(fb[1], xa_1); SB();
    mmw<6>(ac[2][1][1], fb[0], fb[7]); SB();
    lds_rd128<43008>(fb[7], xa_1); SB();
    mm(ac[2][1][0], fb[0], fb[2]); SB();
    lds_rd128<44032>(fb[0], xa_1); SB();
    mmw<6>(ac[2][0][0], fb[4], fb[8]); SB();
    lds_rd128<45056>(fb[2], xa_1); SB();
    mmw<6>(ac[2][0][1], fb[4], fb[5]); SB();
    mmw<5>(ac[2][1][1], fb[3], fb[5]); SB();
    lds_rd128<46080>(fb[4], xa_1); SB();
    lds_rd32(fl, lf_rd); SB();
    mm(ac[2][1][0], fb[3], fb[8]); SB();
    mmw<6>(ac[3][0][0], fb[6], wr[6][0]); SB();
    lds_rd128<47104>(fb[5], xa_1); SB();
    mm(ac[3][0][1], fb[6], wr[6][1]); SB();
    mmw<6>(ac[3][1][1], fb[1], wr[6][1]); SB();
    lds_rd128<48128>(fb[3], xa_1); SB();
    mm(ac[3][1][0], fb[1], wr[6][0]); SB();
    EM0(2, 0); SB();
    EM0(2, 1); SB();
    mmchkw<29, 2>(ac[3][0][0], fb[7], wr[6][2], fl, lf_rd, bud); SB();
    lds_rd128<16384>(fb[8], xa_0); SB();
    EM1(2, 0, 3, 4); SB();
    EM1(2, 1, 11, 12); SB();
    mm(ac[3][0][1], fb[7], wr[6][3]); SB();
    EM1(2, 0, 5, 6); SB();
    EM1(2, 1, 13, 14); SB();
    EM1(2, 0, 7, 15); SB();
    EK(0); SB();
    mm(ac[3][1][1], fb[0], wr[6][3]); SB();
    lds_rd128<17408>(fb[6], xa_0); SB();
    EM2(); SB();
    EK(1); SB();
    ES0(); SB();
    mm(ac[3][1][0], fb[0], wr[6][2]); SB();
    ES1(); SB();
    mm(ac[3][0][0], fb[2], wr[7][0]); SB();
    lds_rd128<18432>(fb[1], xa_0); SB();
    EX(2); SB();
    EH(2, 0); SB();
    EH(2, 1); SB();
    mm(ac[3][0][1], fb[2], wr[7][1]); SB();
    EH(2, 2); SB();
    EH(2, 3); SB();
    ET(0); SB();
    ET(1); SB();
    mm(ac[3][1][1], fb[4], wr[7][1]); SB();
    lds_rd128<19456>(fb[7], xa_0); SB();
    ET(2); SB();
    EU(3); SB();
    mm(ac[3][1][0], fb[4], wr[7][0]); SB();
    SEND(2, 0); SB();
    mmw<4>(ac[3][0][0], fb[5], wr[7][2]); SB();
    lds_rd128<20480>(fb[0], xa_0); SB();
    EH(2, 4); SB();
    EH(2, 5); SB();
    EH(2, 6); SB();
    mm(ac[3][0][1], fb[5], wr[7][3]); SB();
    EH(2, 7); SB();
    ET(4); SB();
    ET(5); SB();
    ET(6); SB();
    mm(ac[3][1][1], fb[3], wr[7][3]); SB();
    lds_rd128<21504>(fb[2], xa_0); SB();
    EU(7); SB();
    mm(ac[3][1][0], fb[3], wr[7][2]); SB();
    SEND(2, 1); SB();
    mmw<4>(ac[3][0][0], fb[8], wr[8][0]); SB();
    lds_rd128<22528>(fb[4], xa_0); SB();
    mm(ac[3][0][1], fb[8], wr[8][1]); SB();
    mm(ac[3][1][1], fb[6], wr[8][1]); SB();
    lds_rd128<23552>(fb[5], xa_0); SB();
    mm(ac[3][1][0], fb[6], wr[8][0]); SB();
    mmw<4>(ac[3][0][0], fb[1], wr[8][2]); SB();
    lds_rd128<24576>(fb[3], xa_0); SB();
    mm(ac[3][0][1], fb[1], wr[8][3]); SB();
    mm(ac[3][1][1], fb[7], wr[8][3]); SB();
    lds_rd128<25600>(fb[8], xa_0); SB();
    mm(ac[3][1][0], fb[7], wr[8][2]); SB();
    mmw<4>(ac[3][0][0], fb[0], wr[9][0]); SB();
    lds_rd128<26624>(fb[6], xa_0); SB();
    mm(ac[3][0][1], fb[0], wr[9][1]); SB();
    mm(ac[3][1][1], fb[2], wr[9][1]); SB();
    lds_rd128<27648>(fb[1], xa_0); SB();
    mm(ac[3][1][0], fb[2], wr[9][0]); SB();
    mmw<4>(ac[3][0][0], fb[4], wr[9][2]); SB();
    lds_rd128<28672>(fb[7], xa_0); SB();
    mm(ac[3][0][1], fb[4], wr[9][3]); SB();
    mm(ac[3][1][1], fb[5], wr[9][3]); SB();
    lds_rd128<29696>(fb[0], xa_0); SB();
    lds_rd32(fl, lf_rd); SB();
    mm(ac[3][1][0], fb[5], wr[9][2]); SB();
    mmw<5>(ac[3][0][0], fb[3], wr[10][0]); SB();
    lds_rd128<30720>(fb[2], xa_0); SB();
    mm(ac[3][0][1], fb[3], wr[10][1]); SB();
    mm(ac[3][1][1], fb[8], wr[10][1]); SB();
    lds_rd128<31744>(fb[4], xa_0); SB();
    mm(ac[3][1][0], fb[8], wr[10][0]); SB();
    mmchkw<30, 2>(ac[3][0][0], fb[6], wr[10][2], fl, lf_rd, bud); SB();
    lds_rd128<49152>(fb[5], xa_1); SB();
    mm(ac[3][0][1], fb[6], wr[10][3]); SB();
    mm(ac[3][1][1], fb[1], wr[10][3]); SB();
    lds_rd128<50176>(fb[3], xa_1); SB();
    mm(ac[3][1][0], fb[1], wr[10][2]); SB();
    mm(ac[3][0][0], fb[7], wr[11][0]); SB();
    lds_rd128<51200>(fb[8], xa_1); SB();
    mm(ac[3][0][1], fb[7], wr[11][1]); SB();
    mm(ac[3][1][1], fb[0], wr[11][1]); SB();
    lds_rd128<52224>(fb[6], xa_1); SB();
    mm(ac[3][1][0], fb[0], wr[11][0]); SB();
    mmw<4>(ac[3][0][0], fb[2], wr[11][2]); SB();
    lds_rd128<53248>(fb[1], xa_1); SB();
    mm(ac[3][0][1], fb[2], wr[11][3]); SB();
    mm(ac[3][1][1], fb[4], wr[11][3]); SB();
    lds_rd128<54272>(fb[7], xa_1); SB();
    mm(ac[3][1][0], fb[4], wr[11][2]); SB();
    mmw<4>(ac[3][0][0], fb[5], wr[12][0]); SB();
    lds_rd128<55296>(fb[0], xa_1); SB();
    RV_LD(0, ""); SB();
    mm(ac[3][0][1], fb[5], wr[12][1]); SB();
    mm(ac[3][1][1], fb[3], wr[12][1]); SB();
    lds_rd128<56320>(fb[2], xa_1); SB();
    mm(ac[3][1][0], fb[3], wr[12][0]); SB();
    mmw<4>(ac[3][0][0], fb[8], wr[12][2]); SB();
    lds_rd128<0>(fb[4], wa0_1); SB();
    mm(ac[3][0][1], fb[8], wr[12][3]); SB();
    lds_rd128<57344>(fb[5], xa_1); SB();
    mm(ac[3][1][1], fb[6], wr[12][3]); SB();
    lds_rd128<2048>(fb[3], wa0_1); SB();
    mm(ac[3][1][0], fb[6], wr[12][2]); SB();
    lds_rd128<58368>(fb[8], xa_1); SB();
    mmw<6>(ac[3][0][0], fb[1], wr[13][0]); SB();
    lds_rd128<0>(fb[6], wa1_1); SB();
    mm(ac[3][0][1], fb[1], wr[13][1]); SB();
    lds_rd128<59392>(fb[1], xa_1); SB();
    mm(ac[3][1][1], fb[7], wr[13][1]); SB();
    mm(ac[3][1][0], fb[7], wr[13][0]); SB();
    lds_rd128<2048>(fb[7], wa1_1); SB();
    mmw<7>(ac[3][0][0], fb[0], wr[13][2]); SB();
    mm(ac[3][0][1], fb[0], wr[13][3]); SB();
    lds_rd128<60416>(fb[0], xa_1); SB();
    mm(ac[3][1][1], fb[2], wr[13][3]); SB();
    lds_rd32(f2, d2_rd); SB();
    mm(ac[3][1][0], fb[2], wr[13][2]); SB();
    lds_rd128<32768>(fb[2], wa0_0); SB();
    mmw<7>(ac[3][0][0], fb[5], fb[4]); SB();
    RV_LD(1, ""); SB();
    mm(ac[3][0][1], fb[5], fb[3]); SB();
    lds_rd128<61440>(fb[5], xa_1); SB();
    mmw<6>(ac[3][1][1], fb[8], fb[3]); SB();
    lds_rd128<34816>(fb[3], wa0_0); SB();
    mm(ac[3][1][0], fb[8], fb[4]); SB();
    lds_rd128<62464>(fb[4], xa_1); SB();
    mmw<6>(ac[3][0][0], fb[1], fb[6]); SB();
    lds_rd128<32768>(fb[8], wa1_0); SB();
    asm volatile("s_waitcnt lgkmcnt(5)" : "+v"(fb[0]), "+v"(f2)::"memory"); SB();
    chk<1>(f2, d2_rd, bud); SB();
    D_RD(0); SB();
    D_RD(1); SB();
    D_RD(2); SB();
    D_RD(3); SB();
    G_RD(0); SB();
    G_RD(1); SB();
    mm(ac[3][0][1], fb[1], fb[7]); SB();
    lds_rd128<63488>(fb[1], xa_1); SB();
    mm(ac[3][1][1], fb[0], fb[7]); SB();
    lds_rd128<34816>(fb[7], wa1_0); SB();
    mm(ac[3][1][0], fb[0], fb[6]); SB();
    lds_rd128<64512>(fb[0], xa_1); SB();
    RV_LD(2, ""); SB();
    mmw<12>(ac[3][0][0], fb[5], fb[2]); SB();
    mmw<11>(ac[3][0][1], fb[5], fb[3]); SB();
    mmw<10>(ac[3][1][1], fb[4], fb[3]); SB();
    mm(ac[3][1][0], fb[4], fb[2]); SB();
    mmw<2>(ac[3][0][0], fb[1], fb[8]); SB();
    mmw<1>(ac[3][0][1], fb[1], fb[7]); SB();
    mmw<0>(ac[3][1][1], fb[0], fb[7]); SB();
    mm(ac[3][1][0], fb[0], fb[8]); SB();
    asm volatile("s_nop 7" : "+v"(ac[3][0][0]), "+v"(ac[3][0][1]), "+v"(ac[3][1][0]), "+v"(ac[3][1][1])::"memory");
    const float om[2] = {rbf(1.0f - gs[0]), rbf(1.0f - gs[1])};
    gs[0] = h ? -gs[0] : gs[0];
    gs[1] = h ? -gs[1] : gs[1];
    float y[16];
#pragma unroll
    for (int p = 0; p < 8; ++p) {
      const unsigned o = dq[p >> 1][p & 1];
      const f32x2 v = f32x2{AV(3, 2 * p), AV(3, 2 * p + 1)} * gs[p >> 2] + f32x2{lo2f(o), hi2f(o)} * om[p >> 2];
      y[2 * p] = v[0];
      y[2 * p + 1] = v[1];
    }
    asm volatile("" : "+v"(oo), "+s"(out), "+v"(gs[0]), "+v"(gs[1]));
    auto strip = [&](u32x4 (&r)[2]) {
      const u32x4 t = {tb[0] ^ 0x40004u, tb[1] ^ 0x40004u, tb[2] ^ 0x40004u, tb[3] ^ 0x40004u};
      r[0] ^= t;
      r[1] ^= t;
      const unsigned o = ((r[0].x | r[0].y | r[0].z | r[1].x | r[1].y | r[1].z) & 0x30003u) | ((r[0].w | r[1].w) & 0x30000u);
      const unsigned a = r[0].x & r[0].y & r[0].z & r[0].w & r[1].x & r[1].y & r[1].z & r[1].w & 0x40004u;
      return (o | a) == 0x40004u;
    };
#define RV_FIN(S, Y)                                                                                                     \
  asm volatile("s_waitcnt vmcnt(%18)" : "+v"(rv[S][0]), "+v"(rv[S][1]), "+v"(y[0]), "+v"(y[1]), "+v"(y[2]), "+v"(y[3]), "+v"(y[4]), "+v"(y[5]), \
               "+v"(y[6]), "+v"(y[7]), "+v"(y[8]), "+v"(y[9]), "+v"(y[10]), "+v"(y[11]), "+v"(y[12]), "+v"(y[13]), "+v"(y[14]), "+v"(y[15])     \
               : "i"(Y) : "memory");                                                                                     \
  if (__builtin_expect(!__all(strip(rv[S])), 0)) {                                                                       \
    _Pragma("clang loop unroll(disable)") for (int it = 0; it < SPIN_MAX; ++it) {                                        \
      RV_LD(S, "\n\ts_waitcnt vmcnt(0)");                                                                                \
      if (__all(strip(rv[S]))) break;                                                                                    \
    }                                                                                                                    \
  }                                                                                                                      \
  {                                                                                                                      \
    const float sc = __uint_as_float((((rv[S][0].w & 3u) | (rv[S][1].w & 3u) << 2) << 27) + ((S) == 2 ? 0xff800000u : 0x7f800000u)); \
    rv[S][0].w &= ~3u;                                                                                                   \
    rv[S][1].w &= ~3u;                                                                                                   \
    _Pragma("unroll") for (int p = 0; p < 8; ++p) {                                                                      \
      const float f = sc * gs[p >> 2];                                                                                   \
      unsigned o = rv[S][p >> 2][p & 3];                                                                                 \
      asm("v_fma_mix_f32 %0, %2, %3, %0 op_sel_hi:[1,0,0]\n\tv_fma_mix_f32 %1, %2, %3, %1 op_sel:[1,0,0] op_sel_hi:[1,0,0]" \
          : "+v"(y[2 * p]), "+v"(y[2 * p + 1]) : "v"(o), "v"(f));                                                        \
    }                                                                                                                    \
  }
    RV_FIN(0, 4);
    RV_FIN(1, 2);
    RV_FIN(2, 0);
    const unsigned idx = (4095u - (__builtin_amdgcn_readfirstlane(f2) & 0xfffu)) & (S_ - 1);
    if (__builtin_expect(idx >> 5 == (unsigned)(ti * 4 + wid), 0)) {
      SPIN(d_rd, 1);
      u32x2 dn_[2];
      asm volatile("ds_read_b64 %0, %2\n\tds_read_b64 %1, %2 offset:32\n\ts_waitcnt lgkmcnt(0)" : "=&v"(dn_[0]), "=&v"(dn_[1]) : "v"(sb + RING + L_UPD + fq * 8) : "memory");
#pragma unroll
      for (int p = 0; p < 8; ++p)
        if ((idx & 31u) == (unsigned)((p >> 2) * 16 + fr)) {
          const unsigned o = dq[p >> 1][p & 1], n = dn_[p >> 1 & 1][p & 1];
          y[2 * p] += om[p >> 2] * (lo2f(n) - lo2f(o));
          y[2 * p + 1] += om[p >> 2] * (hi2f(n) - hi2f(o));
        }
    }
#pragma unroll
    for (int mi = 0; mi < 2; ++mi) {
      const float* z = y + mi * 8;
      const auto s0 = __builtin_amdgcn_permlane16_swap(pk2(z[0], z[1]), pk2(z[4], z[5]), false, false);
      const auto s1 = __builtin_amdgcn_permlane16_swap(pk2(z[2], z[3]), pk2(z[6], z[7]), false, false);
      asm volatile("global_store_dwordx4 %0, %1, %2\n\ts_nop 1" ::"v"(oo + mi * (16u * D_ * 2)), "v"(u32x4{s0[0], s1[0], s0[1], s1[1]}), "s"(out) : "memory");
    }
  }
  asm volatile(".subsection 1\n7777:\n\ts_setpc_b64 vcc\n\t.subsection 0");
}

extern "C" void kernel_launch(void* const* d_in, const int* in_sizes, int n_in, void* d_out, int out_size,
                              void* d_ws, size_t ws_size, hipStream_t stream) {
  const u16* x = (const u16*)d_in[0];
  const u16* wsre = (const u16*)d_in[1];
  const u16* wsim = (const u16*)d_in[2];
  const u16* wdre = (const u16*)d_in[3];
  const u16* wdim = (const u16*)d_in[4];
  const u16* w_r = (const u16*)d_in[6];
  const u16* w_gate = (const u16*)d_in[7];
  const u16* w_up = (const u16*)d_in[8];
  const u16* w_down = (const u16*)d_in[9];
  const u16* gate_w = (const u16*)d_in[10];
  u16* out = (u16*)d_out;
  fused_k<<<256, 512, 0, stream>>>(wsre, wsim, x, w_r, gate_w, wdre, wdim, w_gate, w_up, w_down, (char*)d_ws, out);
}
```
